```python
import torch

def _softmax(x, axis=-1):
    return torch.softmax(x, dim=axis)
def _gelu(x):
    return torch.nn.functional.gelu(x, approximate="tanh")
def _silu(x):
    return torch.nn.functional.silu(x)
def _sigmoid(x):
    return torch.sigmoid(x)
def _layernorm(x, axis=-1, eps=1e-5):
    m = x.mean(dim=axis, keepdim=True)
    v = x.var(dim=axis, keepdim=True, unbiased=False)
    return (x - m) / torch.sqrt(v + eps)
def _scatter_add(tgt, idx, src, axis=0):
    return tgt.clone().index_add_(axis, idx, src)
def _segment_sum(src, seg, n):
    out = torch.zeros((n,) + src.shape[1:], dtype=src.dtype)
    return out.index_add_(0, seg, src)
def _pad(x, pw):
    flat = tuple(v for pair in reversed(pw) for v in pair)
    return torch.nn.functional.pad(x, flat)

kernel_name = 'recurrent_scan__linear_kernel_hedgehog__dense_activation_mish__prompt'

def setup_inputs():

    _w = (lambda a: (lambda t: t.to(torch.bfloat16) if t.is_floating_point() else t)(torch.as_tensor(a)))
    def _A_setup():
        B, S, D, H = 1, 2048, 640, 32
        Dh = D // H
        return {
            "x": _w((lambda *s: torch.randn(*s, dtype=torch.bfloat16) * 0.05)(B, S, D)),
            "wqkv": _w((lambda *s: torch.randn(*s, dtype=torch.bfloat16) * 0.4)(D, 3 * D)),
            "w_phi": _w((lambda *s: torch.randn(*s, dtype=torch.bfloat16) * 0.4)(H, Dh, Dh) * (Dh ** -0.5)),
            "wo": _w((lambda *s: torch.randn(*s, dtype=torch.bfloat16) * 0.2)(D, D)),
        }
    _A_in = _A_setup()

    def _B_setup():
        B, S, D, F = 1, 2048, 640, 1728
        return {
            "x": _w((lambda *s: torch.randn(*s, dtype=torch.bfloat16) * 0.05)(B, S, D)),
            "w1": _w((lambda *s: torch.randn(*s, dtype=torch.bfloat16) * 0.4)(D, F) * (D ** -0.5)),
            "b1": _w((lambda *s: torch.randn(*s, dtype=torch.bfloat16) * 0.4)(F)),
            "w2": _w((lambda *s: torch.randn(*s, dtype=torch.bfloat16) * 0.1)(F, D) * (F ** -0.5)),
        }
    _B_in = _B_setup()

    return {
        "A_x": _A_in["x"],
        "A_wqkv": _A_in["wqkv"],
        "A_w_phi": _A_in["w_phi"],
        "A_wo": _A_in["wo"],
        "B_x": _B_in["x"],
        "B_w1": _B_in["w1"],
        "B_b1": _B_in["b1"],
        "B_w2": _B_in["w2"],
    }

def reference(A_x, A_wqkv, A_w_phi, A_wo, B_x, B_w1, B_b1, B_w2):
    def _A_ref(x, wqkv, w_phi, wo):
        B, S, D = x.shape
        H = 32; Dh = D // H
        qkv = (lambda x, *d: x.permute(*d))((x @ wqkv).reshape(B, S, 3, H, Dh), 2, 0, 3, 1, 4)
        q, k, v = qkv[0], qkv[1], qkv[2]
        pq = torch.einsum("bhsd,hdf->bhsf", q, w_phi)
        pk = torch.einsum("bhsd,hdf->bhsf", k, w_phi)
        phi_q = torch.concatenate([_softmax(pq, axis=-1), _softmax(-pq, axis=-1)], axis=-1)
        phi_k = torch.concatenate([_softmax(pk, axis=-1), _softmax(-pk, axis=-1)], axis=-1)
        kv = torch.einsum("bhsf,bhsd->bhfd", phi_k, v)
        z  = torch.einsum("bhsf->bhf", phi_k)
        num = torch.einsum("bhqf,bhfd->bhqd", phi_q, kv)
        den = torch.einsum("bhqf,bhf->bhq", phi_q, z)[..., None] + 1e-6
        o = num / den
        return (lambda x, *d: x.permute(*d))(o, 0, 2, 1, 3).reshape(B, S, D) @ wo

    def _B_ref(x, w1, b1, w2):
        h = torch.einsum("bsd,df->bsf", x, w1) + b1
        sp = torch.log1p(torch.exp(-torch.abs(h))) + torch.where(h > 0, h, torch.zeros_like(h))
        act = h * torch.tanh(sp)
        return torch.einsum("bsf,fd->bsd", act, w2)

    x = A_x
    xn = _layernorm(x, axis=-1)
    s = _A_ref(xn, A_wqkv, A_w_phi, A_wo)
    g = _silu(_B_ref(xn, B_w1, B_b1, B_w2))
    return x + s * g
```

```cpp
#include <hip/hip_runtime.h>
#include <hip/hip_bf16.h>
typedef unsigned short u16;
__device__ __forceinline__ float bf2f(u16 v){ return __uint_as_float(((unsigned)v)<<16); }
__device__ __forceinline__ u16 f2bf(float f){ __hip_bfloat16 b = __float2bfloat16(f); return *reinterpret_cast<u16*>(&b); }
__device__ __forceinline__ float rbf(float f){ return bf2f(f2bf(f)); }
constexpr int S=2048, D=640, H=32, DH=20, F=1728;

__global__ void k_ln(const u16* x, u16* xn){
  int r = blockIdx.x; int t = threadIdx.x;
  float s=0; float v[10];
  for(int i=0;i<10;i++){ v[i]=bf2f(x[r*D+t+64*i]); s+=v[i]; }
  for(int o=32;o>0;o>>=1) s+=__shfl_xor(s,o);
  float m=s/D; float q=0;
  for(int i=0;i<10;i++){ float d=v[i]-m; q+=d*d; }
  for(int o=32;o>0;o>>=1) q+=__shfl_xor(q,o);
  float rs = 1.0f/sqrtf(q/D+1e-5f);
  for(int i=0;i<10;i++) xn[r*D+t+64*i]=f2bf((v[i]-m)*rs);
}
__global__ void k_gemm(const u16* A, const u16* B, float* C, int M, int N, int K){
  __shared__ float sa[16][17], sb[16][17];
  int tx=threadIdx.x, ty=threadIdx.y;
  int row=blockIdx.y*16+ty, col=blockIdx.x*16+tx;
  float acc=0;
  for(int k0=0;k0<K;k0+=16){
    sa[ty][tx]=bf2f(A[row*K+k0+tx]);
    sb[ty][tx]=bf2f(B[(k0+ty)*N+col]);
    __syncthreads();
    for(int k=0;k<16;k++) acc+=sa[ty][k]*sb[k][tx];
    __syncthreads();
  }
  C[row*N+col]=acc;
}
__global__ void k_phi(const float* qkv, const u16* wphi, float* phi  ){
  int idx = blockIdx.x*blockDim.x+threadIdx.x;
  int s = idx % S; int h = (idx/S)%H; int w = idx/(S*H);
  if(w>=2) return;
  float q[DH]; for(int d=0;d<DH;d++) q[d]=rbf(qkv[s*3*D + w*D + h*DH + d]);
  float p[DH]; float mx=-1e30f, mn=1e30f;
  for(int f=0;f<DH;f++){ float a=0; for(int d=0;d<DH;d++) a+=q[d]*bf2f(wphi[(h*DH+d)*DH+f]); p[f]=a; mx=fmaxf(mx,a); mn=fminf(mn,a);}
  float s1=0,s2=0; float e1[DH],e2[DH];
  for(int f=0;f<DH;f++){ e1[f]=__expf(p[f]-mx); e2[f]=__expf(mn-p[f]); s1+=e1[f]; s2+=e2[f]; }
  float* o = phi + (((long)w*H+h)*S+s)*40;
  for(int f=0;f<DH;f++){ o[f]=e1[f]/s1; o[20+f]=e2[f]/s2; }
}
__global__ void k_kv(const float* qkv, const float* phi, float* kv){
  int h=blockIdx.x; int t=threadIdx.x; if(t>=840) return;
  int f=t/21, d=t%21; float a=0;
  const float* pk = phi + ((long)H+h)*S*40;
  for(int s=0;s<S;s++){ float v = d<20 ? rbf(qkv[s*3*D+2*D+h*DH+d]) : 1.0f; a+=pk[s*40+f]*v; }
  kv[h*840+t]=a;
}
__global__ void k_o(const float* phi, const float* kv, u16* o){
  int idx = blockIdx.x*blockDim.x+threadIdx.x;
  int s = idx % S; int h = idx/S;
  const float* pq = phi + ((long)h*S+s)*40;
  float den=1e-6f; for(int f=0;f<40;f++) den+=pq[f]*kv[h*840+f*21+20];
  for(int d=0;d<DH;d++){ float n=0; for(int f=0;f<40;f++) n+=pq[f]*kv[h*840+f*21+d]; o[s*D+h*DH+d]=f2bf(n/den);}
}
__global__ void k_mish(const float* hh, const u16* b1, u16* act){
  int idx = blockIdx.x*blockDim.x+threadIdx.x;
  float h = hh[idx]+bf2f(b1[idx%F]);
  float sp = log1pf(expf(-fabsf(h)))+fmaxf(h,0.f);
  act[idx]=f2bf(h*tanhf(sp));
}
__global__ void k_fin(const u16* x, const float* s, const float* b, u16* out){
  int idx = blockIdx.x*blockDim.x+threadIdx.x;
  float bb=b[idx]; float g = bb/(1.f+expf(-bb));
  out[idx]=f2bf(bf2f(x[idx])+s[idx]*g);
}
extern "C" void kernel_launch(void* const* d_in, const int* in_sizes, int n_in,
                              void* d_out, int out_size, void* d_ws, size_t ws_size,
                              hipStream_t stream) {
  const u16* x=(const u16*)d_in[0]; const u16* wqkv=(const u16*)d_in[1]; const u16* wphi=(const u16*)d_in[2];
  const u16* wo=(const u16*)d_in[3]; const u16* w1=(const u16*)d_in[5]; const u16* b1=(const u16*)d_in[6]; const u16* w2=(const u16*)d_in[7];
  char* ws=(char*)d_ws; size_t off=0;
  auto alloc=[&](size_t n){ void* p=ws+off; off+=(n+255)/256*256; return p; };
  u16* xn=(u16*)alloc(S*D*2); float* qkv=(float*)alloc((size_t)S*3*D*4); float* phi=(float*)alloc((size_t)2*H*S*40*4);
  float* kv=(float*)alloc(H*840*4); u16* o=(u16*)alloc(S*D*2); float* sb=(float*)alloc(S*D*4); float* hh=(float*)alloc((size_t)S*F*4);
  u16* act=(u16*)alloc((size_t)S*F*2); float* bb=(float*)alloc(S*D*4);
  k_ln<<<S,64,0,stream>>>(x,xn);
  k_gemm<<<dim3(3*D/16,S/16),dim3(16,16),0,stream>>>(xn,wqkv,qkv,S,3*D,D);
  k_phi<<<2*H*S/256,256,0,stream>>>(qkv,wphi,phi);
  k_kv<<<H,896,0,stream>>>(qkv,phi,kv);
  k_o<<<H*S/256,256,0,stream>>>(phi,kv,o);
  k_gemm<<<dim3(D/16,S/16),dim3(16,16),0,stream>>>(o,wo,sb,S,D,D);
  k_gemm<<<dim3(F/16,S/16),dim3(16,16),0,stream>>>(xn,w1,hh,S,F,D);
  k_mish<<<S*F/256,256,0,stream>>>(hh,b1,act);
  k_gemm<<<dim3(D/16,S/16),dim3(16,16),0,stream>>>(act,w2,bb,S,D,F);
  k_fin<<<S*D/256,256,0,stream>>>(x,sb,bb,(u16*)d_out);
}
```

```cpp
#include <hip/hip_runtime.h>
#include <hip/hip_bf16.h>

typedef unsigned short u16;
typedef __bf16 bf16x2 __attribute__((ext_vector_type(2)));
typedef __bf16 bf16x4 __attribute__((ext_vector_type(4)));
typedef __bf16 bf16x8 __attribute__((ext_vector_type(8)));
typedef float f32x2 __attribute__((ext_vector_type(2)));
typedef float f32x4 __attribute__((ext_vector_type(4)));
typedef unsigned u32x2 __attribute__((ext_vector_type(2)));
typedef unsigned u32x4 __attribute__((ext_vector_type(4)));
#define LDS3 __attribute__((address_space(3)))
#define DI __device__ __forceinline__
#define MFMA16(a, b, c) __builtin_amdgcn_mfma_f32_16x16x32_bf16((a), (b), (c), 0, 0, 0)

namespace hk {
constexpr int S = 2048, D = 640, F = 1728;
constexpr float LOG2E = 1.4426950408889634f;
constexpr size_t OFF_ACT = 0;
constexpr size_t OFF_O = OFF_ACT + (size_t)128 * 54 * 1024;
constexpr size_t OFF_PHI = OFF_O + (size_t)128 * 20 * 1024;
constexpr size_t OFF_KVP = OFF_PHI + (size_t)128 * 8 * 5 * 1024;
constexpr size_t OFF_KVH = OFF_KVP + (size_t)8 * 16 * 36 * 1024;
constexpr size_t OFF_W2F = OFF_KVH + (size_t)8 * 46 * 512;
constexpr int STAGE = 49152, XB = 16384, WROW = 480;
constexpr int L_RAW = 3 * STAGE;
constexpr int L_BIAS = L_RAW + 4096;
constexpr int L_APHI = L_RAW + 5120;
constexpr int L_END = L_APHI + 9216;
constexpr int E_STATP = 0;
constexpr int E_CSP = 2048;
constexpr int E_CS = 2048 + 3840;
constexpr int E_STAT = E_CS + 960;
constexpr int E_PHI = 8192;
constexpr int E_V = E_PHI + 40960;

DI unsigned pack2(float a, float b) {
  f32x2 v = {a, b};
  return __builtin_bit_cast(unsigned, __builtin_convertvector(v, bf16x2));
}
DI bf16x2 as_bf2(unsigned u) { return __builtin_bit_cast(bf16x2, u); }
DI float bf_lo(unsigned u) { return __uint_as_float(u << 16); }
DI float bf_hi(unsigned u) { return __uint_as_float(u & 0xffff0000u); }
DI float allsum4(float x) {
  unsigned u = __float_as_uint(x);
  auto r = __builtin_amdgcn_permlane16_swap(u, u, false, false);
  float y = __uint_as_float(r[0]) + __uint_as_float(r[1]);
  unsigned v = __float_as_uint(y);
  auto q = __builtin_amdgcn_permlane32_swap(v, v, false, false);
  return __uint_as_float(q[0]) + __uint_as_float(q[1]);
}
template <int OP>
DI float op2(float a, float b) { return OP == 0 ? fmaxf(a, b) : (OP == 1 ? fminf(a, b) : a + b); }
template <int OP>
DI float rscatter(float m0, float m1, float m2, float m3) {
  auto a = __builtin_amdgcn_permlane16_swap(__float_as_uint(m0), __float_as_uint(m1), false, false);
  auto b = __builtin_amdgcn_permlane16_swap(__float_as_uint(m2), __float_as_uint(m3), false, false);
  float A = op2<OP>(__uint_as_float(a[0]), __uint_as_float(a[1]));
  float B = op2<OP>(__uint_as_float(b[0]), __uint_as_float(b[1]));
  auto c = __builtin_amdgcn_permlane32_swap(__float_as_uint(A), __float_as_uint(B), false, false);
  return op2<OP>(__uint_as_float(c[0]), __uint_as_float(c[1]));
}
DI void agather5(float R, int g, float (&o)[5]) {
  unsigned u = __float_as_uint(R);
  auto c = __builtin_amdgcn_permlane32_swap(u, u, false, false);
  auto a = __builtin_amdgcn_permlane16_swap(c[0], c[0], false, false);
  auto b = __builtin_amdgcn_permlane16_swap(c[1], c[1], false, false);
  float h0 = __uint_as_float(a[0]), h1 = __uint_as_float(a[1]), h2 = __uint_as_float(b[0]), h3 = __uint_as_float(b[1]);
  o[0] = h0;
  o[1] = g == 0 ? h0 : h1;
  o[2] = g <= 1 ? h1 : h2;
  o[3] = g <= 2 ? h2 : h3;
  o[4] = h3;
}
template <int OP>
DI float head_reduce(const float (&q)[5], int g, float ident) {
  float m0 = op2<OP>(q[0], g == 0 ? q[1] : ident);
  float m1 = op2<OP>(g >= 1 ? q[1] : ident, g <= 1 ? q[2] : ident);
  float m2 = op2<OP>(g >= 2 ? q[2] : ident, g <= 2 ? q[3] : ident);
  float m3 = op2<OP>(g == 3 ? q[3] : ident, q[4]);
  return rscatter<OP>(m0, m1, m2, m3);
}
DI void dual_softmax(const f32x4 (&p)[5], int g, u32x2 (&pos)[5], u32x2 (&neg)[5]) {
  float qx[5], qn[5];
#pragma unroll
  for (int t = 0; t < 5; ++t) {
    qx[t] = fmaxf(fmaxf(p[t][0], p[t][1]), fmaxf(p[t][2], p[t][3]));
    qn[t] = fminf(fminf(p[t][0], p[t][1]), fminf(p[t][2], p[t][3]));
  }
  float mx[5], mn[5];
  agather5(head_reduce<0>(qx, g, -3.0e38f) * LOG2E, g, mx);
  agather5(head_reduce<1>(qn, g, 3.0e38f) * LOG2E, g, mn);
  f32x4 ep[5], en[5];
  float sp[5], sn[5];
#pragma unroll
  for (int t = 0; t < 5; ++t) {
#pragma unroll
    for (int r = 0; r < 4; ++r) {
      ep[t][r] = __builtin_amdgcn_exp2f(fmaf(p[t][r], LOG2E, -mx[t]));
      en[t][r] = __builtin_amdgcn_exp2f(fmaf(p[t][r], -LOG2E, mn[t]));
    }
    sp[t] = (ep[t][0] + ep[t][1]) + (ep[t][2] + ep[t][3]);
    sn[t] = (en[t][0] + en[t][1]) + (en[t][2] + en[t][3]);
  }
  float ip[5], in_[5];
  agather5(__builtin_amdgcn_rcpf(head_reduce<2>(sp, g, 0.f)), g, ip);
  agather5(__builtin_amdgcn_rcpf(head_reduce<2>(sn, g, 0.f)), g, in_);
#pragma unroll
  for (int t = 0; t < 5; ++t) {
    pos[t][0] = pack2(ep[t][0] * ip[t], ep[t][1] * ip[t]);
    pos[t][1] = pack2(ep[t][2] * ip[t], ep[t][3] * ip[t]);
    neg[t][0] = pack2(en[t][0] * in_[t], en[t][1] * in_[t]);
    neg[t][1] = pack2(en[t][2] * in_[t], en[t][3] * in_[t]);
  }
}
DI float mish1(float h) {
  float e = __builtin_amdgcn_exp2f(h * LOG2E);
  float u = e + 1.0f;
  float d = fmaf(u, u, 1.0f);
  float r = __builtin_amdgcn_rcpf(d);
  float n = e * (e + 2.0f);
  float t = (e > 1.0e18f) ? 1.0f : n * r;
  return h * t;
}
DI bf16x8 mk8(u32x2 a, u32x2 b) {
  u32x4 v = {a[0], a[1], b[0], b[1]};
  return __builtin_bit_cast(bf16x8, v);
}

struct P1 {
  const u16* x; const u16* wqkv; const u16* wphi; const u16* wo; const u16* w1; const u16* b1; const u16* w2;
  char* ws;
};

DI int kv_t(int lo) { return (int)((0x248db692449200ull >> (3 * lo)) & 7); }
DI int kv_n(int lo) { return (int)((0x2c76355a351148ull >> (3 * lo)) & 7); }
DI unsigned lds_addr(const void* p) { return (unsigned)(size_t)(LDS3 const char*)p; }
DI void glds1(const void* g, unsigned lds) {
  unsigned keep;
  asm volatile("s_mov_b32 %0, m0\n\ts_mov_b32 m0, %2\n\ts_nop 0\n\tglobal_load_lds_dwordx4 %1, off\n\ts_mov_b32 m0, %0"
               : "=&s"(keep) : "v"(g), "s"(lds) : "memory");
}
DI void glds6(const void* g0, const void* g1, const void* g2, const void* g3, const void* g4, const void* g5, unsigned lds) {
  unsigned keep;
  asm volatile("s_mov_b32 %0, m0\n\t"
               "s_mov_b32 m0, %7\n\ts_nop 0\n\tglobal_load_lds_dwordx4 %1, off\n\t"
               "s_add_u32 m0, %7, 0x2000\n\ts_nop 0\n\tglobal_load_lds_dwordx4 %2, off\n\t"
               "s_add_u32 m0, %7, 0x4000\n\ts_nop 0\n\tglobal_load_lds_dwordx4 %3, off\n\t"
               "s_add_u32 m0, %7, 0x6000\n\ts_nop 0\n\tglobal_load_lds_dwordx4 %4, off\n\t"
               "s_add_u32 m0, %7, 0x8000\n\ts_nop 0\n\tglobal_load_lds_dwordx4 %5, off\n\t"
               "s_add_u32 m0, %7, 0xa000\n\ts_nop 0\n\tglobal_load_lds_dwordx4 %6, off\n\t"
               "s_mov_b32 m0, %0"
               : "=&s"(keep) : "v"(g0), "v"(g1), "v"(g2), "v"(g3), "v"(g4), "v"(g5), "s"(lds) : "memory", "scc");
}

template <int TYPE, int C>
DI void phase1_body(const P1& p, char* smem, int rb, int g8, int w, int lane) {
  constexpr int NT = (TYPE == 1 && C == 1) ? 8 : 7;
  const int r = w & 3;
  const int lg = lane >> 4, l15 = lane & 15;
  const char* xsrc[2];
  const char* wsrc[4];
  unsigned winc[4];
#pragma unroll
  for (int i = 0; i < 2; ++i) {
    int ci = (i * 8 + w) * 64 + lane;
    int row = ci >> 3, pos = ci & 7;
    int ch = pos ^ ((row >> 1) & 7);
    xsrc[i] = (const char*)p.x + (size_t)(rb * 128 + row) * 1280 + ch * 16;
  }
  const int t0w = TYPE == 0 ? 4 * g8 : min(32 + 10 * g8, 98);
#pragma unroll
  for (int i = 0; i < 4; ++i) {
    int ci = (i * 8 + w) * 64 + lane;
    if (ci >= 1920) ci -= 128;
    int pos = ci / 30, c16 = ci - pos * 30;
    int unit = c16 >> 1, half = c16 & 1;
    int kk = pos & 15;
    int k = (pos & ~15) | ((kk >= 4 && kk < 12) ? (kk ^ 12) : kk);
    const char* base; unsigned stride; int col;
    if (TYPE == 0) {
      if (unit < 5) { base = (const char*)p.wqkv; stride = 3840; col = 640 + 80 * g8 + 16 * unit; }
      else if (unit < 10) { base = (const char*)p.wqkv; stride = 3840; col = 1280 + 80 * g8 + 16 * (unit - 5); }
      else { base = (const char*)p.w1; stride = 3456; col = 16 * (t0w + (unit < 14 ? unit - 10 : 3)); }
    } else {
      if (unit < 5) { base = (const char*)p.wqkv; stride = 3840; col = 80 * g8 + 16 * unit; }
      else { base = (const char*)p.w1; stride = 3456; col = 16 * (t0w + unit - 5); }
    }
    wsrc[i] = base + (size_t)k * stride + col * 2 + half * 16;
    winc[i] = stride * 64;
  }
  const unsigned lds0 = lds_addr(smem);
  auto issue = [&](int stage) {
    glds6(xsrc[0], xsrc[1], wsrc[0], wsrc[1], wsrc[2], wsrc[3], lds0 + stage * STAGE + w * 1024);
#pragma unroll
    for (int i = 0; i < 2; ++i) xsrc[i] += 128;
#pragma unroll
    for (int i = 0; i < 4; ++i) wsrc[i] += winc[i];
  };
  if (w < 4) {
    int ci = w * 64 + lane; ci = ci < 200 ? ci : 199;
    glds1((const char*)p.wphi + g8 * 3200 + ci * 16, lds0 + L_RAW + w * 1024);
  } else if (w == 4) {
    int nb = TYPE == 0 ? 8 : 20;
    int ci = lane < nb ? lane : nb - 1;
    glds1((const char*)p.b1 + t0w * 32 + ci * 16, lds0 + L_BIAS);
  }
  issue(0);
  issue(1);

  const int xrow = 32 * r + l15;
  const int xsw = (l15 >> 1) & 7;
  const unsigned xa0 = xrow * 128 + ((lg ^ xsw) * 16);
  const unsigned xa1 = xa0 ^ 64;
  const unsigned wa = XB + (16 * (lg >> 1) + 4 * (lg & 1) + (l15 >> 2)) * WROW + (l15 & 3) * 8;

  f32x4 acc[NT][2];
#pragma unroll
  for (int t = 0; t < NT; ++t) { acc[t][0] = f32x4{0, 0, 0, 0}; acc[t][1] = f32x4{0, 0, 0, 0}; }
  float cs[NT];
#pragma unroll
  for (int t = 0; t < NT; ++t) cs[t] = 0.f;
  float s1[2] = {0.f, 0.f}, s2[2] = {0.f, 0.f};
  const bf16x2 ones2 = {(__bf16)1.0f, (__bf16)1.0f};

  auto unit_of = [](int t) constexpr {
    return TYPE == 0 ? (C == 0 ? (t < 5 ? t : t + 5) : (t < 5 ? t + 5 : t + 7)) : (C == 0 ? t : t + 7);
  };

  int stage = 0;
  for (int i = 0; i < 10; ++i) {
    if (i < 9) asm volatile("s_waitcnt vmcnt(6)" ::: "memory");
    else asm volatile("s_waitcnt vmcnt(0)" ::: "memory");
    asm volatile("s_waitcnt lgkmcnt(0)" ::: "memory");
    __builtin_amdgcn_s_barrier();
    __builtin_amdgcn_sched_barrier(0);
    if (i < 8) { int st2 = stage + 2; st2 = st2 >= 3 ? st2 - 3 : st2; issue(st2); }
    if (i == 0) {
      for (int e = w * 64 + lane; e < 576; e += 512) {
        int fr = e >> 6, ln = e & 63;
        int to = (fr + 1) >> 1;
        int ks = (fr == 0) ? 0 : (to <= 2 ? ((fr + 1) & 1) : 1 + ((fr + 1) & 1));
        int f = 16 * to + (ln & 15), gg = ln >> 4;
        int hf = f / 20, fi = f - hf * 20;
        unsigned v[4];
#pragma unroll
        for (int jj = 0; jj < 4; ++jj) {
          unsigned two = 0;
#pragma unroll
          for (int h2 = 0; h2 < 2; ++h2) {
            int j = jj * 2 + h2;
            int d = 32 * ks + (j < 4 ? 4 * gg + j : 16 + 4 * gg + j - 4);
            int di = d - hf * 20;
            bool ok = di >= 0 && di < 20;
            int idx = ok ? (hf * 400 + di * 20 + fi) : 0;
            unsigned val = *(const u16*)(smem + L_RAW + idx * 2);
            val = ok ? val : 0u;
            two |= val << (16 * h2);
          }
          v[jj] = two;
        }
        *(u32x4*)(smem + L_APHI + e * 16) = u32x4{v[0], v[1], v[2], v[3]};
      }
    }
    const char* sb = smem + stage * STAGE;
#pragma unroll
    for (int ks = 0; ks < 2; ++ks) {
      bf16x8 xf[2];
#pragma unroll
      for (int tt = 0; tt < 2; ++tt) xf[tt] = *(const bf16x8*)(sb + (ks ? xa1 : xa0) + tt * 2048);
      bf16x8 wf[NT];
#pragma unroll
      for (int t = 0; t < NT; ++t) {
        const int u = unit_of(t);
        bf16x4 lo = __builtin_amdgcn_ds_read_tr16_b64_v4bf16((LDS3 bf16x4*)(sb + wa + (32 * ks) * WROW + u * 32));
        bf16x4 hi = __builtin_amdgcn_ds_read_tr16_b64_v4bf16((LDS3 bf16x4*)(sb + wa + (32 * ks + 8) * WROW + u * 32));
        wf[t] = __builtin_shufflevector(lo, hi, 0, 1, 2, 3, 4, 5, 6, 7);
      }
#pragma unroll
      for (int t = 0; t < NT; ++t) {
        constexpr bool isV = false;
        const bool v = (TYPE == 0 && C == 1 && t < 5);
#pragma unroll
        for (int tt = 0; tt < 2; ++tt) acc[t][tt] = v ? MFMA16(xf[tt], wf[t], acc[t][tt]) : MFMA16(wf[t], xf[tt], acc[t][tt]);
        (void)isV;
      }
      if (ks == C) {
#pragma unroll
        for (int tt = 0; tt < 2; ++tt) {
          u32x4 xv = __builtin_bit_cast(u32x4, xf[tt]);
#pragma unroll
          for (int j = 0; j < 4; ++j) {
            bf16x2 e = as_bf2(xv[j]);
            s1[tt] = __builtin_amdgcn_fdot2_f32_bf16(e, ones2, s1[tt], false);
            s2[tt] = __builtin_amdgcn_fdot2_f32_bf16(e, e, s2[tt], false);
          }
        }
      }
      if ((2 * (i & 1) + ks) == r) {
#pragma unroll
        for (int t = 0; t < NT; ++t) {
          u32x4 wv = __builtin_bit_cast(u32x4, wf[t]);
#pragma unroll
          for (int j = 0; j < 4; ++j) cs[t] = __builtin_amdgcn_fdot2_f32_bf16(as_bf2(wv[j]), ones2, cs[t], false);
        }
      }
    }
    stage = stage == 2 ? 0 : stage + 1;
  }
#pragma unroll
  for (int tt = 0; tt < 2; ++tt) { s1[tt] = allsum4(s1[tt]); s2[tt] = allsum4(s2[tt]); }
#pragma unroll
  for (int t = 0; t < NT; ++t) cs[t] = allsum4(cs[t]);
  __syncthreads();
  if (lg == 0) {
#pragma unroll
    for (int tt = 0; tt < 2; ++tt) *(f32x2*)(smem + E_STATP + (C * 128 + 32 * r + 16 * tt + l15) * 8) = f32x2{s1[tt], s2[tt]};
#pragma unroll
    for (int t = 0; t < NT; ++t) *(float*)(smem + E_CSP + (r * 240 + unit_of(t) * 16 + l15) * 4) = cs[t];
  }
  __syncthreads();
  {
    int tid = w * 64 + lane;
    if (tid < 240) {
      const float* q = (const float*)(smem + E_CSP) + tid;
      if (TYPE == 1 || tid < 224) *(float*)(smem + E_CS + tid * 4) = (q[0] + q[240]) + (q[480] + q[720]);
    } else if (tid >= 256 && tid < 384) {
      int tk = tid - 256;
      f32x2 a = *(const f32x2*)(smem + E_STATP + tk * 8), b = *(const f32x2*)(smem + E_STATP + (128 + tk) * 8);
      float mean = (a[0] + b[0]) * (1.0f / 640.0f);
      float var = fmaxf((a[1] + b[1]) * (1.0f / 640.0f) - mean * mean, 0.f);
      float rstd = 1.0f / sqrtf(var + 1e-5f);
      *(f32x2*)(smem + E_STAT + tk * 8) = f32x2{rstd, -rstd * mean};
    }
  }
  __syncthreads();
  f32x2 st[2];
#pragma unroll
  for (int tt = 0; tt < 2; ++tt) st[tt] = *(const f32x2*)(smem + E_STAT + (32 * r + 16 * tt + l15) * 8);

  if (C == 0) {
#pragma unroll
    for (int tt = 0; tt < 2; ++tt) {
      u32x2 kb[6];
#pragma unroll
      for (int t = 0; t < 5; ++t) {
        f32x4 c4 = *(const f32x4*)(smem + E_CS + (t * 16 + 4 * lg) * 4);
        float y0 = fmaf(acc[t][tt][0], st[tt][0], st[tt][1] * c4[0]);
        float y1 = fmaf(acc[t][tt][1], st[tt][0], st[tt][1] * c4[1]);
        float y2 = fmaf(acc[t][tt][2], st[tt][0], st[tt][1] * c4[2]);
        float y3 = fmaf(acc[t][tt][3], st[tt][0], st[tt][1] * c4[3]);
        kb[t] = u32x2{pack2(y0, y1), pack2(y2, y3)};
      }
      kb[5] = u32x2{0u, 0u};
      bf16x8 b0 = mk8(kb[0], kb[1]), b1 = mk8(kb[2], kb[3]), b2 = mk8(kb[4], kb[5]);
      auto ap = [&](int i) { return *(const bf16x8*)(smem + L_APHI + i * 1024 + lane * 16); };
      f32x4 z4 = {0, 0, 0, 0};
      f32x4 pk[5];
      pk[0] = MFMA16(ap(0), b0, z4);
      pk[1] = MFMA16(ap(2), b1, MFMA16(ap(1), b0, z4));
      pk[2] = MFMA16(ap(4), b1, MFMA16(ap(3), b0, z4));
      pk[3] = MFMA16(ap(6), b2, MFMA16(ap(5), b1, z4));
      pk[4] = MFMA16(ap(8), b2, MFMA16(ap(7), b1, z4));
      u32x2 pos[5], neg[5];
      dual_softmax(pk, lg, pos, neg);
      if (TYPE == 0) {
        int tok = 32 * r + 16 * tt + l15;
#pragma unroll
        for (int t = 0; t < 5; ++t) {
          *(u32x2*)(smem + E_PHI + (t * 128 + tok) * 32 + lg * 8) = pos[t];
          *(u32x2*)(smem + E_PHI + ((5 + t) * 128 + tok) * 32 + lg * 8) = neg[t];
        }
      } else {
        char* dst = p.ws + OFF_PHI + ((size_t)((rb * 8 + 2 * r + tt) * 8 + g8) * 5) * 1024 + lane * 16;
        *(u32x4*)(dst) = u32x4{pos[0][0], pos[0][1], pos[1][0], pos[1][1]};
        *(u32x4*)(dst + 1024) = u32x4{pos[2][0], pos[2][1], pos[3][0], pos[3][1]};
        *(u32x4*)(dst + 2048) = u32x4{neg[0][0], neg[0][1], neg[1][0], neg[1][1]};
        *(u32x4*)(dst + 3072) = u32x4{neg[2][0], neg[2][1], neg[3][0], neg[3][1]};
        *(u32x4*)(dst + 4096) = u32x4{pos[4][0], pos[4][1], neg[4][0], neg[4][1]};
      }
    }
  }
  if (TYPE == 0 && C == 1) {
    f32x4 sa[2], sb2[2];
#pragma unroll
    for (int tt = 0; tt < 2; ++tt) {
      sa[tt] = *(const f32x4*)(smem + E_STAT + (32 * r + 16 * tt + 4 * lg) * 8);
      sb2[tt] = *(const f32x4*)(smem + E_STAT + (32 * r + 16 * tt + 4 * lg + 2) * 8);
    }
#pragma unroll
    for (int t = 0; t < 5; ++t) {
      float c1 = *(const float*)(smem + E_CS + ((5 + t) * 16 + l15) * 4);
      unsigned o[4];
#pragma unroll
      for (int tt = 0; tt < 2; ++tt) {
        float y0 = fmaf(acc[t][tt][0], sa[tt][0], sa[tt][1] * c1);
        float y1 = fmaf(acc[t][tt][1], sa[tt][2], sa[tt][3] * c1);
        float y2 = fmaf(acc[t][tt][2], sb2[tt][0], sb2[tt][1] * c1);
        float y3 = fmaf(acc[t][tt][3], sb2[tt][2], sb2[tt][3] * c1);
        o[2 * tt] = pack2(y0, y1); o[2 * tt + 1] = pack2(y2, y3);
      }
      *(u32x4*)(smem + E_V + (r * 5 + t) * 1024 + lane * 16) = u32x4{o[0], o[1], o[2], o[3]};
    }
  }
  {
    constexpr int TW0 = (TYPE == 1 && C == 1) ? 0 : 5;
    constexpr int NP = (NT - TW0) / 2;
#pragma unroll
    for (int pp = 0; pp < NP; ++pp) {
      const int ta = TW0 + 2 * pp;
      const int ua = unit_of(ta);
      const int wl = TYPE == 0 ? ua - 10 : ua - 5;
      const int wt = t0w + wl;
      bool skip = (TYPE == 1) && (g8 == 7) && (wl < 4);
      u32x2 bb[2]; f32x4 c4[2];
#pragma unroll
      for (int h = 0; h < 2; ++h) {
        bb[h] = *(const u32x2*)(smem + L_BIAS + (wl + h) * 32 + lg * 8);
        c4[h] = *(const f32x4*)(smem + E_CS + ((ua + h) * 16 + 4 * lg) * 4);
      }
#pragma unroll
      for (int tt = 0; tt < 2; ++tt) {
        unsigned o[4];
#pragma unroll
        for (int h = 0; h < 2; ++h) {
          float h0 = fmaf(acc[ta + h][tt][0], st[tt][0], fmaf(st[tt][1], c4[h][0], bf_lo(bb[h][0])));
          float h1 = fmaf(acc[ta + h][tt][1], st[tt][0], fmaf(st[tt][1], c4[h][1], bf_hi(bb[h][0])));
          float h2 = fmaf(acc[ta + h][tt][2], st[tt][0], fmaf(st[tt][1], c4[h][2], bf_lo(bb[h][1])));
          float h3 = fmaf(acc[ta + h][tt][3], st[tt][0], fmaf(st[tt][1], c4[h][3], bf_hi(bb[h][1])));
          o[2 * h] = pack2(mish1(h0), mish1(h1));
          o[2 * h + 1] = pack2(mish1(h2), mish1(h3));
        }
        if (!skip)
          *(u32x4*)(p.ws + OFF_ACT + ((size_t)(rb * 8 + 2 * r + tt) * 54 + (wt >> 1)) * 1024 + lane * 16) = u32x4{o[0], o[1], o[2], o[3]};
      }
    }
  }
  if (TYPE == 0) {
    __syncthreads();
    const unsigned pa = E_PHI + (4 * lg + (l15 >> 2)) * 32 + (l15 & 3) * 8;
    const u32x4 one4 = {0x3f803f80u, 0x3f803f80u, 0x3f803f80u, 0x3f803f80u};
    for (int tau = w; tau < 36; tau += 8) {
      int sg = tau >= 18 ? 1 : 0, lo = tau - 18 * sg;
      int t = kv_t(lo), tn = kv_n(lo);
      f32x4 a = {0, 0, 0, 0};
#pragma unroll
      for (int ks = 0; ks < 4; ++ks) {
        const char* q = smem + pa + ((sg * 5 + t) * 128 + ks * 32) * 32;
        bf16x4 a0 = __builtin_amdgcn_ds_read_tr16_b64_v4bf16((LDS3 bf16x4*)(q));
        bf16x4 a1 = __builtin_amdgcn_ds_read_tr16_b64_v4bf16((LDS3 bf16x4*)(q + 512));
        u32x4 bv = *(const u32x4*)(smem + E_V + (ks * 5 + (tn < 5 ? tn : 0)) * 1024 + lane * 16);
        bv = tn < 5 ? bv : one4;
        a = MFMA16(__builtin_shufflevector(a0, a1, 0, 1, 2, 3, 4, 5, 6, 7), __builtin_bit_cast(bf16x8, bv), a);
      }
      *(f32x4*)(p.ws + OFF_KVP + ((size_t)(g8 * 16 + rb) * 36 + tau) * 1024 + lane * 16) = a;
    }
  }
}

__global__ __launch_bounds__(512, 2) void k_phase1(P1 p) {
  extern __shared__ __attribute__((aligned(16))) char smem[];
  const int b = blockIdx.x, xcd = b & 7, idx = b >> 3;
  const int w = __builtin_amdgcn_readfirstlane(threadIdx.x >> 6), lane = threadIdx.x & 63;
  const int rb = (xcd >> 2) * 8 + (idx >> 2);
  const int jl = idx & 3, xc = xcd & 3;
  const int type = jl >> 1, g8 = 2 * xc + (jl & 1);
  const int c = w >> 2;
  if (type == 0) {
    if (c == 0) phase1_body<0, 0>(p, smem, rb, g8, w, lane);
    else phase1_body<0, 1>(p, smem, rb, g8, w, lane);
  } else {
    if (c == 0) phase1_body<1, 0>(p, smem, rb, g8, w, lane);
    else phase1_body<1, 1>(p, smem, rb, g8, w, lane);
  }
  for (int fr = b * 8 + w; fr < 2960; fr += 2048) {
    int nt = fr % 5, q = fr / 5, u = q % 74, ct = q / 74;
    const u16* src = u < 54 ? p.w2 + (size_t)(32 * u) * 640 : p.wo + (size_t)(32 * (u - 54)) * 640;
    src += 80 * ct + 16 * nt + (lane & 15) + (size_t)(4 * (lane >> 4)) * 640;
    unsigned v[8];
#pragma unroll
    for (int j = 0; j < 8; ++j) v[j] = src[(size_t)(j < 4 ? j : 12 + j) * 640];
    *(u32x4*)(p.ws + OFF_W2F + (size_t)fr * 1024 + lane * 16) =
        u32x4{v[0] | (v[1] << 16), v[2] | (v[3] << 16), v[4] | (v[5] << 16), v[6] | (v[7] << 16)};
  }
}

__global__ __launch_bounds__(64) void k_kvred(char* ws) {
  const int g8 = blockIdx.x / 36, tau = blockIdx.x % 36, lane = threadIdx.x;
  const int lg = lane >> 4, l15 = lane & 15;
  f32x4 a = {0, 0, 0, 0};
#pragma unroll
  for (int rb = 0; rb < 16; ++rb) a += *(const f32x4*)(ws + OFF_KVP + ((size_t)(g8 * 16 + rb) * 36 + tau) * 1024 + lane * 16);
  int sg = tau >= 18 ? 1 : 0, lo = tau - 18 * sg;
  int t = kv_t(lo), tn = kv_n(lo);
  int hf = (16 * t + 4 * lg) / 20;
  u32x2 v = {pack2(a[0], a[1]), pack2(a[2], a[3])};
  u32x2 z = {0u, 0u};
  char* base = ws + OFF_KVH + (size_t)g8 * 46 * 512 + lane * 8;
  if (tn < 5) {
    int hd = (16 * tn + l15) / 20;
    int idx = sg * 13 + lo - t;
    *(u32x2*)(base + idx * 512) = (hd == hf) ? v : z;
  } else {
    int gp = l15 >> 2, rp = l15 & 3;
    int h0 = (16 * rp + 4 * gp) / 20;
    *(u32x2*)(base + (26 + (sg * 5 + t) * 2) * 512) = (h0 == hf) ? v : z;
    *(u32x2*)(base + (26 + (sg * 5 + t) * 2 + 1) * 512) = (rp == 0 && hf == 3) ? v : z;
  }
}

DI void compute_o(const bf16x8 (&ph)[5], const char* kvh  , u32x2 (&o)[5]) {
  auto H = [&](int i) { return *(const u32x2*)(kvh + i * 512); };
  const u32x2 z2 = {0u, 0u};
  f32x4 z4 = {0, 0, 0, 0};
  f32x4 n[5];
  n[0] = MFMA16(mk8(H(0), H(2)), ph[0], z4);           n[0] = MFMA16(mk8(H(13), H(15)), ph[2], n[0]);
  n[1] = MFMA16(mk8(H(1), H(3)), ph[0], z4);           n[1] = MFMA16(mk8(H(14), H(16)), ph[2], n[1]);
  n[1] = MFMA16(mk8(H(5), z2), ph[1], n[1]);           n[1] = MFMA16(mk8(H(18), z2), ph[3], n[1]);
  n[2] = MFMA16(mk8(z2, H(4)), ph[0], z4);             n[2] = MFMA16(mk8(z2, H(17)), ph[2], n[2]);
  n[2] = MFMA16(mk8(H(6), H(8)), ph[1], n[2]);         n[2] = MFMA16(mk8(H(19), H(21)), ph[3], n[2]);
  n[3] = MFMA16(mk8(H(7), H(9)), ph[1], z4);           n[3] = MFMA16(mk8(H(20), H(22)), ph[3], n[3]);
  n[3] = MFMA16(mk8(H(11), H(24)), ph[4], n[3]);
  n[4] = MFMA16(mk8(z2, H(10)), ph[1], z4);            n[4] = MFMA16(mk8(z2, H(23)), ph[3], n[4]);
  n[4] = MFMA16(mk8(H(12), H(25)), ph[4], n[4]);
  auto Z = [&](int sg, int t, int wh) { return H(26 + (sg * 5 + t) * 2 + wh); };
  f32x4 d0 = MFMA16(mk8(Z(0, 0, 0), Z(0, 1, 0)), ph[0], z4);
  d0 = MFMA16(mk8(Z(0, 2, 0), Z(0, 3, 0)), ph[1], d0);
  d0 = MFMA16(mk8(Z(1, 0, 0), Z(1, 1, 0)), ph[2], d0);
  d0 = MFMA16(mk8(Z(1, 2, 0), Z(1, 3, 0)), ph[3], d0);
  d0 = MFMA16(mk8(Z(0, 4, 0), Z(1, 4, 0)), ph[4], d0);
  f32x4 d1 = MFMA16(mk8(z2, Z(0, 3, 1)), ph[1], z4);
  d1 = MFMA16(mk8(z2, Z(1, 3, 1)), ph[3], d1);
  d1 = MFMA16(mk8(Z(0, 4, 1), Z(1, 4, 1)), ph[4], d1);
  float den[5] = {d0[0], d0[1], d0[2], d0[3], d1[0]};
#pragma unroll
  for (int t = 0; t < 5; ++t) {
    float rc = __builtin_amdgcn_rcpf(den[t] + 1e-6f);
    o[t] = u32x2{pack2(n[t][0] * rc, n[t][1] * rc), pack2(n[t][2] * rc, n[t][3] * rc)};
  }
}

__global__ __launch_bounds__(256) void k_o(char* ws) {
  const int wid = blockIdx.x * 4 + (threadIdx.x >> 6), lane = threadIdx.x & 63;
  const int tt = wid >> 3, g8 = wid & 7;
  bf16x8 ph[5];
#pragma unroll
  for (int k = 0; k < 5; ++k) ph[k] = *(const bf16x8*)(ws + OFF_PHI + ((size_t)(tt * 8 + g8) * 5 + k) * 1024 + lane * 16);
  u32x2 o[5];
  compute_o(ph, ws + OFF_KVH + (size_t)g8 * 46 * 512 + lane * 8, o);
#pragma unroll
  for (int t = 0; t < 5; ++t) {
    int T = 5 * g8 + t;
    *(u32x2*)(ws + OFF_O + ((size_t)tt * 20 + (T >> 1)) * 1024 + lane * 16 + (T & 1) * 8) = o[t];
  }
}

#define GLD(dst, ptr, off) asm volatile("global_load_dwordx4 %0, %1, off offset:" #off : "=v"(dst) : "v"(ptr) : "memory")

template <int NST>
DI void phase2_stream(const char* pa, unsigned astr, const char* pb, f32x4 (&acc)[5][4]) {
  const char* pa1 = pa + astr; const char* pa2 = pa1 + astr; const char* pa3 = pa2 + astr;
  constexpr int ST = 3;
  u32x4 v[ST][9];
#define ISSUE(s) do { GLD(v[s][0], pa, 0); GLD(v[s][1], pa1, 0); GLD(v[s][2], pa2, 0); GLD(v[s][3], pa3, 0); \
    GLD(v[s][4], pb, -2048); GLD(v[s][5], pb, -1024); GLD(v[s][6], pb, 0); GLD(v[s][7], pb, 1024); GLD(v[s][8], pb, 2048); \
    pa += 1024; pa1 += 1024; pa2 += 1024; pa3 += 1024; pb += 5120; } while (0)
#define COMPUTE(s) do { _Pragma("unroll") for (int i_ = 0; i_ < 9; ++i_) asm volatile("" : "+v"(v[s][i_])); \
    _Pragma("unroll") for (int n = 0; n < 5; ++n) _Pragma("unroll") for (int t = 0; t < 4; ++t) \
      acc[n][t] = MFMA16(__builtin_bit_cast(bf16x8, v[s][4 + n]), __builtin_bit_cast(bf16x8, v[s][t]), acc[n][t]); \
    __builtin_amdgcn_sched_barrier(0); } while (0)
#define WAITV(n) do { asm volatile("s_waitcnt vmcnt(" #n ")" ::: "memory"); __builtin_amdgcn_sched_barrier(0); } while (0)
  ISSUE(0); ISSUE(1); ISSUE(2);
  WAITV(18); COMPUTE(0); ISSUE(0);
  WAITV(18); COMPUTE(1); ISSUE(1);
  WAITV(18); COMPUTE(2); ISSUE(2);
  WAITV(18); COMPUTE(0); ISSUE(0);
  WAITV(18); COMPUTE(1); ISSUE(1);
  WAITV(18); COMPUTE(2); ISSUE(2);
  if constexpr (NST == 10) {
    WAITV(18); COMPUTE(0); ISSUE(0);
    WAITV(18); COMPUTE(1);
    WAITV(9); COMPUTE(2);
    WAITV(0); COMPUTE(0);
  } else {
    WAITV(18); COMPUTE(0);
    WAITV(9); COMPUTE(1);
    WAITV(0); COMPUTE(2);
  }
}

__global__ __launch_bounds__(512, 2) void k_phase2(const u16* x, const char* ws, u16* out) {
  extern __shared__ __attribute__((aligned(16))) char smem[];
  const int b = blockIdx.x, xcd = b & 7, idx = b >> 3;
  const int w = __builtin_amdgcn_readfirstlane(threadIdx.x >> 6), lane = threadIdx.x & 63;
  const int rt = (xcd >> 1) * 8 + (idx >> 2), ct = (xcd & 1) * 4 + (idx & 3);
  f32x4 acc[5][4];
#pragma unroll
  for (int n = 0; n < 5; ++n)
#pragma unroll
    for (int t = 0; t < 4; ++t) acc[n][t] = f32x4{0, 0, 0, 0};
  if (w < 6) {
    phase2_stream<9>(ws + OFF_ACT + ((size_t)(rt * 4) * 54 + 9 * w) * 1024 + lane * 16, 54 * 1024,
                     ws + OFF_W2F + ((size_t)ct * 74 + 9 * w) * 5120 + lane * 16 + 2048, acc);
  } else {
    phase2_stream<10>(ws + OFF_O + ((size_t)(rt * 4) * 20 + 10 * (w - 6)) * 1024 + lane * 16, 20 * 1024,
                      ws + OFF_W2F + ((size_t)ct * 74 + 54 + 10 * (w - 6)) * 5120 + lane * 16 + 2048, acc);
  }
#pragma unroll
  for (int n = 0; n < 5; ++n)
#pragma unroll
    for (int t = 0; t < 4; ++t) *(f32x4*)(smem + (w * 20 + n * 4 + t) * 1024 + lane * 16) = acc[n][t];
  __syncthreads();
  for (int tile = w; tile < 20; tile += 8) {
    int n = tile >> 2, t = tile & 3;
    const char* q = smem + tile * 1024 + lane * 16;
    f32x4 bsum = *(const f32x4*)(q);
#pragma unroll
    for (int k = 1; k < 6; ++k) bsum += *(const f32x4*)(q + k * 20480);
    f32x4 ssum = *(const f32x4*)(q + 6 * 20480) + *(const f32x4*)(q + 7 * 20480);
    size_t o = (size_t)(rt * 64 + t * 16 + (lane & 15)) * 640 + ct * 80 + n * 16 + 4 * (lane >> 4);
    u32x2 xv = *(const u32x2*)(x + o);
    float r[4];
    float xs[4] = {bf_lo(xv[0]), bf_hi(xv[0]), bf_lo(xv[1]), bf_hi(xv[1])};
#pragma unroll
    for (int j = 0; j < 4; ++j) {
      float bb = bsum[j];
      float g = bb * __builtin_amdgcn_rcpf(1.0f + __builtin_amdgcn_exp2f(-bb * LOG2E));
      r[j] = fmaf(ssum[j], g, xs[j]);
    }
    *(u32x2*)(out + o) = u32x2{pack2(r[0], r[1]), pack2(r[2], r[3])};
  }
}
}

extern "C" void kernel_launch(void* const* d_in, const int* in_sizes, int n_in, void* d_out, int out_size, void* d_ws,
                              size_t ws_size, hipStream_t stream) {
  using namespace hk;
  P1 p;
  p.x = (const u16*)d_in[0]; p.wqkv = (const u16*)d_in[1]; p.wphi = (const u16*)d_in[2]; p.wo = (const u16*)d_in[3];
  p.w1 = (const u16*)d_in[5]; p.b1 = (const u16*)d_in[6]; p.w2 = (const u16*)d_in[7]; p.ws = (char*)d_ws;
  (void)hipFuncSetAttribute((const void*)k_phase1, hipFuncAttributeMaxDynamicSharedMemorySize, L_END);
  (void)hipFuncSetAttribute((const void*)k_phase2, hipFuncAttributeMaxDynamicSharedMemorySize, 163840);
  k_phase1<<<256, 512, L_END, stream>>>(p);
  k_kvred<<<288, 64, 0, stream>>>((char*)d_ws);
  k_o<<<256, 256, 0, stream>>>((char*)d_ws);
  k_phase2<<<256, 512, 163840, stream>>>(p.x, (const char*)d_ws, (u16*)d_out);
}
```

```cpp
#include <hip/hip_runtime.h>
#include <hip/hip_bf16.h>

typedef unsigned short u16;
typedef __bf16 bf16x2 __attribute__((ext_vector_type(2)));
typedef __bf16 bf16x4 __attribute__((ext_vector_type(4)));
typedef __bf16 bf16x8 __attribute__((ext_vector_type(8)));
typedef float f32x2 __attribute__((ext_vector_type(2)));
typedef float f32x4 __attribute__((ext_vector_type(4)));
typedef unsigned u32x2 __attribute__((ext_vector_type(2)));
typedef unsigned u32x4 __attribute__((ext_vector_type(4)));
#define LDS3 __attribute__((address_space(3)))
#define DI __device__ __forceinline__
#define MFMA16(a, b, c) __builtin_amdgcn_mfma_f32_16x16x32_bf16((a), (b), (c), 0, 0, 0)

#define STAMP(i)
#define STAMP_DECL
#define STAMP_FLUSH(ws, slot)
namespace hk {
constexpr int S = 2048, D = 640, F = 1728;
constexpr float LOG2E = 1.4426950408889634f;
constexpr float LN2 = 0.6931471805599453f;
constexpr size_t OFF_ACT = 0;
constexpr size_t OFF_O = OFF_ACT + (size_t)128 * 54 * 1024;
constexpr size_t OFF_PHI = OFF_O + (size_t)128 * 20 * 1024;
constexpr size_t OFF_KVP = OFF_PHI + (size_t)128 * 8 * 5 * 1024;
constexpr size_t OFF_KVH = OFF_KVP + (size_t)8 * 16 * 60 * 256;
constexpr size_t OFF_W2F = OFF_KVH + (size_t)8 * 30 * 256;
constexpr size_t OFF_STP = OFF_W2F + (size_t)8 * 74 * 5 * 1024;
constexpr size_t OFF_CSP = OFF_STP + (size_t)16 * 10 * 1024;
constexpr size_t OFF_FLG = OFF_CSP + (size_t)16 * 10 * 1024;
constexpr int F_EARLY = 128;
constexpr int XB = 16384, WROW = 480, STAGE = XB + 64 * WROW;
constexpr int L_PAD = 3 * STAGE;
constexpr int L_RAW = L_PAD + 2048;
constexpr int L_BIAS = L_RAW + 4096;
constexpr int L_ZERO = L_BIAS + 1024;
constexpr int L_BIASL = L_ZERO + 64;
constexpr int L_CS = L_BIASL + 640;
constexpr int L_SUM = L_CS + 960;
constexpr int L_END = L_SUM + 1024;
constexpr int E_PHI = 0;
constexpr int E_V = E_PHI + 40960;

DI unsigned pack2(float a, float b) {
  f32x2 v = {a, b};
  return __builtin_bit_cast(unsigned, __builtin_convertvector(v, bf16x2));
}
DI unsigned pack2v(f32x2 v) { return __builtin_bit_cast(unsigned, __builtin_convertvector(v, bf16x2)); }
DI bf16x2 as_bf2(unsigned u) { return __builtin_bit_cast(bf16x2, u); }
DI float bf_lo(unsigned u) { return __uint_as_float(u << 16); }
DI float bf_hi(unsigned u) { return __uint_as_float(u & 0xffff0000u); }
DI float rscatter_add(float m0, float m1, float m2, float m3) {
  auto a = __builtin_amdgcn_permlane16_swap(__float_as_uint(m0), __float_as_uint(m1), false, false);
  auto b = __builtin_amdgcn_permlane16_swap(__float_as_uint(m2), __float_as_uint(m3), false, false);
  float A = __uint_as_float(a[0]) + __uint_as_float(a[1]);
  float B = __uint_as_float(b[0]) + __uint_as_float(b[1]);
  auto c = __builtin_amdgcn_permlane32_swap(__float_as_uint(A), __float_as_uint(B), false, false);
  return __uint_as_float(c[0]) + __uint_as_float(c[1]);
}
DI f32x2 lo2(f32x4 v) { return __builtin_shufflevector(v, v, 0, 1); }
DI f32x2 hi2(f32x4 v) { return __builtin_shufflevector(v, v, 2, 3); }
DI f32x2 fma2(f32x2 a, f32x2 b, f32x2 c) { return __builtin_elementwise_fma(a, b, c); }
DI f32x2 splat2(float a) { return f32x2{a, a}; }
DI f32x2 exp2v(f32x2 a) { return f32x2{__builtin_amdgcn_exp2f(a[0]), __builtin_amdgcn_exp2f(a[1])}; }
DI f32x2 rcpv(f32x2 a) { return f32x2{__builtin_amdgcn_rcpf(a[0]), __builtin_amdgcn_rcpf(a[1])}; }
DI void dual_softmax(const f32x4 (&p)[5], u32x2 (&pos)[5], u32x2 (&neg)[5]) {
  float mx = fmaxf(p[0][0], p[0][1]), mn = fminf(p[0][0], p[0][1]);
  mx = fmaxf(fmaxf(mx, p[0][2]), p[0][3]); mn = fminf(fminf(mn, p[0][2]), p[0][3]);
#pragma unroll
  for (int t = 1; t < 5; ++t) {
    mx = fmaxf(fmaxf(mx, p[t][0]), p[t][1]); mx = fmaxf(fmaxf(mx, p[t][2]), p[t][3]);
    mn = fminf(fminf(mn, p[t][0]), p[t][1]); mn = fminf(fminf(mn, p[t][2]), p[t][3]);
  }
  const f32x2 L = splat2(LOG2E), nL = splat2(-LOG2E), cx = splat2(-mx * LOG2E), cn = splat2(mn * LOG2E);
  f32x2 ep[10], en[10];
#pragma unroll
  for (int t = 0; t < 5; ++t) {
    ep[2 * t] = exp2v(fma2(lo2(p[t]), L, cx)); ep[2 * t + 1] = exp2v(fma2(hi2(p[t]), L, cx));
    en[2 * t] = exp2v(fma2(lo2(p[t]), nL, cn)); en[2 * t + 1] = exp2v(fma2(hi2(p[t]), nL, cn));
  }
  f32x2 sp = ep[0], sn = en[0];
#pragma unroll
  for (int i = 1; i < 10; ++i) { sp += ep[i]; sn += en[i]; }
  const f32x2 ip = splat2(__builtin_amdgcn_rcpf(sp[0] + sp[1])), in_ = splat2(__builtin_amdgcn_rcpf(sn[0] + sn[1]));
#pragma unroll
  for (int t = 0; t < 5; ++t) {
    pos[t] = u32x2{pack2v(ep[2 * t] * ip), pack2v(ep[2 * t + 1] * ip)};
    neg[t] = u32x2{pack2v(en[2 * t] * in_), pack2v(en[2 * t + 1] * in_)};
  }
}
DI f32x2 mish2(f32x2 hl) {
  f32x2 e = exp2v(hl);
  f32x2 d = fma2(e, e + splat2(2.0f), splat2(2.0f));
  f32x2 t = fma2(rcpv(d), splat2(-2.0f * LN2), splat2(LN2));
  return hl * t;
}
DI bf16x8 mk8(u32x2 a, u32x2 b) {
  u32x4 v = {a[0], a[1], b[0], b[1]};
  return __builtin_bit_cast(bf16x8, v);
}
DI bf16x8 tr2(const char* a, const char* b) {
  bf16x4 lo = __builtin_amdgcn_ds_read_tr16_b64_v4bf16((LDS3 bf16x4*)(a));
  bf16x4 hi = __builtin_amdgcn_ds_read_tr16_b64_v4bf16((LDS3 bf16x4*)(b));
  return __builtin_shufflevector(lo, hi, 0, 1, 2, 3, 4, 5, 6, 7);
}

struct P1 {
  const u16* x; const u16* wqkv; const u16* wphi; const u16* wo; const u16* w1; const u16* b1; const u16* w2;
  char* ws;
};

DI unsigned lds_addr(const void* p) { return (unsigned)(size_t)(LDS3 const char*)p; }
DI void glds1(const void* g, unsigned lds) {
  unsigned keep;
  asm volatile("s_mov_b32 %0, m0\n\ts_mov_b32 m0, %2\n\ts_nop 0\n\tglobal_load_lds_dwordx4 %1, off\n\ts_mov_b32 m0, %0"
               : "=&s"(keep) : "v"(g), "s"(lds) : "memory");
}
DI void glds6(const void* g0, const void* g1, const void* g2, const void* g3, const void* g4, const void* g5, unsigned lds, unsigned lds5) {
  unsigned keep;
  asm volatile("s_mov_b32 %0, m0\n\t"
               "s_mov_b32 m0, %7\n\ts_nop 0\n\tglobal_load_lds_dwordx4 %1, off\n\t"
               "s_add_u32 m0, %7, 0x2000\n\ts_nop 0\n\tglobal_load_lds_dwordx4 %2, off\n\t"
               "s_add_u32 m0, %7, 0x4000\n\ts_nop 0\n\tglobal_load_lds_dwordx4 %3, off\n\t"
               "s_add_u32 m0, %7, 0x6000\n\ts_nop 0\n\tglobal_load_lds_dwordx4 %4, off\n\t"
               "s_add_u32 m0, %7, 0x8000\n\ts_nop 0\n\tglobal_load_lds_dwordx4 %5, off\n\t"
               "s_mov_b32 m0, %8\n\ts_nop 0\n\tglobal_load_lds_dwordx4 %6, off\n\t"
               "s_mov_b32 m0, %0"
               : "=&s"(keep) : "v"(g0), "v"(g1), "v"(g2), "v"(g3), "v"(g4), "v"(g5), "s"(lds), "s"(lds5) : "memory", "scc");
}

typedef unsigned long long u64;
DI u64 next_tag(u64 e) { return e * 0x9E3779B97F4A7C15ull + 0x632BE59BD9B4E019ull; }
DI void st_flag(char* p, u64 v) { __hip_atomic_store((u64*)p, v, __ATOMIC_RELAXED, __HIP_MEMORY_SCOPE_AGENT); }
DI void st_f32_sc1(char* p, float v) { __hip_atomic_store((float*)p, v, __ATOMIC_RELAXED, __HIP_MEMORY_SCOPE_AGENT); }
DI void poll256(const char* flags, u64 tag, int lane) {
  const char* q = flags + lane * 32;
  const unsigned tl = (unsigned)tag, th = (unsigned)(tag >> 32);
  for (int spin = 0; spin < (1 << 18); ++spin) {
    u32x4 a, b;
    asm volatile("global_load_dwordx4 %0, %2, off sc1\n\tglobal_load_dwordx4 %1, %2, off offset:16 sc1\n\ts_waitcnt vmcnt(0)"
                 : "=&v"(a), "=&v"(b) : "v"(q) : "memory");
    bool ok = a[0] == tl && a[1] == th && a[2] == tl && a[3] == th && b[0] == tl && b[1] == th && b[2] == tl && b[3] == th;
    if (__all(ok)) break;
    __builtin_amdgcn_s_sleep(2);
  }
}

template <int NT> struct Frags { bf16x8 x[2]; bf16x8 w[NT]; };

template <int TYPE, int C>
DI void phase1_body(const P1& p, char* smem, int rb, int g8, int w, int lane) {
  constexpr int NT = (TYPE == 1 && C == 1) ? 8 : 7;
  constexpr bool VW = (TYPE == 0 && C == 1);
  STAMP_DECL; STAMP(0);
  const int r = w & 3;
  const int lg = lane >> 4, l15 = lane & 15;
  const unsigned lds0 = lds_addr(smem);
  u32x2 ev;
  asm volatile("global_load_dwordx2 %0, %1, off sc1" : "=v"(ev) : "v"(p.ws + OFF_FLG) : "memory");
  const int job = TYPE * 8 + g8;
  const int rot = (rb + job) % 10;
  int cnext = rot;
  const char* xsrc[2];
  const char* wsrc[4];
  unsigned winc[4];
#pragma unroll
  for (int i = 0; i < 2; ++i) {
    int row = (i * 8 + w) * 8 + (lane >> 3);
    int ch = (lane & 7) ^ ((row >> 1) & 7);
    xsrc[i] = (const char*)p.x + (unsigned)((rb * 128 + row) * 1280 + ch * 16 + rot * 128);
  }
  const int t0w = TYPE == 0 ? 4 * g8 : min(32 + 10 * g8, 98);
#pragma unroll
  for (int i = 0; i < 4; ++i) {
    int ci = (i * 8 + w) * 64 + lane;
    if (i == 3) ci = ci >= 1920 ? ci - 128 : ci;
    int pos = (ci * 2185) >> 16, c16 = ci - pos * 30;
    int kk = pos & 15;
    int k = (kk >= 4 && kk < 12) ? (pos ^ 12) : pos;
    bool isq = c16 < (TYPE == 0 ? 20 : 10);
    int colb;
    if (TYPE == 0) colb = isq ? (1280 + 160 * g8 + c16 * 16 + (c16 >= 10 ? 1120 : 0)) : (32 * t0w + (min(c16, 27) - 20) * 16);
    else colb = isq ? (160 * g8 + c16 * 16) : (32 * t0w + (c16 - 10) * 16);
    unsigned stride = isq ? 3840u : 3456u;
    wsrc[i] = (isq ? (const char*)p.wqkv : (const char*)p.w1) + (unsigned)((k + rot * 64) * stride + colb);
    winc[i] = stride * 64;
  }
  auto wrap = [&]() {
    if (++cnext == 10) {
      cnext = 0;
#pragma unroll
      for (int i = 0; i < 2; ++i) xsrc[i] -= 1280;
#pragma unroll
      for (int i = 0; i < 4; ++i) wsrc[i] -= 10 * winc[i];
    }
  };
  auto issue = [&](int stage) {
    unsigned sb = lds0 + stage * STAGE + w * 1024;
    glds6(xsrc[0], xsrc[1], wsrc[0], wsrc[1], wsrc[2], wsrc[3], sb, w < 6 ? sb + 0xa000 : lds0 + L_PAD + (w - 6) * 1024);
#pragma unroll
    for (int i = 0; i < 2; ++i) xsrc[i] += 128;
#pragma unroll
    for (int i = 0; i < 4; ++i) wsrc[i] += winc[i];
    wrap();
  };
  if (w < 4) {
    int ci = w * 64 + lane; ci = ci < 200 ? ci : 199;
    glds1((const char*)p.wphi + g8 * 3200 + ci * 16, lds0 + L_RAW + w * 1024);
  } else if (w == 4) {
    int nb = TYPE == 0 ? 8 : 20;
    int ci = lane < nb ? lane : nb - 1;
    glds1((const char*)p.b1 + t0w * 32 + ci * 16, lds0 + L_BIAS);
  } else if (w == 5) {
    if (lane < 16) *(unsigned*)(smem + L_ZERO + lane * 4) = 0u;
  }
  issue(0);
  __builtin_amdgcn_s_barrier();
  issue(1);
  issue(2);

  const unsigned xa0 = (32 * r + l15) * 128 + ((lg ^ ((l15 >> 1) & 7)) * 16);
  const unsigned xa1 = xa0 ^ 64;
  const unsigned wrow = XB + (16 * (lg >> 1) + 4 * (lg & 1) + (l15 >> 2)) * WROW;
  const unsigned wa = wrow + (l15 & 3) * 8;
  const unsigned wv = wrow + 160 + (l15 & 3) * 40;

  f32x4 acc[NT][2];
#pragma unroll
  for (int t = 0; t < NT; ++t) { acc[t][0] = f32x4{0, 0, 0, 0}; acc[t][1] = f32x4{0, 0, 0, 0}; }
  float cs[2] = {0.f, 0.f};
  float s1 = 0.f, s2 = 0.f;
  const bf16x2 ones2 = {(__bf16)1.0f, (__bf16)1.0f};

  auto unit_of = [](int t) constexpr {
    return TYPE == 0 ? (C == 0 ? (t < 5 ? t : t + 5) : (t < 5 ? t + 5 : t + 7)) : (C == 0 ? t : t + 7);
  };
  auto rd_x = [&](Frags<NT>& f, const char* sb, int ks, int tt) { f.x[tt] = *(const bf16x8*)(sb + (ks ? xa1 : xa0) + tt * 2048); };
  auto rd_w = [&](Frags<NT>& f, const char* sb, int ks, int t) {
    const char* q = (VW && t < 5) ? sb + wv + t * 8 : sb + wa + unit_of(t) * 32;
    f.w[t] = tr2(q + (32 * ks) * WROW, q + (32 * ks + 8) * WROW);
  };
  auto dots2 = [&](const bf16x8& wa_, const bf16x8& wb_) {
    u32x4 a = __builtin_bit_cast(u32x4, wa_), b = __builtin_bit_cast(u32x4, wb_);
    unsigned a0 = a[0], a1 = a[1], a2 = a[2], a3 = a[3], b0 = b[0], b1 = b[1], b2 = b[2], b3 = b[3];
    asm volatile("s_nop 1\n\t"
                 "v_dot2c_f32_bf16 %0, 0x3f803f80, %2\n\tv_dot2c_f32_bf16 %1, 0x3f803f80, %6\n\t"
                 "v_dot2c_f32_bf16 %0, 0x3f803f80, %3\n\tv_dot2c_f32_bf16 %1, 0x3f803f80, %7\n\t"
                 "v_dot2c_f32_bf16 %0, 0x3f803f80, %4\n\tv_dot2c_f32_bf16 %1, 0x3f803f80, %8\n\t"
                 "v_dot2c_f32_bf16 %0, 0x3f803f80, %5\n\tv_dot2c_f32_bf16 %1, 0x3f803f80, %9\n\ts_nop 3"
                 : "+v"(cs[0]), "+v"(cs[1]) : "v"(a0), "v"(a1), "v"(a2), "v"(a3), "v"(b0), "v"(b1), "v"(b2), "v"(b3));
  };
  auto kstep = [&](const Frags<NT>& f, Frags<NT>& g, const char* nsb, int nks, bool do_rd, bool do_issue, int istage, bool do_dots) {
    u32x4 xv = __builtin_bit_cast(u32x4, f.x[C]);
    unsigned sbi = lds0 + istage * STAGE + w * 1024;
#pragma unroll
    for (int t = 0; t < NT; ++t) {
      if (do_rd) {
        if (t < 2) rd_x(g, nsb, nks, t);
        rd_w(g, nsb, nks, t);
      }
#pragma unroll
      for (int tt = 0; tt < 2; ++tt)
        acc[t][tt] = (VW && t < 5) ? MFMA16(f.x[tt], f.w[t], acc[t][tt]) : MFMA16(f.w[t], f.x[tt], acc[t][tt]);
      if (do_dots && t < 4) {
        bf16x2 e = as_bf2(xv[t]);
        s1 = __builtin_amdgcn_fdot2_f32_bf16(e, ones2, s1, false);
        s2 = __builtin_amdgcn_fdot2_f32_bf16(e, e, s2, false);
      }
      if (do_issue && t < 6) {
        if (t < 2) { glds1(xsrc[t], sbi + t * 0x2000); xsrc[t] += 128; }
        else {
          glds1(wsrc[t - 2], (t < 5 || w < 6) ? sbi + t * 0x2000 : lds0 + L_PAD + (w - 6) * 1024);
          wsrc[t - 2] += winc[t - 2];
        }
      }
      __builtin_amdgcn_sched_barrier(0);
    }
    if (do_issue) wrap();
    if (do_dots) {
      if (r == 0) dots2(f.w[0], f.w[4]);
      else if (r == 1) dots2(f.w[1], f.w[5]);
      else if (r == 2) dots2(f.w[2], f.w[6]);
      else dots2(f.w[3], f.w[NT - 1]);
    }
  };

  Frags<NT> f0, f1;
  asm volatile("s_waitcnt vmcnt(12)" ::: "memory");
  __builtin_amdgcn_s_waitcnt(0xC07F);
  __builtin_amdgcn_s_barrier();
  __builtin_amdgcn_sched_barrier(0);
  STAMP(1);
  rd_x(f0, smem, 0, 0); rd_x(f0, smem, 0, 1);
#pragma unroll
  for (int t = 0; t < NT; ++t) rd_w(f0, smem, 0, t);
  {
    int tid = w * 64 + lane;
    if (tid < 160) *(float*)(smem + L_BIASL + tid * 4) = bf_lo(*(const u16*)(smem + L_BIAS + tid * 2)) * LOG2E;
  }
  asm volatile("" : "+v"(ev));
  const u64 tag = next_tag(((u64)__builtin_amdgcn_readfirstlane(ev[1]) << 32) | (unsigned)__builtin_amdgcn_readfirstlane(ev[0]));
  __builtin_amdgcn_s_waitcnt(0xC07F);
  __builtin_amdgcn_sched_barrier(0);
  kstep(f0, f1, smem, 1, true, false, 0, true);
  asm volatile("s_waitcnt vmcnt(6)" ::: "memory");
  __builtin_amdgcn_s_waitcnt(0xC07F);
  __builtin_amdgcn_s_barrier();
  __builtin_amdgcn_sched_barrier(0);
  kstep(f1, f0, smem + STAGE, 0, true, false, 0, true);
  {
    float v = rscatter_add(s1, s2, cs[0], cs[1]);
    int t1 = r + 4;
    int u0 = (TYPE == 0 && C == 1) ? r + 5 : ((TYPE == 1 && C == 1) ? r + 7 : r);
    int u1 = TYPE == 0 ? (C == 0 ? (t1 < 5 ? t1 : t1 + 5) : (t1 < 5 ? t1 + 5 : t1 + 7)) : (C == 0 ? t1 : t1 + 7);
    size_t a = lg < 2 ? OFF_STP + (size_t)(rb * 10 + rot) * 1024 + lg * 512 + (32 * r + 16 * C + l15) * 4
                      : OFF_CSP + (size_t)(job * 10 + rot) * 1024 + ((lg == 2 ? u0 : u1) * 16 + l15) * 4;
    bool on = lg < 2 ? job < 10 : (rb < 10 && (lg == 2 || t1 < NT));
    if (on) st_f32_sc1(p.ws + a, v);
  }
  issue(0);
  int stage = 1;
  unsigned pv[10];
#pragma unroll 1
  for (int i = 1; i < 9; ++i) {
    __builtin_amdgcn_s_waitcnt(0xC07F);
    __builtin_amdgcn_sched_barrier(0);
    kstep(f0, f1, smem + stage * STAGE, 1, true, false, 0, false);
    int nstage = stage == 2 ? 0 : stage + 1;
    if (i < 8) asm volatile("s_waitcnt vmcnt(6)" ::: "memory");
    else asm volatile("s_waitcnt vmcnt(0)" ::: "memory");
    __builtin_amdgcn_s_waitcnt(0xC07F);
    __builtin_amdgcn_s_barrier();
    __builtin_amdgcn_sched_barrier(0);
    if (i == 1 && w == 0 && lane == 0) st_flag(p.ws + OFF_FLG + F_EARLY + blockIdx.x * 8, tag);
    kstep(f1, f0, smem + nstage * STAGE, 0, true, i < 7, stage, false);
    stage = nstage;
  }
  poll256(p.ws + OFF_FLG + F_EARLY, tag, lane);
  if (blockIdx.x == 0 && w == 0 && lane == 0) st_flag(p.ws + OFF_FLG, tag);
  {
    int tid = w * 64 + lane;
    const char* q = p.ws + (tid < 256 ? OFF_STP + (size_t)rb * 10240 + tid * 4 : OFF_CSP + (size_t)job * 10240 + (tid - 256) * 4);
    const char* q2 = q + 5120;
    asm volatile("global_load_dword %0, %10, off sc1\n\tglobal_load_dword %1, %10, off offset:1024 sc1\n\t"
                 "global_load_dword %2, %10, off offset:2048 sc1\n\tglobal_load_dword %3, %10, off offset:3072 sc1\n\t"
                 "global_load_dword %4, %11, off offset:-1024 sc1\n\tglobal_load_dword %5, %11, off sc1\n\t"
                 "global_load_dword %6, %11, off offset:1024 sc1\n\tglobal_load_dword %7, %11, off offset:2048 sc1\n\t"
                 "global_load_dword %8, %11, off offset:3072 sc1\n\tglobal_load_dword %9, %12, off sc1"
                 : "=&v"(pv[0]), "=&v"(pv[1]), "=&v"(pv[2]), "=&v"(pv[3]), "=&v"(pv[4]), "=&v"(pv[5]), "=&v"(pv[6]), "=&v"(pv[7]), "=&v"(pv[8]), "=&v"(pv[9])
                 : "v"(q), "v"(q2), "v"(q2 + 4096) : "memory");
  }
  __builtin_amdgcn_s_waitcnt(0xC07F);
  __builtin_amdgcn_sched_barrier(0);
  kstep(f0, f1, smem + stage * STAGE, 1, true, false, 0, false);
  __builtin_amdgcn_s_waitcnt(0xC07F);
  __builtin_amdgcn_sched_barrier(0);
  kstep(f1, f0, smem, 0, false, false, 0, false);
  STAMP(3);
  {
    asm volatile("s_waitcnt vmcnt(0)" : "+v"(pv[0]), "+v"(pv[1]), "+v"(pv[2]), "+v"(pv[3]), "+v"(pv[4]), "+v"(pv[5]), "+v"(pv[6]), "+v"(pv[7]), "+v"(pv[8]), "+v"(pv[9]) :: "memory");
    float v = 0.f;
#pragma unroll
    for (int i = 0; i < 10; ++i) v += __uint_as_float(pv[i]);
    int tid = w * 64 + lane;
    unsigned a = tid < 256 ? L_SUM + (tid & 127) * 8 + (tid >> 7) * 4 : L_CS + (tid - 256) * 4;
    if (tid < 496) *(float*)(smem + a) = v;
  }
  __syncthreads();
  auto stat_of = [&](int tok) {
    f32x2 a = *(const f32x2*)(smem + L_SUM + tok * 8);
    float mean = a[0] * (1.0f / 640.0f);
    float var = fmaxf(fmaf(-mean, mean, a[1] * (1.0f / 640.0f)), 0.f);
    float rstd = __builtin_amdgcn_rsqf(var + 1e-5f);
    return f32x2{rstd, -rstd * mean};
  };
  f32x2 st[2];
#pragma unroll
  for (int tt = 0; tt < 2; ++tt) st[tt] = stat_of(32 * r + 16 * tt + l15);
  STAMP(4);
  if (C == 0) {
    bf16x8 ap[5][3];
    {
      const char* pa[6];
#pragma unroll
      for (int i = 0; i < 6; ++i) {
        int d0 = 16 * i + 4 * lg;
        int h = (d0 * 13) >> 8;
        bool ok = (h == (l15 & 3)) && i < 5;
        pa[i] = smem + (ok ? L_RAW + h * 800 + (d0 - 20 * h + (l15 >> 2)) * 40 : L_ZERO);
      }
#pragma unroll
      for (int t = 0; t < 5; ++t)
#pragma unroll
        for (int ks = 0; ks < 3; ++ks) ap[t][ks] = tr2(pa[2 * ks] + t * 8, pa[2 * ks + 1] + t * 8);
    }
    f32x4 c4[5];
#pragma unroll
    for (int t = 0; t < 5; ++t) c4[t] = *(const f32x4*)(smem + L_CS + (t * 16 + 4 * lg) * 4);
#pragma unroll
    for (int tt = 0; tt < 2; ++tt) {
      u32x2 kb[5];
      const f32x2 rs = splat2(st[tt][0]), nb = splat2(st[tt][1]);
#pragma unroll
      for (int t = 0; t < 5; ++t)
        kb[t] = u32x2{pack2v(fma2(lo2(acc[t][tt]), rs, nb * lo2(c4[t]))), pack2v(fma2(hi2(acc[t][tt]), rs, nb * hi2(c4[t])))};
      bf16x8 b0 = mk8(kb[0], kb[1]), b1 = mk8(kb[2], kb[3]), b2 = mk8(kb[4], u32x2{0u, 0u});
      f32x4 pk[5];
#pragma unroll
      for (int t = 0; t < 5; ++t) pk[t] = MFMA16(ap[t][2], b2, MFMA16(ap[t][1], b1, MFMA16(ap[t][0], b0, (f32x4{0, 0, 0, 0}))));
      u32x2 pos[5], neg[5];
      dual_softmax(pk, pos, neg);
      if (TYPE == 0) {
        int tok = 32 * r + 16 * tt + l15;
#pragma unroll
        for (int t = 0; t < 5; ++t) {
          *(u32x2*)(smem + E_PHI + (t * 128 + tok) * 32 + lg * 8) = pos[t];
          *(u32x2*)(smem + E_PHI + ((5 + t) * 128 + tok) * 32 + lg * 8) = neg[t];
        }
      } else {
        char* dst = p.ws + OFF_PHI + ((size_t)((rb * 8 + 2 * r + tt) * 8 + g8) * 5) * 1024 + lane * 16;
        *(u32x4*)(dst) = u32x4{pos[0][0], pos[0][1], pos[1][0], pos[1][1]};
        *(u32x4*)(dst + 1024) = u32x4{pos[2][0], pos[2][1], pos[3][0], pos[3][1]};
        *(u32x4*)(dst + 2048) = u32x4{neg[0][0], neg[0][1], neg[1][0], neg[1][1]};
        *(u32x4*)(dst + 3072) = u32x4{neg[2][0], neg[2][1], neg[3][0], neg[3][1]};
        *(u32x4*)(dst + 4096) = u32x4{pos[4][0], pos[4][1], neg[4][0], neg[4][1]};
      }
    }
  }
  if (VW) {
    f32x2 sv[2][4];
#pragma unroll
    for (int tt = 0; tt < 2; ++tt)
#pragma unroll
      for (int j = 0; j < 4; ++j) sv[tt][j] = stat_of(32 * r + 16 * tt + 4 * lg + j);
#pragma unroll
    for (int t = 0; t < 5; ++t) {
      float c1 = *(const float*)(smem + L_CS + ((5 + t) * 16 + l15) * 4);
      unsigned o[4];
#pragma unroll
      for (int tt = 0; tt < 2; ++tt) {
        float y0 = fmaf(acc[t][tt][0], sv[tt][0][0], sv[tt][0][1] * c1);
        float y1 = fmaf(acc[t][tt][1], sv[tt][1][0], sv[tt][1][1] * c1);
        float y2 = fmaf(acc[t][tt][2], sv[tt][2][0], sv[tt][2][1] * c1);
        float y3 = fmaf(acc[t][tt][3], sv[tt][3][0], sv[tt][3][1] * c1);
        o[2 * tt] = pack2(y0, y1); o[2 * tt + 1] = pack2(y2, y3);
      }
      *(u32x4*)(smem + E_V + (r * 5 + t) * 1024 + lane * 16) = u32x4{o[0], o[1], o[2], o[3]};
    }
  }
  STAMP(5);
  {
    constexpr int TW0 = (TYPE == 1 && C == 1) ? 0 : 5;
    constexpr int NP = (NT - TW0) / 2;
    f32x2 rsL[2], nbL[2];
#pragma unroll
    for (int tt = 0; tt < 2; ++tt) { rsL[tt] = splat2(st[tt][0] * LOG2E); nbL[tt] = splat2(st[tt][1] * LOG2E); }
#pragma unroll
    for (int pp = 0; pp < NP; ++pp) {
      const int ta = TW0 + 2 * pp;
      const int ua = unit_of(ta);
      const int wl = TYPE == 0 ? ua - 10 : ua - 5;
      const int wt = t0w + wl;
      bool skip = (TYPE == 1) && (g8 == 7) && (wl < 4);
      f32x4 bb[2], c4[2];
#pragma unroll
      for (int h = 0; h < 2; ++h) {
        bb[h] = *(const f32x4*)(smem + L_BIASL + ((wl + h) * 16 + 4 * lg) * 4);
        c4[h] = *(const f32x4*)(smem + L_CS + ((ua + h) * 16 + 4 * lg) * 4);
      }
#pragma unroll
      for (int tt = 0; tt < 2; ++tt) {
        unsigned o[4];
#pragma unroll
        for (int h = 0; h < 2; ++h) {
          o[2 * h] = pack2v(mish2(fma2(lo2(acc[ta + h][tt]), rsL[tt], fma2(nbL[tt], lo2(c4[h]), lo2(bb[h])))));
          o[2 * h + 1] = pack2v(mish2(fma2(hi2(acc[ta + h][tt]), rsL[tt], fma2(nbL[tt], hi2(c4[h]), hi2(bb[h])))));
        }
        if (!skip)
          *(u32x4*)(p.ws + OFF_ACT + ((size_t)(rb * 8 + 2 * r + tt) * 54 + (wt >> 1)) * 1024 + lane * 16) = u32x4{o[0], o[1], o[2], o[3]};
      }
    }
  }
  STAMP(6);
  if (TYPE == 0) {
    __syncthreads();
    const unsigned pa = E_PHI + (4 * lg + (l15 >> 2)) * 32 + (l15 & 3) * 8;
    const u32x4 one4 = {0x3f803f80u, 0x3f803f80u, 0x3f803f80u, 0x3f803f80u};
    const bool valid = (l15 >> 2) == lg;
    for (int un = w; un < 20; un += 8) {
      int m = un >> 1, hf = un & 1;
      f32x4 a[3] = {{0, 0, 0, 0}, {0, 0, 0, 0}, {0, 0, 0, 0}};
#pragma unroll
      for (int ks = 0; ks < 4; ++ks) {
        const char* q = smem + pa + (m * 128 + ks * 32) * 32;
        bf16x8 af = tr2(q, q + 512);
        const char* vb = smem + E_V + (ks * 5 + hf * 3) * 1024 + lane * 16;
        a[0] = MFMA16(af, *(const bf16x8*)(vb), a[0]);
        a[1] = MFMA16(af, *(const bf16x8*)(vb + 1024), a[1]);
        u32x4 b2 = *(const u32x4*)(vb + (hf ? 0 : 2048));
        b2 = hf ? one4 : b2;
        a[2] = MFMA16(af, __builtin_bit_cast(bf16x8, b2), a[2]);
      }
      if (valid) {
        char* dst = p.ws + OFF_KVP + ((size_t)(g8 * 16 + rb) * 60 + m * 6 + hf * 3) * 256 + (lg * 4 + (l15 & 3)) * 16;
#pragma unroll
        for (int j = 0; j < 3; ++j) *(f32x4*)(dst + j * 256) = a[j];
      }
    }
  }
  STAMP(7);
  STAMP_FLUSH(p.ws, blockIdx.x * 8 + w);
}

__global__ __launch_bounds__(512, 2) void k_phase1(P1 p) {
  extern __shared__ __attribute__((aligned(16))) char smem[];
  const int b = blockIdx.x, xcd = b & 7, idx = b >> 3;
  const int w = __builtin_amdgcn_readfirstlane(threadIdx.x >> 6), lane = threadIdx.x & 63;
  const int rb = (xcd >> 2) * 8 + (idx >> 2);
  const int jl = idx & 3, xc = xcd & 3;
  const int type = jl >> 1, g8 = 2 * xc + (jl & 1);
  const int c = w >> 2;
  if (type == 0) {
    if (c == 0) phase1_body<0, 0>(p, smem, rb, g8, w, lane);
    else phase1_body<0, 1>(p, smem, rb, g8, w, lane);
  } else {
    if (c == 0) phase1_body<1, 0>(p, smem, rb, g8, w, lane);
    else phase1_body<1, 1>(p, smem, rb, g8, w, lane);
  }
  for (int fr = b * 8 + w; fr < 2960; fr += 2048) {
    int nt = fr % 5, q = fr / 5, u = q % 74, ct = q / 74;
    int lg = lane >> 4;
    const u16 *s0, *s1;
    if (u < 54) { s0 = p.w2 + (size_t)(32 * u + 4 * lg) * 640; s1 = s0 + 16 * 640; }
    else {
      int T = 2 * (u - 54), T1 = T + 1;
      s0 = p.wo + (size_t)((4 * (T / 5) + lg) * 20 + 4 * (T % 5)) * 640;
      s1 = p.wo + (size_t)((4 * (T1 / 5) + lg) * 20 + 4 * (T1 % 5)) * 640;
    }
    int col = 80 * ct + 16 * nt + (lane & 15);
    unsigned v[8];
#pragma unroll
    for (int j = 0; j < 4; ++j) { v[j] = s0[j * 640 + col]; v[4 + j] = s1[j * 640 + col]; }
    *(u32x4*)(p.ws + OFF_W2F + (size_t)fr * 1024 + lane * 16) =
        u32x4{v[0] | (v[1] << 16), v[2] | (v[3] << 16), v[4] | (v[5] << 16), v[6] | (v[7] << 16)};
  }
}

__global__ __launch_bounds__(64) void k_kvred(char* ws) {
  const int g8 = blockIdx.x / 15, un = blockIdx.x % 15, lane = threadIdx.x;
  f32x4 a = {0, 0, 0, 0};
#pragma unroll
  for (int rb = 0; rb < 16; ++rb) a += *(const f32x4*)(ws + OFF_KVP + (size_t)(g8 * 16 + rb) * 15360 + un * 1024 + lane * 16);
  int tau = un * 4 + (lane >> 4), m = tau / 6, tn = tau - 6 * m;
  int sg = m >= 5 ? 1 : 0, t = m - 5 * sg;
  int ks = t < 4 ? sg * 2 + (t >> 1) : 4, hf = t < 4 ? (t & 1) : sg;
  *(u32x2*)(ws + OFF_KVH + (size_t)g8 * 7680 + (tn * 5 + ks) * 256 + (lane & 15) * 16 + hf * 8) = u32x2{pack2(a[0], a[1]), pack2(a[2], a[3])};
}

__global__ __launch_bounds__(256) void k_o(char* ws) {
  const int wid = blockIdx.x * 4 + (threadIdx.x >> 6), lane = threadIdx.x & 63;
  const int tt = wid >> 3, g8 = wid & 7;
  const int lg = lane >> 4, l15 = lane & 15;
  bf16x8 ph[5];
#pragma unroll
  for (int k = 0; k < 5; ++k) ph[k] = *(const bf16x8*)(ws + OFF_PHI + ((size_t)(tt * 8 + g8) * 5 + k) * 1024 + lane * 16);
  const bool valid = (l15 >> 2) == lg;
  const char* kv = ws + OFF_KVH + (size_t)g8 * 7680 + (lg * 4 + (l15 & 3)) * 16;
  f32x4 n[6];
#pragma unroll
  for (int tn = 0; tn < 6; ++tn) {
    n[tn] = f32x4{0, 0, 0, 0};
#pragma unroll
    for (int ks = 0; ks < 5; ++ks) {
      u32x4 a = *(const u32x4*)(kv + (tn * 5 + ks) * 256);
      a = valid ? a : u32x4{0u, 0u, 0u, 0u};
      n[tn] = MFMA16(__builtin_bit_cast(bf16x8, a), ph[ks], n[tn]);
    }
  }
  float rc = __builtin_amdgcn_rcpf(n[5][0] + 1e-6f);
#pragma unroll
  for (int t = 0; t < 5; ++t) {
    int T = 5 * g8 + t;
    *(u32x2*)(ws + OFF_O + ((size_t)tt * 20 + (T >> 1)) * 1024 + lane * 16 + (T & 1) * 8) =
        u32x2{pack2(n[t][0] * rc, n[t][1] * rc), pack2(n[t][2] * rc, n[t][3] * rc)};
  }
}

#define GLD(dst, ptr, off) asm volatile("global_load_dwordx4 %0, %1, off offset:" #off : "=v"(dst) : "v"(ptr) : "memory")

template <int NST>
DI void phase2_stream(const char* pa, unsigned astr, const char* pb, f32x4 (&acc)[5][4]) {
  const char* pa1 = pa + astr; const char* pa2 = pa1 + astr; const char* pa3 = pa2 + astr;
  constexpr int ST = 3;
  u32x4 v[ST][9];
#define ISSUE(s) do { GLD(v[s][0], pa, 0); GLD(v[s][1], pa1, 0); GLD(v[s][2], pa2, 0); GLD(v[s][3], pa3, 0); \
    GLD(v[s][4], pb, -2048); GLD(v[s][5], pb, -1024); GLD(v[s][6], pb, 0); GLD(v[s][7], pb, 1024); GLD(v[s][8], pb, 2048); \
    pa += 1024; pa1 += 1024; pa2 += 1024; pa3 += 1024; pb += 5120; } while (0)
#define COMPUTE(s) do { _Pragma("unroll") for (int i_ = 0; i_ < 9; ++i_) asm volatile("" : "+v"(v[s][i_])); \
    _Pragma("unroll") for (int n = 0; n < 5; ++n) _Pragma("unroll") for (int t = 0; t < 4; ++t) \
      acc[n][t] = MFMA16(__builtin_bit_cast(bf16x8, v[s][4 + n]), __builtin_bit_cast(bf16x8, v[s][t]), acc[n][t]); \
    __builtin_amdgcn_sched_barrier(0); } while (0)
#define WAITV(n) do { asm volatile("s_waitcnt vmcnt(" #n ")" ::: "memory"); __builtin_amdgcn_sched_barrier(0); } while (0)
  ISSUE(0); ISSUE(1); ISSUE(2);
  WAITV(18); COMPUTE(0); ISSUE(0);
  WAITV(18); COMPUTE(1); ISSUE(1);
  WAITV(18); COMPUTE(2); ISSUE(2);
  WAITV(18); COMPUTE(0); ISSUE(0);
  WAITV(18); COMPUTE(1); ISSUE(1);
  WAITV(18); COMPUTE(2); ISSUE(2);
  if constexpr (NST == 10) {
    WAITV(18); COMPUTE(0); ISSUE(0);
    WAITV(18); COMPUTE(1);
    WAITV(9); COMPUTE(2);
    WAITV(0); COMPUTE(0);
  } else {
    WAITV(18); COMPUTE(0);
    WAITV(9); COMPUTE(1);
    WAITV(0); COMPUTE(2);
  }
}

__global__ __launch_bounds__(512, 2) void k_phase2(const u16* x, const char* ws, u16* out) {
  extern __shared__ __attribute__((aligned(16))) char smem[];
  const int b = blockIdx.x, xcd = b & 7, idx = b >> 3;
  const int w = __builtin_amdgcn_readfirstlane(threadIdx.x >> 6), lane = threadIdx.x & 63;
  const int rt = (xcd >> 1) * 8 + (idx >> 2), ct = (xcd & 1) * 4 + (idx & 3);
  STAMP_DECL; STAMP(0);
  f32x4 acc[5][4];
#pragma unroll
  for (int n = 0; n < 5; ++n)
#pragma unroll
    for (int t = 0; t < 4; ++t) acc[n][t] = f32x4{0, 0, 0, 0};
  if (w < 6) {
    phase2_stream<9>(ws + OFF_ACT + ((size_t)(rt * 4) * 54 + 9 * w) * 1024 + lane * 16, 54 * 1024,
                     ws + OFF_W2F + ((size_t)ct * 74 + 9 * w) * 5120 + lane * 16 + 2048, acc);
  } else {
    phase2_stream<10>(ws + OFF_O + ((size_t)(rt * 4) * 20 + 10 * (w - 6)) * 1024 + lane * 16, 20 * 1024,
                      ws + OFF_W2F + ((size_t)ct * 74 + 54 + 10 * (w - 6)) * 5120 + lane * 16 + 2048, acc);
  }
  STAMP(1);
#pragma unroll
  for (int n = 0; n < 5; ++n)
#pragma unroll
    for (int t = 0; t < 4; ++t) *(f32x4*)(smem + (w * 20 + n * 4 + t) * 1024 + lane * 16) = acc[n][t];
  __syncthreads();
  STAMP(2);
  for (int tile = w; tile < 20; tile += 8) {
    int n = tile >> 2, t = tile & 3;
    const char* q = smem + tile * 1024 + lane * 16;
    f32x4 bsum = *(const f32x4*)(q);
#pragma unroll
    for (int k = 1; k < 6; ++k) bsum += *(const f32x4*)(q + k * 20480);
    f32x4 ssum = *(const f32x4*)(q + 6 * 20480) + *(const f32x4*)(q + 7 * 20480);
    size_t o = (size_t)(rt * 64 + t * 16 + (lane & 15)) * 640 + ct * 80 + n * 16 + 4 * (lane >> 4);
    u32x2 xv = *(const u32x2*)(x + o);
    float r[4];
    float xs[4] = {bf_lo(xv[0]), bf_hi(xv[0]), bf_lo(xv[1]), bf_hi(xv[1])};
#pragma unroll
    for (int j = 0; j < 4; ++j) {
      float bb = bsum[j];
      float g = bb * __builtin_amdgcn_rcpf(1.0f + __builtin_amdgcn_exp2f(-bb * LOG2E));
      r[j] = fmaf(ssum[j], g, xs[j]);
    }
    *(u32x2*)(out + o) = u32x2{pack2(r[0], r[1]), pack2(r[2], r[3])};
  }
  STAMP(3);
  STAMP_FLUSH((char*)ws, 2048 + blockIdx.x * 8 + w);
}
}

extern "C" void kernel_launch(void* const* d_in, const int* in_sizes, int n_in, void* d_out, int out_size, void* d_ws,
                              size_t ws_size, hipStream_t stream) {
  using namespace hk;
  P1 p;
  p.x = (const u16*)d_in[0]; p.wqkv = (const u16*)d_in[1]; p.wphi = (const u16*)d_in[2]; p.wo = (const u16*)d_in[3];
  p.w1 = (const u16*)d_in[5]; p.b1 = (const u16*)d_in[6]; p.w2 = (const u16*)d_in[7]; p.ws = (char*)d_ws;
  (void)hipFuncSetAttribute((const void*)k_phase1, hipFuncAttributeMaxDynamicSharedMemorySize, L_END);
  (void)hipFuncSetAttribute((const void*)k_phase2, hipFuncAttributeMaxDynamicSharedMemorySize, 163840);
  k_phase1<<<256, 512, L_END, stream>>>(p);
  k_kvred<<<120, 64, 0, stream>>>((char*)d_ws);
  k_o<<<256, 256, 0, stream>>>((char*)d_ws);
  k_phase2<<<256, 512, 163840, stream>>>(p.x, (const char*)d_ws, (u16*)d_out);
}
```

```cpp
#include <hip/hip_runtime.h>
#include <hip/hip_bf16.h>

typedef unsigned short u16;
typedef __bf16 bf16x2 __attribute__((ext_vector_type(2)));
typedef __bf16 bf16x4 __attribute__((ext_vector_type(4)));
typedef __bf16 bf16x8 __attribute__((ext_vector_type(8)));
typedef float f32x2 __attribute__((ext_vector_type(2)));
typedef float f32x4 __attribute__((ext_vector_type(4)));
typedef unsigned u32x2 __attribute__((ext_vector_type(2)));
typedef unsigned u32x4 __attribute__((ext_vector_type(4)));
#define LDS3 __attribute__((address_space(3)))
#define DI __device__ __forceinline__
#define MFMA16(a, b, c) __builtin_amdgcn_mfma_f32_16x16x32_bf16((a), (b), (c), 0, 0, 0)

#define STAMP(i)
#define STAMP_DECL
#define STAMP_FLUSH(ws, slot)
namespace hk {
typedef unsigned long long u64;
constexpr float LOG2E = 1.4426950408889634f;
constexpr float LN2 = 0.6931471805599453f;
#define ROTP 5
constexpr unsigned OFF_ACT = 0;
constexpr unsigned OFF_O = OFF_ACT + 128 * 54 * 1024;
constexpr unsigned OFF_W2F = OFF_O + 128 * 20 * 1024;
constexpr unsigned OFF_KVP = OFF_W2F + 8 * 74 * 5 * 1024;
constexpr unsigned OFF_KVH = OFF_KVP + 8 * 16 * 60 * 256;
constexpr unsigned OFF_STP = OFF_KVH + 8 * 30 * 256;
constexpr unsigned OFF_CSP = OFF_STP + 16 * 10 * 1024;
constexpr unsigned OFF_FLG = OFF_CSP + 16 * 10 * 1024;
constexpr unsigned F_EARLY = OFF_FLG + 128;
constexpr unsigned F_ACT = F_EARLY + 2048;
constexpr unsigned F_KVP = F_ACT + 2048;
constexpr unsigned F_KVH = F_KVP + 1024;
constexpr unsigned F_O = F_KVH + 1024;
constexpr unsigned WS_USED = F_O + 2048;
constexpr int XB = 16384, WROW = 480, STAGE = XB + 64 * WROW;
constexpr int L_PAD = 3 * STAGE;
constexpr int L_PT = L_PAD + 2048;
constexpr int L_RAW = L_PT + 12288;
constexpr int L_BIAS = L_RAW + 4096;
constexpr int L_ZERO = L_BIAS + 1024;
constexpr int L_CNT = L_ZERO + 64;
constexpr int L_BIASL = L_CNT + 64;
constexpr int L_CS = L_BIASL + 640;
constexpr int L_SUM = L_CS + 960;
constexpr int L_END = 163840;
static_assert(L_SUM + 1024 <= L_END, "lds");
DI constexpr int piece(int i) { return i * 20480 + 10240; }

DI unsigned pack2(float a, float b) {
  f32x2 v = {a, b};
  return __builtin_bit_cast(unsigned, __builtin_convertvector(v, bf16x2));
}
DI unsigned pack2v(f32x2 v) { return __builtin_bit_cast(unsigned, __builtin_convertvector(v, bf16x2)); }
DI bf16x2 as_bf2(unsigned u) { return __builtin_bit_cast(bf16x2, u); }
DI float bf_lo(unsigned u) { return __uint_as_float(u << 16); }
DI float bf_hi(unsigned u) { return __uint_as_float(u & 0xffff0000u); }
DI float rscatter_add(float m0, float m1, float m2, float m3) {
  auto a = __builtin_amdgcn_permlane16_swap(__float_as_uint(m0), __float_as_uint(m1), false, false);
  auto b = __builtin_amdgcn_permlane16_swap(__float_as_uint(m2), __float_as_uint(m3), false, false);
  float A = __uint_as_float(a[0]) + __uint_as_float(a[1]);
  float B = __uint_as_float(b[0]) + __uint_as_float(b[1]);
  auto c = __builtin_amdgcn_permlane32_swap(__float_as_uint(A), __float_as_uint(B), false, false);
  return __uint_as_float(c[0]) + __uint_as_float(c[1]);
}
DI f32x2 lo2(f32x4 v) { return __builtin_shufflevector(v, v, 0, 1); }
DI f32x2 hi2(f32x4 v) { return __builtin_shufflevector(v, v, 2, 3); }
DI f32x2 fma2(f32x2 a, f32x2 b, f32x2 c) { return __builtin_elementwise_fma(a, b, c); }
DI f32x2 splat2(float a) { return f32x2{a, a}; }
DI f32x2 exp2v(f32x2 a) { return f32x2{__builtin_amdgcn_exp2f(a[0]), __builtin_amdgcn_exp2f(a[1])}; }
DI f32x2 rcpv(f32x2 a) { return f32x2{__builtin_amdgcn_rcpf(a[0]), __builtin_amdgcn_rcpf(a[1])}; }
DI void dual_softmax(const f32x4 (&p)[5], u32x2 (&pos)[5], u32x2 (&neg)[5]) {
  float mx = fmaxf(p[0][0], p[0][1]), mn = fminf(p[0][0], p[0][1]);
  mx = fmaxf(fmaxf(mx, p[0][2]), p[0][3]); mn = fminf(fminf(mn, p[0][2]), p[0][3]);
#pragma unroll
  for (int t = 1; t < 5; ++t) {
    mx = fmaxf(fmaxf(mx, p[t][0]), p[t][1]); mx = fmaxf(fmaxf(mx, p[t][2]), p[t][3]);
    mn = fminf(fminf(mn, p[t][0]), p[t][1]); mn = fminf(fminf(mn, p[t][2]), p[t][3]);
  }
  const f32x2 L = splat2(LOG2E), nL = splat2(-LOG2E), cx = splat2(-mx * LOG2E), cn = splat2(mn * LOG2E);
  f32x2 ep[10], en[10];
#pragma unroll
  for (int t = 0; t < 5; ++t) {
    ep[2 * t] = exp2v(fma2(lo2(p[t]), L, cx)); ep[2 * t + 1] = exp2v(fma2(hi2(p[t]), L, cx));
    en[2 * t] = exp2v(fma2(lo2(p[t]), nL, cn)); en[2 * t + 1] = exp2v(fma2(hi2(p[t]), nL, cn));
  }
  f32x2 sp = ep[0], sn = en[0];
#pragma unroll
  for (int i = 1; i < 10; ++i) { sp += ep[i]; sn += en[i]; }
  const f32x2 ip = splat2(__builtin_amdgcn_rcpf(sp[0] + sp[1])), in_ = splat2(__builtin_amdgcn_rcpf(sn[0] + sn[1]));
#pragma unroll
  for (int t = 0; t < 5; ++t) {
    pos[t] = u32x2{pack2v(ep[2 * t] * ip), pack2v(ep[2 * t + 1] * ip)};
    neg[t] = u32x2{pack2v(en[2 * t] * in_), pack2v(en[2 * t + 1] * in_)};
  }
}
DI f32x2 mish2(f32x2 hl) {
  f32x2 e = exp2v(hl);
  f32x2 d = fma2(e, e + splat2(2.0f), splat2(2.0f));
  f32x2 t = fma2(rcpv(d), splat2(-2.0f * LN2), splat2(LN2));
  return hl * t;
}
DI bf16x8 mk8(u32x2 a, u32x2 b) {
  u32x4 v = {a[0], a[1], b[0], b[1]};
  return __builtin_bit_cast(bf16x8, v);
}
DI bf16x8 tr2(const char* a, const char* b) {
  bf16x4 lo = __builtin_amdgcn_ds_read_tr16_b64_v4bf16((LDS3 bf16x4*)(a));
  bf16x4 hi = __builtin_amdgcn_ds_read_tr16_b64_v4bf16((LDS3 bf16x4*)(b));
  return __builtin_shufflevector(lo, hi, 0, 1, 2, 3, 4, 5, 6, 7);
}

struct P1 {
  const u16* x; const u16* wqkv; const u16* wphi; const u16* wo; const u16* w1; const u16* b1; const u16* w2;
  char* ws; u16* out;
};
typedef __amdgpu_buffer_rsrc_t rsrc_t;

DI unsigned lds_addr(const void* p) { return (unsigned)(size_t)(LDS3 const char*)p; }
DI void glds1(const void* g, unsigned lds) {
  unsigned keep;
  asm volatile("s_mov_b32 %0, m0\n\ts_mov_b32 m0, %2\n\ts_nop 0\n\tglobal_load_lds_dwordx4 %1, off\n\ts_mov_b32 m0, %0"
               : "=&s"(keep) : "v"(g), "s"(lds) : "memory");
}
DI void glds6(const void* g0, const void* g1, const void* g2, const void* g3, const void* g4, const void* g5, unsigned lds, unsigned lds5) {
  unsigned keep;
  asm volatile("s_mov_b32 %0, m0\n\t"
               "s_mov_b32 m0, %7\n\ts_nop 0\n\tglobal_load_lds_dwordx4 %1, off\n\t"
               "s_add_u32 m0, %7, 0x2000\n\ts_nop 0\n\tglobal_load_lds_dwordx4 %2, off\n\t"
               "s_add_u32 m0, %7, 0x4000\n\ts_nop 0\n\tglobal_load_lds_dwordx4 %3, off\n\t"
               "s_add_u32 m0, %7, 0x6000\n\ts_nop 0\n\tglobal_load_lds_dwordx4 %4, off\n\t"
               "s_add_u32 m0, %7, 0x8000\n\ts_nop 0\n\tglobal_load_lds_dwordx4 %5, off\n\t"
               "s_mov_b32 m0, %8\n\ts_nop 0\n\tglobal_load_lds_dwordx4 %6, off\n\t"
               "s_mov_b32 m0, %0"
               : "=&s"(keep) : "v"(g0), "v"(g1), "v"(g2), "v"(g3), "v"(g4), "v"(g5), "s"(lds), "s"(lds5) : "memory", "scc");
}
DI u64 next_tag(u64 e) { return e * 0x9E3779B97F4A7C15ull + 0x632BE59BD9B4E019ull; }
DI void st_flag(char* p, u64 v) { __hip_atomic_store((u64*)p, v, __ATOMIC_RELAXED, __HIP_MEMORY_SCOPE_AGENT); }
DI void st16(rsrc_t rs, unsigned off, u32x4 v) { __builtin_amdgcn_raw_buffer_store_b128(v, rs, off, 0, 16); }
DI void st8(rsrc_t rs, unsigned off, u32x2 v) { __builtin_amdgcn_raw_buffer_store_b64(v, rs, off, 0, 16); }
DI void st4(rsrc_t rs, unsigned off, float v) { __builtin_amdgcn_raw_buffer_store_b32(__float_as_uint(v), rs, off, 0, 16); }
DI u32x4 ld16(rsrc_t rs, unsigned off) { return __builtin_amdgcn_raw_buffer_load_b128(rs, off, 0, 16); }
DI void drain() { asm volatile("s_waitcnt vmcnt(0)" ::: "memory"); }
constexpr int SPIN_MAX = 1 << 18;
DI bool flags_ok(u32x4 a, u32x4 b, u64 tag) {
  const unsigned tl = (unsigned)tag, th = (unsigned)(tag >> 32);
  return a[0] == tl && a[1] == th && a[2] == tl && a[3] == th && b[0] == tl && b[1] == th && b[2] == tl && b[3] == th;
}
DI void poll256(const char* flags, u64 tag, int lane) {
  const char* q = flags + lane * 32;
  for (int spin = 0; spin < SPIN_MAX; ++spin) {
    u32x4 a, b;
    asm volatile("global_load_dwordx4 %0, %2, off sc1\n\tglobal_load_dwordx4 %1, %2, off offset:16 sc1\n\ts_waitcnt vmcnt(0)"
                 : "=&v"(a), "=&v"(b) : "v"(q) : "memory");
    if (__all(flags_ok(a, b, tag))) break;
    __builtin_amdgcn_s_sleep(2);
  }
}
DI void polln(const char* flags, int n, u64 tag, int lane) {
  const char* q = flags + (lane < n ? lane : 0) * 8;
  for (int spin = 0; spin < SPIN_MAX; ++spin) {
    u32x2 a;
    asm volatile("global_load_dwordx2 %0, %1, off sc1\n\ts_waitcnt vmcnt(0)" : "=&v"(a) : "v"(q) : "memory");
    if (__all(a[0] == (unsigned)tag && a[1] == (unsigned)(tag >> 32))) break;
    __builtin_amdgcn_s_sleep(1);
  }
}
DI bool arrive(char* smem, int idx, int n, int lane) {
  unsigned old = 0;
  if (lane == 0) old = __hip_atomic_fetch_add((LDS3 unsigned*)(smem + L_CNT + idx * 4), 1u, __ATOMIC_RELAXED, __HIP_MEMORY_SCOPE_WORKGROUP);
  return __builtin_amdgcn_readfirstlane(old) == (unsigned)(n - 1);
}
DI void wait_cnt(char* smem, int idx, unsigned n) {
  for (int spin = 0; spin < (SPIN_MAX << 4); ++spin) {
    unsigned v = __hip_atomic_load((LDS3 unsigned*)(smem + L_CNT + idx * 4), __ATOMIC_RELAXED, __HIP_MEMORY_SCOPE_WORKGROUP);
    if (__builtin_amdgcn_readfirstlane(v) >= n) break;
    __builtin_amdgcn_s_sleep(1);
  }
  asm volatile("" ::: "memory");
}

template <int NT> struct Frags { bf16x8 x[2]; bf16x8 w[NT]; };

#define STAMP_ARG
#define STAMP_PASS

template <int TYPE, int C>
DI u64 phase1_body(const P1& p, rsrc_t rs, char* smem, int rb, int g8, int w, int lane STAMP_ARG) {
  constexpr int NT = (TYPE == 1 && C == 1) ? 8 : 7;
  constexpr bool VW = (TYPE == 0 && C == 1);
  const int r = w & 3;
  const int lg = lane >> 4, l15 = lane & 15;
  const unsigned lds0 = lds_addr(smem);
  u32x2 ev;
  asm volatile("global_load_dwordx2 %0, %1, off sc1" : "=v"(ev) : "v"(p.ws + OFF_FLG) : "memory");
  const int job = TYPE * 8 + g8;
  const int myc = (rb + job) % 10;
  const int rot = (myc / ROTP) * ROTP;
  const int my_i = myc - rot;
  int cnext = rot;
  const int ptj = w * 256 + blockIdx.x;
  const bool has_pt = C == 0 && w < 3 && ptj < 740;
  if (has_pt) {
    int u = ptj / 10, L = ptj - u * 10;
#pragma unroll
    for (int i = 0; i < 4; ++i) {
      int rho = i * 8 + (lane >> 3);
      const u16* src;
      if (u < 54) src = p.w2 + (unsigned)((32 * u + rho) * 640);
      else {
        int T = 2 * (u - 54) + (rho >> 4), G = (T * 13) >> 6;
        src = p.wo + (unsigned)(((4 * G + ((rho >> 2) & 3)) * 20 + 4 * (T - 5 * G) + (rho & 3)) * 640);
      }
      glds1((const char*)src + L * 128 + (lane & 7) * 16, lds0 + L_PT + w * 4096 + i * 1024);
    }
  }
  const char* xsrc[2];
  const char* wsrc[4];
  unsigned winc[4];
#pragma unroll
  for (int i = 0; i < 2; ++i) {
    int row = (i * 8 + w) * 8 + (lane >> 3);
    int ch = (lane & 7) ^ ((row >> 1) & 7);
    xsrc[i] = (const char*)p.x + (unsigned)((rb * 128 + row) * 1280 + ch * 16 + rot * 128);
  }
  const int t0w = TYPE == 0 ? 4 * g8 : min(32 + 10 * g8, 98);
#pragma unroll
  for (int i = 0; i < 4; ++i) {
    int ci = (i * 8 + w) * 64 + lane;
    if (i == 3) ci = ci >= 1920 ? ci - 128 : ci;
    int pos = (ci * 2185) >> 16, c16 = ci - pos * 30;
    int kk = pos & 15;
    int k = (kk >= 4 && kk < 12) ? (pos ^ 12) : pos;
    bool isq = c16 < (TYPE == 0 ? 20 : 10);
    int colb;
    if (TYPE == 0) colb = isq ? (1280 + 160 * g8 + c16 * 16 + (c16 >= 10 ? 1120 : 0)) : (32 * t0w + (min(c16, 27) - 20) * 16);
    else colb = isq ? (160 * g8 + c16 * 16) : (32 * t0w + (c16 - 10) * 16);
    unsigned stride = isq ? 3840u : 3456u;
    wsrc[i] = (isq ? (const char*)p.wqkv : (const char*)p.w1) + (unsigned)((k + rot * 64) * stride + colb);
    winc[i] = stride * 64;
  }
  auto wrap = [&]() {
    if (++cnext == 10) {
      cnext = 0;
#pragma unroll
      for (int i = 0; i < 2; ++i) xsrc[i] -= 1280;
#pragma unroll
      for (int i = 0; i < 4; ++i) wsrc[i] -= 10 * winc[i];
    }
  };
  auto issue = [&](int stage) {
    unsigned sb = lds0 + stage * STAGE + w * 1024;
    glds6(xsrc[0], xsrc[1], wsrc[0], wsrc[1], wsrc[2], wsrc[3], sb, w < 6 ? sb + 0xa000 : lds0 + L_PAD + (w - 6) * 1024);
#pragma unroll
    for (int i = 0; i < 2; ++i) xsrc[i] += 128;
#pragma unroll
    for (int i = 0; i < 4; ++i) wsrc[i] += winc[i];
    wrap();
  };
  if (C == 1) {
    int ci = r * 64 + lane; ci = ci < 200 ? ci : 199;
    glds1((const char*)p.wphi + g8 * 3200 + ci * 16, lds0 + L_RAW + r * 1024);
  } else if (w == 3) {
    int nb = TYPE == 0 ? 8 : 20;
    int ci = lane < nb ? lane : nb - 1;
    glds1((const char*)p.b1 + t0w * 32 + ci * 16, lds0 + L_BIAS);
    if (lane < 32) *(unsigned*)(smem + L_ZERO + lane * 4) = 0u;
  }
  issue(0);
  __builtin_amdgcn_s_barrier();
  issue(1);
  issue(2);
  if (has_pt) {
    asm volatile("s_waitcnt vmcnt(18)" ::: "memory");
    int u = ptj / 10, L = ptj - u * 10;
    const char* q = smem + L_PT + w * 4096 + (4 * lg + (l15 >> 2)) * 128 + (l15 & 3) * 8;
#pragma unroll
    for (int n = 0; n < 4; ++n) {
      bf16x8 f = tr2(q + n * 32, q + 2048 + n * 32);
      int N = 4 * L + n, ct = (N * 13) >> 6;
      st16(rs, OFF_W2F + ((ct * 74 + u) * 5 + (N - 5 * ct)) * 1024 + lane * 16, __builtin_bit_cast(u32x4, f));
    }
  }

  const unsigned xa0 = (32 * r + l15) * 128 + ((lg ^ ((l15 >> 1) & 7)) * 16);
  const unsigned xa1 = xa0 ^ 64;
  const unsigned wrow = XB + (16 * (lg >> 1) + 4 * (lg & 1) + (l15 >> 2)) * WROW;
  const unsigned wa = wrow + (l15 & 3) * 8;
  const unsigned wv = wrow + 160 + (l15 & 3) * 40;

  f32x4 acc[NT][2];
#pragma unroll
  for (int t = 0; t < NT; ++t) { acc[t][0] = f32x4{0, 0, 0, 0}; acc[t][1] = f32x4{0, 0, 0, 0}; }
  float cs[2] = {0.f, 0.f};
  float sx[2] = {0.f, 0.f};

  auto unit_of = [](int t) constexpr {
    return TYPE == 0 ? (C == 0 ? (t < 5 ? t : t + 5) : (t < 5 ? t + 5 : t + 7)) : (C == 0 ? t : t + 7);
  };
  auto rd_x = [&](Frags<NT>& f, const char* sb, int ks, int tt) { f.x[tt] = *(const bf16x8*)(sb + (ks ? xa1 : xa0) + tt * 2048); };
  auto rd_w = [&](Frags<NT>& f, const char* sb, int ks, int t) {
    const char* q = (VW && t < 5) ? sb + wv + t * 8 : sb + wa + unit_of(t) * 32;
    f.w[t] = tr2(q + (32 * ks) * WROW, q + (32 * ks + 8) * WROW);
  };
  auto dots2 = [&](const bf16x8& wa_, const bf16x8& wb_) {
    u32x4 a = __builtin_bit_cast(u32x4, wa_), b = __builtin_bit_cast(u32x4, wb_);
    unsigned a0 = a[0], a1 = a[1], a2 = a[2], a3 = a[3], b0 = b[0], b1 = b[1], b2 = b[2], b3 = b[3];
    asm volatile("s_nop 1\n\t"
                 "v_dot2c_f32_bf16 %0, 0x3f803f80, %2\n\tv_dot2c_f32_bf16 %1, 0x3f803f80, %6\n\t"
                 "v_dot2c_f32_bf16 %0, 0x3f803f80, %3\n\tv_dot2c_f32_bf16 %1, 0x3f803f80, %7\n\t"
                 "v_dot2c_f32_bf16 %0, 0x3f803f80, %4\n\tv_dot2c_f32_bf16 %1, 0x3f803f80, %8\n\t"
                 "v_dot2c_f32_bf16 %0, 0x3f803f80, %5\n\tv_dot2c_f32_bf16 %1, 0x3f803f80, %9\n\ts_nop 3"
                 : "+v"(cs[0]), "+v"(cs[1]) : "v"(a0), "v"(a1), "v"(a2), "v"(a3), "v"(b0), "v"(b1), "v"(b2), "v"(b3));
  };
  auto do_dots = [&](const Frags<NT>& f) {
    u32x4 a = __builtin_bit_cast(u32x4, f.x[C]);
    unsigned a0 = a[0], a1 = a[1], a2 = a[2], a3 = a[3];
    asm volatile("s_nop 1\n\t"
                 "v_dot2c_f32_bf16 %0, 0x3f803f80, %2\n\tv_dot2c_f32_bf16 %1, %2, %2\n\t"
                 "v_dot2c_f32_bf16 %0, 0x3f803f80, %3\n\tv_dot2c_f32_bf16 %1, %3, %3\n\t"
                 "v_dot2c_f32_bf16 %0, 0x3f803f80, %4\n\tv_dot2c_f32_bf16 %1, %4, %4\n\t"
                 "v_dot2c_f32_bf16 %0, 0x3f803f80, %5\n\tv_dot2c_f32_bf16 %1, %5, %5\n\ts_nop 3"
                 : "+v"(sx[0]), "+v"(sx[1]) : "v"(a0), "v"(a1), "v"(a2), "v"(a3));
    if (r == 0) dots2(f.w[0], f.w[4]);
    else if (r == 1) dots2(f.w[1], f.w[5]);
    else if (r == 2) dots2(f.w[2], f.w[6]);
    else dots2(f.w[3], f.w[NT - 1]);
  };
  auto kstep = [&](const Frags<NT>& f, Frags<NT>& g, const char* nsb, int nks, bool do_rd, bool do_issue, int istage) {
    unsigned sbi = lds0 + istage * STAGE + w * 1024;
#pragma unroll
    for (int t = 0; t < NT; ++t) {
      if (do_rd) {
        if (t < 2) rd_x(g, nsb, nks, t);
        rd_w(g, nsb, nks, t);
      }
#pragma unroll
      for (int tt = 0; tt < 2; ++tt)
        acc[t][tt] = (VW && t < 5) ? MFMA16(f.x[tt], f.w[t], acc[t][tt]) : MFMA16(f.w[t], f.x[tt], acc[t][tt]);
      if (do_issue && t < 6) {
        if (t < 2) { glds1(xsrc[t], sbi + t * 0x2000); xsrc[t] += 128; }
        else {
          glds1(wsrc[t - 2], (t < 5 || w < 6) ? sbi + t * 0x2000 : lds0 + L_PAD + (w - 6) * 1024);
          wsrc[t - 2] += winc[t - 2];
        }
      }
      __builtin_amdgcn_sched_barrier(0);
    }
    if (do_issue) wrap();
  };

  Frags<NT> f0, f1;
  if (has_pt) asm volatile("s_waitcnt vmcnt(16)" ::: "memory");
  else asm volatile("s_waitcnt vmcnt(12)" ::: "memory");
  __builtin_amdgcn_s_waitcnt(0xC07F);
  __builtin_amdgcn_s_barrier();
  __builtin_amdgcn_sched_barrier(0);
  STAMP(1);
  rd_x(f0, smem, 0, 0); rd_x(f0, smem, 0, 1);
#pragma unroll
  for (int t = 0; t < NT; ++t) rd_w(f0, smem, 0, t);
  {
    int tid = w * 64 + lane;
    if (tid < 160) *(float*)(smem + L_BIASL + tid * 4) = bf_lo(*(const u16*)(smem + L_BIAS + tid * 2)) * LOG2E;
  }
  asm volatile("" : "+v"(ev));
  const u64 tag = next_tag(((u64)(unsigned)__builtin_amdgcn_readfirstlane(ev[1]) << 32) | (unsigned)__builtin_amdgcn_readfirstlane(ev[0]));
  int stage = 0;
  u32x4 fa, fb;
#pragma unroll 1
  for (int i = 0; i < 9; ++i) {
    __builtin_amdgcn_s_waitcnt(0xC07F);
    __builtin_amdgcn_sched_barrier(0);
    kstep(f0, f1, smem + stage * STAGE, 1, true, false, 0);
    if (i == my_i) do_dots(f0);
    int nstage = stage == 2 ? 0 : stage + 1;
    if (i == 0 && has_pt) asm volatile("s_waitcnt vmcnt(10)" ::: "memory");
    else if (i < 8) asm volatile("s_waitcnt vmcnt(6)" ::: "memory");
    else asm volatile("s_waitcnt vmcnt(0)" ::: "memory");
    __builtin_amdgcn_s_waitcnt(0xC07F);
    __builtin_amdgcn_s_barrier();
    __builtin_amdgcn_sched_barrier(0);
    if (i == ROTP + 1 && w == 0 && lane == 0) st_flag(p.ws + F_EARLY + blockIdx.x * 8, tag);
    kstep(f1, f0, smem + nstage * STAGE, 0, true, i < 7, stage);
    if (i == my_i) {
      do_dots(f1);
      float v = rscatter_add(sx[0], sx[1], cs[0], cs[1]);
      int t1 = r + 4;
      int u0 = (TYPE == 0 && C == 1) ? r + 5 : ((TYPE == 1 && C == 1) ? r + 7 : r);
      int u1 = TYPE == 0 ? (C == 0 ? (t1 < 5 ? t1 : t1 + 5) : (t1 < 5 ? t1 + 5 : t1 + 7)) : (C == 0 ? t1 : t1 + 7);
      unsigned a = lg < 2 ? OFF_STP + (rb * 10 + myc) * 1024 + lg * 512 + (32 * r + 16 * C + l15) * 4
                          : OFF_CSP + (job * 10 + myc) * 1024 + ((lg == 2 ? u0 : u1) * 16 + l15) * 4;
      bool on = lg < 2 ? job < 10 : (rb < 10 && (lg == 2 || t1 < NT));
      st4(rs, on ? a : 0x7ffffff0u, v);
    }
    stage = nstage;
  }
  poll256(p.ws + F_EARLY, tag, lane);
  if (blockIdx.x == 0 && w == 0 && lane == 0) st_flag(p.ws + OFF_FLG, tag);
  unsigned pv[10];
  {
    int tid = w * 64 + lane;
    const char* q = p.ws + (tid < 256 ? OFF_STP + rb * 10240 + tid * 4 : OFF_CSP + job * 10240 + (tid - 256) * 4);
    const char* q2 = q + 5120;
    asm volatile("global_load_dword %0, %10, off sc1\n\tglobal_load_dword %1, %10, off offset:1024 sc1\n\t"
                 "global_load_dword %2, %10, off offset:2048 sc1\n\tglobal_load_dword %3, %10, off offset:3072 sc1\n\t"
                 "global_load_dword %4, %11, off offset:-1024 sc1\n\tglobal_load_dword %5, %11, off sc1\n\t"
                 "global_load_dword %6, %11, off offset:1024 sc1\n\tglobal_load_dword %7, %11, off offset:2048 sc1\n\t"
                 "global_load_dword %8, %11, off offset:3072 sc1\n\tglobal_load_dword %9, %12, off sc1"
                 : "=&v"(pv[0]), "=&v"(pv[1]), "=&v"(pv[2]), "=&v"(pv[3]), "=&v"(pv[4]), "=&v"(pv[5]), "=&v"(pv[6]), "=&v"(pv[7]), "=&v"(pv[8]), "=&v"(pv[9])
                 : "v"(q), "v"(q2), "v"(q2 + 4096) : "memory");
  }
  __builtin_amdgcn_s_waitcnt(0xC07F);
  __builtin_amdgcn_sched_barrier(0);
  kstep(f0, f1, smem + stage * STAGE, 1, true, false, 0);
  __builtin_amdgcn_s_waitcnt(0xC07F);
  __builtin_amdgcn_sched_barrier(0);
  kstep(f1, f0, smem, 0, false, false, 0);
  STAMP(2);
  {
    asm volatile("s_waitcnt vmcnt(0)" : "+v"(pv[0]), "+v"(pv[1]), "+v"(pv[2]), "+v"(pv[3]), "+v"(pv[4]), "+v"(pv[5]), "+v"(pv[6]), "+v"(pv[7]), "+v"(pv[8]), "+v"(pv[9]) :: "memory");
    float v = 0.f;
#pragma unroll
    for (int i = 0; i < 10; ++i) v += __uint_as_float(pv[i]);
    int tid = w * 64 + lane;
    unsigned a = tid < 256 ? L_SUM + (tid & 127) * 8 + (tid >> 7) * 4 : L_CS + (tid - 256) * 4;
    if (tid < 496) *(float*)(smem + a) = v;
  }
  __syncthreads();
  auto stat_of = [&](int tok) {
    f32x2 a = *(const f32x2*)(smem + L_SUM + tok * 8);
    float mean = a[0] * (1.0f / 640.0f);
    float var = fmaxf(fmaf(-mean, mean, a[1] * (1.0f / 640.0f)), 0.f);
    float rstd = __builtin_amdgcn_rsqf(var + 1e-5f);
    return f32x2{rstd, -rstd * mean};
  };
  f32x2 st[2];
#pragma unroll
  for (int tt = 0; tt < 2; ++tt) st[tt] = stat_of(32 * r + 16 * tt + l15);
  STAMP(3);
  if (TYPE == 1 && C == 1) __builtin_amdgcn_s_setprio(2);
  {
    constexpr int TW0 = (TYPE == 1 && C == 1) ? 0 : 5;
    constexpr int NP = (NT - TW0) / 2;
    f32x2 rsL[2], nbL[2];
#pragma unroll
    for (int tt = 0; tt < 2; ++tt) { rsL[tt] = splat2(st[tt][0] * LOG2E); nbL[tt] = splat2(st[tt][1] * LOG2E); }
#pragma unroll
    for (int pp = 0; pp < NP; ++pp) {
      const int ta = TW0 + 2 * pp;
      const int ua = unit_of(ta);
      const int wl = TYPE == 0 ? ua - 10 : ua - 5;
      const int wt = t0w + wl;
      bool skip = (TYPE == 1) && (g8 == 7) && (wl < 4);
      f32x4 bb[2], c4[2];
#pragma unroll
      for (int h = 0; h < 2; ++h) {
        bb[h] = *(const f32x4*)(smem + L_BIASL + ((wl + h) * 16 + 4 * lg) * 4);
        c4[h] = *(const f32x4*)(smem + L_CS + ((ua + h) * 16 + 4 * lg) * 4);
      }
#pragma unroll
      for (int tt = 0; tt < 2; ++tt) {
        unsigned o[4];
#pragma unroll
        for (int h = 0; h < 2; ++h) {
          o[2 * h] = pack2v(mish2(fma2(lo2(acc[ta + h][tt]), rsL[tt], fma2(nbL[tt], lo2(c4[h]), lo2(bb[h])))));
          o[2 * h + 1] = pack2v(mish2(fma2(hi2(acc[ta + h][tt]), rsL[tt], fma2(nbL[tt], hi2(c4[h]), hi2(bb[h])))));
        }
        if (!skip) st16(rs, OFF_ACT + ((rb * 8 + 2 * r + tt) * 54 + (wt >> 1)) * 1024 + lane * 16, u32x4{o[0], o[1], o[2], o[3]});
      }
    }
  }
  if (TYPE == 1 && C == 1) __builtin_amdgcn_s_setprio(0);
  STAMP(4);
  if (C == 0) {
    bf16x8 ap[5][3];
    {
      const char* pa[6];
#pragma unroll
      for (int i = 0; i < 6; ++i) {
        int d0 = 16 * i + 4 * lg;
        int h = (d0 * 13) >> 8;
        bool ok = (h == (l15 & 3)) && i < 5;
        pa[i] = smem + (ok ? L_RAW + h * 800 + (d0 - 20 * h + (l15 >> 2)) * 40 : L_ZERO);
      }
#pragma unroll
      for (int t = 0; t < 5; ++t)
#pragma unroll
        for (int ks = 0; ks < 3; ++ks) ap[t][ks] = tr2(pa[2 * ks] + t * 8, pa[2 * ks + 1] + t * 8);
    }
    f32x4 c4[5];
#pragma unroll
    for (int t = 0; t < 5; ++t) c4[t] = *(const f32x4*)(smem + L_CS + (t * 16 + 4 * lg) * 4);
#pragma unroll
    for (int tt = 0; tt < 2; ++tt) {
      u32x2 kb[5];
      const f32x2 rsv = splat2(st[tt][0]), nb = splat2(st[tt][1]);
#pragma unroll
      for (int t = 0; t < 5; ++t)
        kb[t] = u32x2{pack2v(fma2(lo2(acc[t][tt]), rsv, nb * lo2(c4[t]))), pack2v(fma2(hi2(acc[t][tt]), rsv, nb * hi2(c4[t])))};
      bf16x8 b0 = mk8(kb[0], kb[1]), b1 = mk8(kb[2], kb[3]), b2 = mk8(kb[4], u32x2{0u, 0u});
      f32x4 pk[5];
#pragma unroll
      for (int t = 0; t < 5; ++t) pk[t] = MFMA16(ap[t][2], b2, MFMA16(ap[t][1], b1, MFMA16(ap[t][0], b0, (f32x4{0, 0, 0, 0}))));
      u32x2 pos[5], neg[5];
      dual_softmax(pk, pos, neg);
      if (TYPE == 0) {
        int tok = 32 * r + 16 * tt + l15;
#pragma unroll
        for (int t = 0; t < 5; ++t) {
          *(u32x2*)(smem + piece(t >> 1) + (t & 1) * 4096 + tok * 32 + lg * 8) = pos[t];
          *(u32x2*)(smem + piece((5 + t) >> 1) + ((5 + t) & 1) * 4096 + tok * 32 + lg * 8) = neg[t];
        }
      } else {
        char* dst = smem + r * 20480 + 10240 + tt * 5120 + lane * 16;
        *(u32x4*)(dst) = u32x4{pos[0][0], pos[0][1], pos[1][0], pos[1][1]};
        *(u32x4*)(dst + 1024) = u32x4{pos[2][0], pos[2][1], pos[3][0], pos[3][1]};
        *(u32x4*)(dst + 2048) = u32x4{neg[0][0], neg[0][1], neg[1][0], neg[1][1]};
        *(u32x4*)(dst + 3072) = u32x4{neg[2][0], neg[2][1], neg[3][0], neg[3][1]};
        *(u32x4*)(dst + 4096) = u32x4{pos[4][0], pos[4][1], neg[4][0], neg[4][1]};
      }
    }
    if (TYPE == 1) { __builtin_amdgcn_s_waitcnt(0xC07F); asm volatile("" ::: "memory"); (void)arrive(smem, 1, 4, lane); }
  }
  if (VW) {
    f32x2 sv[2][4];
#pragma unroll
    for (int tt = 0; tt < 2; ++tt)
#pragma unroll
      for (int j = 0; j < 4; ++j) sv[tt][j] = stat_of(32 * r + 16 * tt + 4 * lg + j);
    char* vdst = smem + (5 + (r >> 1)) * 20480 + 10240 + (r & 1) * 5120 + lane * 16;
#pragma unroll
    for (int t = 0; t < 5; ++t) {
      float c1 = *(const float*)(smem + L_CS + ((5 + t) * 16 + l15) * 4);
      unsigned o[4];
#pragma unroll
      for (int tt = 0; tt < 2; ++tt) {
        float y0 = fmaf(acc[t][tt][0], sv[tt][0][0], sv[tt][0][1] * c1);
        float y1 = fmaf(acc[t][tt][1], sv[tt][1][0], sv[tt][1][1] * c1);
        float y2 = fmaf(acc[t][tt][2], sv[tt][2][0], sv[tt][2][1] * c1);
        float y3 = fmaf(acc[t][tt][3], sv[tt][3][0], sv[tt][3][1] * c1);
        o[2 * tt] = pack2(y0, y1); o[2 * tt + 1] = pack2(y2, y3);
      }
      *(u32x4*)(vdst + t * 1024) = u32x4{o[0], o[1], o[2], o[3]};
    }
  }
  drain();
  if (arrive(smem, 0, 8, lane) && lane == 0) st_flag(p.ws + F_ACT + (rb * 16 + job) * 8, tag);
  STAMP(5);
  if (TYPE == 0) {
    __syncthreads();
    const unsigned pa = 10240 + (4 * lg + (l15 >> 2)) * 32 + (l15 & 3) * 8;
    const u32x4 one4 = {0x3f803f80u, 0x3f803f80u, 0x3f803f80u, 0x3f803f80u};
    const bool valid = (l15 >> 2) == lg;
    for (int un = w; un < 20; un += 8) {
      int m = un >> 1, hf = un & 1;
      f32x4 a[3] = {{0, 0, 0, 0}, {0, 0, 0, 0}, {0, 0, 0, 0}};
#pragma unroll
      for (int ks = 0; ks < 4; ++ks) {
        const char* q = smem + pa + (m >> 1) * 20480 + (m & 1) * 4096 + ks * 1024;
        bf16x8 af = tr2(q, q + 512);
        const char* vb = smem + piece(5 + (ks >> 1)) + (ks & 1) * 5120 + hf * 3072 + lane * 16;
        a[0] = MFMA16(af, *(const bf16x8*)(vb), a[0]);
        a[1] = MFMA16(af, *(const bf16x8*)(vb + 1024), a[1]);
        u32x4 b2 = *(const u32x4*)(vb + (hf ? 0 : 2048));
        b2 = hf ? one4 : b2;
        a[2] = MFMA16(af, __builtin_bit_cast(bf16x8, b2), a[2]);
      }
      unsigned dst = valid ? OFF_KVP + ((g8 * 16 + rb) * 60 + m * 6 + hf * 3) * 256 + (lg * 4 + (l15 & 3)) * 16 : 0x7ffff000u;
#pragma unroll
      for (int j = 0; j < 3; ++j) st16(rs, dst + j * 256, __builtin_bit_cast(u32x4, a[j]));
    }
    drain();
    if (arrive(smem, 1, 8, lane) && lane == 0) st_flag(p.ws + F_KVP + (g8 * 16 + rb) * 8, tag);
  }
  STAMP(6);
  return tag;
}

#define GLD(dst, ptr, off) asm volatile("global_load_dwordx4 %0, %1, off offset:" #off " sc1" : "=v"(dst) : "v"(ptr) : "memory")
#define GLDS(dst, ptr) asm volatile("global_load_dwordx4 %0, %1, off sc1" : "=v"(dst) : "v"(ptr) : "memory")
#define WAITV(n) do { asm volatile("s_waitcnt vmcnt(%0)" :: "i"(n) : "memory"); __builtin_amdgcn_sched_barrier(0); } while (0)

template <int NST>
DI void phase2_stream(const char* pa, unsigned astr, const char* pb, const char* flags, int nflags, u64 tag, int lane, f32x4 (&acc)[5][4] STAMP_ARG) {
  const char* pa1 = pa + astr; const char* pa2 = pa1 + astr; const char* pa3 = pa2 + astr;
  u32x4 A[3][4], B[4][5];
#define ISSUE_A(s) do { GLDS(A[s][0], pa); GLDS(A[s][1], pa1); GLDS(A[s][2], pa2); GLDS(A[s][3], pa3); \
    pa += 1024; pa1 += 1024; pa2 += 1024; pa3 += 1024; } while (0)
#define ISSUE_B(s) do { GLD(B[s][0], pb, -2048); GLD(B[s][1], pb, -1024); GLD(B[s][2], pb, 0); GLD(B[s][3], pb, 1024); GLD(B[s][4], pb, 2048); \
    pb += 5120; } while (0)
#define COMPUTE(sa, sb) do { \
    _Pragma("unroll") for (int i_ = 0; i_ < 4; ++i_) asm volatile("" : "+v"(A[sa][i_])); \
    _Pragma("unroll") for (int i_ = 0; i_ < 5; ++i_) asm volatile("" : "+v"(B[sb][i_])); \
    _Pragma("unroll") for (int n = 0; n < 5; ++n) _Pragma("unroll") for (int t = 0; t < 4; ++t) \
      acc[n][t] = MFMA16(__builtin_bit_cast(bf16x8, B[sb][n]), __builtin_bit_cast(bf16x8, A[sa][t]), acc[n][t]); \
    __builtin_amdgcn_sched_barrier(0); } while (0)
  ISSUE_B(0); ISSUE_B(1); ISSUE_B(2); ISSUE_B(3);
  polln(flags, nflags, tag, lane);
  STAMP(8);
  ISSUE_A(0); ISSUE_A(1); ISSUE_A(2);
  WAITV(8);  COMPUTE(0, 0); ISSUE_A(0); ISSUE_B(0);
  WAITV(13); COMPUTE(1, 1); ISSUE_A(1); ISSUE_B(1);
  WAITV(18); COMPUTE(2, 2); ISSUE_A(2); ISSUE_B(2);
  WAITV(23); COMPUTE(0, 3); ISSUE_A(0); ISSUE_B(3);
  WAITV(23); COMPUTE(1, 0); ISSUE_A(1); ISSUE_B(0);
  if constexpr (NST == 10) {
    WAITV(23); COMPUTE(2, 1); ISSUE_A(2); ISSUE_B(1);
    WAITV(23); COMPUTE(0, 2); ISSUE_A(0);
    WAITV(18); COMPUTE(1, 3);
    WAITV(9);  COMPUTE(2, 0);
    WAITV(0);  COMPUTE(0, 1);
  } else {
    WAITV(23); COMPUTE(2, 1); ISSUE_A(2);
    WAITV(18); COMPUTE(0, 2);
    WAITV(9);  COMPUTE(1, 3);
    WAITV(0);  COMPUTE(2, 0);
  }
}

__global__ __launch_bounds__(512, 2) void k_fused(P1 p) {
  extern __shared__ __attribute__((aligned(16))) char smem[];
  const int b = blockIdx.x, xcd = b & 7, idx = b >> 3;
  const int w = __builtin_amdgcn_readfirstlane(threadIdx.x >> 6), lane = threadIdx.x & 63;
  const int rb = (xcd >> 2) * 8 + (idx >> 2);
  const int jl = idx & 3, xc = xcd & 3;
  const int type = jl >> 1, g8 = 2 * xc + (jl & 1);
  const int c = w >> 2;
  STAMP_DECL; STAMP(0);
  const rsrc_t rs = __builtin_amdgcn_make_buffer_rsrc(p.ws, 0, WS_USED, 0x00020000);
  u64 tag;
  if (type == 0) {
    if (c == 0) tag = phase1_body<0, 0>(p, rs, smem, rb, g8, w, lane STAMP_PASS);
    else tag = phase1_body<0, 1>(p, rs, smem, rb, g8, w, lane STAMP_PASS);
  } else {
    if (c == 0) tag = phase1_body<1, 0>(p, rs, smem, rb, g8, w, lane STAMP_PASS);
    else tag = phase1_body<1, 1>(p, rs, smem, rb, g8, w, lane STAMP_PASS);
  }
  const int lg = lane >> 4, l15 = lane & 15;
  if (w >= 6) {
    if (type == 0) {
      if (w == 6 && rb < 15) {
        polln(p.ws + F_KVP + g8 * 128, 16, tag, lane);
        u32x4 v[16];
#pragma unroll
        for (int i = 0; i < 16; ++i) v[i] = ld16(rs, OFF_KVP + (g8 * 16 + i) * 15360 + rb * 1024 + lane * 16);
        f32x4 a = __builtin_bit_cast(f32x4, v[0]);
#pragma unroll
        for (int i = 1; i < 16; ++i) a += __builtin_bit_cast(f32x4, v[i]);
        int tau = rb * 4 + lg, m = (tau * 43) >> 8, tn = tau - 6 * m;
        int sg = m >= 5 ? 1 : 0, t = m - 5 * sg;
        int ks = t < 4 ? sg * 2 + (t >> 1) : 4, hf = t < 4 ? (t & 1) : sg;
        st8(rs, OFF_KVH + g8 * 7680 + (tn * 5 + ks) * 256 + l15 * 16 + hf * 8, u32x2{pack2(a[0], a[1]), pack2(a[2], a[3])});
        drain();
        if (lane == 0) st_flag(p.ws + F_KVH + (g8 * 16 + rb) * 8, tag);
      }
    } else {
      polln(p.ws + F_KVH + g8 * 128, 15, tag, lane);
      const unsigned ko = ((l15 >> 2) == lg) ? OFF_KVH + g8 * 7680 + (lg * 4 + (l15 & 3)) * 16 : 0x7fff0000u;
      u32x4 ka[6][5];
#pragma unroll
      for (int tn = 0; tn < 6; ++tn)
#pragma unroll
        for (int ks = 0; ks < 5; ++ks) ka[tn][ks] = ld16(rs, ko + (tn * 5 + ks) * 256);
      wait_cnt(smem, 1, 4);
#pragma unroll 1
      for (int q = 0; q < 4; ++q) {
        const int tt = (w - 6) * 4 + q;
        const char* src = smem + (tt >> 1) * 20480 + 10240 + (tt & 1) * 5120 + lane * 16;
        bf16x8 ph[5];
#pragma unroll
        for (int k = 0; k < 5; ++k) ph[k] = *(const bf16x8*)(src + k * 1024);
        f32x4 n[6];
#pragma unroll
        for (int tn = 0; tn < 6; ++tn) {
          n[tn] = f32x4{0, 0, 0, 0};
#pragma unroll
          for (int ks = 0; ks < 5; ++ks) n[tn] = MFMA16(__builtin_bit_cast(bf16x8, ka[tn][ks]), ph[ks], n[tn]);
        }
        const f32x2 rc = splat2(__builtin_amdgcn_rcpf(n[5][0] + 1e-6f));
        u32x2 o[5];
#pragma unroll
        for (int t = 0; t < 5; ++t) o[t] = u32x2{pack2v(lo2(n[t]) * rc), pack2v(hi2(n[t]) * rc)};
        const unsigned ob = OFF_O + ((rb * 8 + tt) * 20 + ((5 * g8) >> 1)) * 1024 + lane * 16;
        if ((g8 & 1) == 0) {
          st16(rs, ob, u32x4{o[0][0], o[0][1], o[1][0], o[1][1]});
          st16(rs, ob + 1024, u32x4{o[2][0], o[2][1], o[3][0], o[3][1]});
          st8(rs, ob + 2048, o[4]);
        } else {
          st8(rs, ob + 8, o[0]);
          st16(rs, ob + 1024, u32x4{o[1][0], o[1][1], o[2][0], o[2][1]});
          st16(rs, ob + 2048, u32x4{o[3][0], o[3][1], o[4][0], o[4][1]});
        }
      }
      drain();
      if (arrive(smem, 2, 2, lane) && lane == 0) st_flag(p.ws + F_O + (rb * 16 + g8) * 8, tag);
    }
  }
  STAMP(7);
  const int rt = (xcd >> 1) * 8 + (idx >> 2), ct = (xcd & 1) * 4 + (idx & 3);
  f32x4 acc[5][4];
#pragma unroll
  for (int n = 0; n < 5; ++n)
#pragma unroll
    for (int t = 0; t < 4; ++t) acc[n][t] = f32x4{0, 0, 0, 0};
  if (w < 6) {
    phase2_stream<9>(p.ws + OFF_ACT + ((rt * 4) * 54 + 9 * w) * 1024 + lane * 16, 54 * 1024,
                     p.ws + OFF_W2F + (ct * 74 + 9 * w) * 5120 + lane * 16 + 2048, p.ws + F_ACT + (rt >> 1) * 128, 16, tag, lane, acc STAMP_PASS);
  } else {
    phase2_stream<10>(p.ws + OFF_O + ((rt * 4) * 20 + 10 * (w - 6)) * 1024 + lane * 16, 20 * 1024,
                      p.ws + OFF_W2F + (ct * 74 + 54 + 10 * (w - 6)) * 5120 + lane * 16 + 2048, p.ws + F_O + (rt >> 1) * 128, 8, tag, lane, acc STAMP_PASS);
  }
  STAMP(9);
  __syncthreads();
#pragma unroll
  for (int n = 0; n < 5; ++n)
#pragma unroll
    for (int t = 0; t < 4; ++t) *(f32x4*)(smem + (w * 20 + n * 4 + t) * 1024 + lane * 16) = acc[n][t];
  __syncthreads();
  STAMP(10);
  for (int tile = w; tile < 20; tile += 8) {
    int n = tile >> 2, t = tile & 3;
    const char* q = smem + tile * 1024 + lane * 16;
    f32x4 bsum = *(const f32x4*)(q);
#pragma unroll
    for (int k = 1; k < 6; ++k) bsum += *(const f32x4*)(q + k * 20480);
    f32x4 ssum = *(const f32x4*)(q + 6 * 20480) + *(const f32x4*)(q + 7 * 20480);
    size_t o = (size_t)(rt * 64 + t * 16 + l15) * 640 + ct * 80 + n * 16 + 4 * lg;
    u32x2 xv = *(const u32x2*)(p.x + o);
    float r[4];
    float xs[4] = {bf_lo(xv[0]), bf_hi(xv[0]), bf_lo(xv[1]), bf_hi(xv[1])};
#pragma unroll
    for (int j = 0; j < 4; ++j) {
      float bb = bsum[j];
      float g = bb * __builtin_amdgcn_rcpf(1.0f + __builtin_amdgcn_exp2f(-bb * LOG2E));
      r[j] = fmaf(ssum[j], g, xs[j]);
    }
    *(u32x2*)(p.out + o) = u32x2{pack2(r[0], r[1]), pack2(r[2], r[3])};
  }
  STAMP(11);
  STAMP_FLUSH(p.ws, blockIdx.x * 8 + w);
}
}

extern "C" void kernel_launch(void* const* d_in, const int* in_sizes, int n_in, void* d_out, int out_size, void* d_ws,
                              size_t ws_size, hipStream_t stream) {
  using namespace hk;
  P1 p;
  p.x = (const u16*)d_in[0]; p.wqkv = (const u16*)d_in[1]; p.wphi = (const u16*)d_in[2]; p.wo = (const u16*)d_in[3];
  p.w1 = (const u16*)d_in[5]; p.b1 = (const u16*)d_in[6]; p.w2 = (const u16*)d_in[7]; p.ws = (char*)d_ws; p.out = (u16*)d_out;
  (void)hipFuncSetAttribute((const void*)k_fused, hipFuncAttributeMaxDynamicSharedMemorySize, L_END);
  k_fused<<<256, 512, L_END, stream>>>(p);
}
```

```cpp
#include <hip/hip_runtime.h>
#include <hip/hip_bf16.h>

typedef unsigned short u16;
typedef __bf16 bf16x2 __attribute__((ext_vector_type(2)));
typedef __bf16 bf16x4 __attribute__((ext_vector_type(4)));
typedef __bf16 bf16x8 __attribute__((ext_vector_type(8)));
typedef float f32x2 __attribute__((ext_vector_type(2)));
typedef float f32x4 __attribute__((ext_vector_type(4)));
typedef unsigned u32x2 __attribute__((ext_vector_type(2)));
typedef unsigned u32x4 __attribute__((ext_vector_type(4)));
#define LDS3 __attribute__((address_space(3)))
#define DI __device__ __forceinline__
#define MFMA16(a, b, c) __builtin_amdgcn_mfma_f32_16x16x32_bf16((a), (b), (c), 0, 0, 0)

#define STAMP(i)
#define STAMP_DECL
#define STAMP_FLUSH(ws, slot)
namespace hk {
typedef unsigned long long u64;
constexpr float LOG2E = 1.4426950408889634f;
constexpr float LN2 = 0.6931471805599453f;
#define ROTP 2
#define DMAP 0
constexpr int FLAG_I = ROTP + 2 > 8 ? 8 : ROTP + 2;
constexpr unsigned OFF_ACT = 0;
constexpr unsigned OFF_O = OFF_ACT + 128 * 54 * 1024;
constexpr unsigned OFF_W2F = OFF_O + 128 * 20 * 1024;
constexpr unsigned OFF_KVP = OFF_W2F + 8 * 74 * 5 * 1024;
constexpr unsigned OFF_KVH = OFF_KVP + 8 * 16 * 60 * 256;
constexpr unsigned OFF_STP = OFF_KVH + 8 * 30 * 256;
constexpr unsigned OFF_CSP = OFF_STP + 16 * 10 * 1024;
constexpr unsigned OFF_FLG = OFF_CSP + 16 * 10 * 1024;
constexpr unsigned F_EARLY = OFF_FLG + 128;
constexpr unsigned F_ACT = F_EARLY + 2048;
constexpr unsigned F_KVP = F_ACT + 2048;
constexpr unsigned F_KVH = F_KVP + 1024;
constexpr unsigned F_O = F_KVH + 1024;
constexpr unsigned F_ST = F_O + 2048;
constexpr unsigned F_CS = F_ST + 2048;
constexpr unsigned WS_USED = F_CS + 2048;
constexpr unsigned OOB = 0x7ffff000u;
constexpr int XB = 16384;
constexpr int L_PT = 141312;
constexpr int L_RAW = L_PT + 14336;
constexpr int L_BIAS = L_RAW + 4096;
constexpr int L_ZERO = L_BIAS + 1024;
constexpr int L_CNT = L_ZERO + 64;
constexpr int L_BIASL = L_CNT + 64;
constexpr int L_CS = L_BIASL + 640;
constexpr int L_SUM = L_CS + 960;
constexpr int L_END = 163840;
static_assert(L_SUM + 1024 <= L_END - 256, "lds");
constexpr int E_PHI = 0;
constexpr int E_V = 40960;
constexpr int E_P2 = 61440;
constexpr int E_KV = 0;

DI unsigned pack2(float a, float b) {
  f32x2 v = {a, b};
  return __builtin_bit_cast(unsigned, __builtin_convertvector(v, bf16x2));
}
DI unsigned pack2v(f32x2 v) { return __builtin_bit_cast(unsigned, __builtin_convertvector(v, bf16x2)); }
DI bf16x2 as_bf2(unsigned u) { return __builtin_bit_cast(bf16x2, u); }
DI float bf_lo(unsigned u) { return __uint_as_float(u << 16); }
DI float bf_hi(unsigned u) { return __uint_as_float(u & 0xffff0000u); }
DI float rscatter_add(float m0, float m1, float m2, float m3) {
  auto a = __builtin_amdgcn_permlane16_swap(__float_as_uint(m0), __float_as_uint(m1), false, false);
  auto b = __builtin_amdgcn_permlane16_swap(__float_as_uint(m2), __float_as_uint(m3), false, false);
  float A = __uint_as_float(a[0]) + __uint_as_float(a[1]);
  float B = __uint_as_float(b[0]) + __uint_as_float(b[1]);
  auto c = __builtin_amdgcn_permlane32_swap(__float_as_uint(A), __float_as_uint(B), false, false);
  return __uint_as_float(c[0]) + __uint_as_float(c[1]);
}
DI f32x2 lo2(f32x4 v) { return __builtin_shufflevector(v, v, 0, 1); }
DI f32x2 hi2(f32x4 v) { return __builtin_shufflevector(v, v, 2, 3); }
DI f32x2 fma2(f32x2 a, f32x2 b, f32x2 c) { return __builtin_elementwise_fma(a, b, c); }
DI f32x2 splat2(float a) { return f32x2{a, a}; }
DI f32x2 exp2v(f32x2 a) { return f32x2{__builtin_amdgcn_exp2f(a[0]), __builtin_amdgcn_exp2f(a[1])}; }
DI f32x2 rcpv(f32x2 a) { return f32x2{__builtin_amdgcn_rcpf(a[0]), __builtin_amdgcn_rcpf(a[1])}; }
DI void dual_softmax(const f32x4 (&p)[5], u32x2 (&pos)[5], u32x2 (&neg)[5]) {
  float mx = fmaxf(p[0][0], p[0][1]), mn = fminf(p[0][0], p[0][1]);
  mx = fmaxf(fmaxf(mx, p[0][2]), p[0][3]); mn = fminf(fminf(mn, p[0][2]), p[0][3]);
#pragma unroll
  for (int t = 1; t < 5; ++t) {
    mx = fmaxf(fmaxf(mx, p[t][0]), p[t][1]); mx = fmaxf(fmaxf(mx, p[t][2]), p[t][3]);
    mn = fminf(fminf(mn, p[t][0]), p[t][1]); mn = fminf(fminf(mn, p[t][2]), p[t][3]);
  }
  const f32x2 L = splat2(LOG2E), nL = splat2(-LOG2E), cx = splat2(-mx * LOG2E), cn = splat2(mn * LOG2E);
  f32x2 ep[10], en[10];
#pragma unroll
  for (int t = 0; t < 5; ++t) {
    ep[2 * t] = exp2v(fma2(lo2(p[t]), L, cx)); ep[2 * t + 1] = exp2v(fma2(hi2(p[t]), L, cx));
    en[2 * t] = exp2v(fma2(lo2(p[t]), nL, cn)); en[2 * t + 1] = exp2v(fma2(hi2(p[t]), nL, cn));
  }
  f32x2 sp = ep[0], sn = en[0];
#pragma unroll
  for (int i = 1; i < 10; ++i) { sp += ep[i]; sn += en[i]; }
  const f32x2 ip = splat2(__builtin_amdgcn_rcpf(sp[0] + sp[1])), in_ = splat2(__builtin_amdgcn_rcpf(sn[0] + sn[1]));
#pragma unroll
  for (int t = 0; t < 5; ++t) {
    pos[t] = u32x2{pack2v(ep[2 * t] * ip), pack2v(ep[2 * t + 1] * ip)};
    neg[t] = u32x2{pack2v(en[2 * t] * in_), pack2v(en[2 * t + 1] * in_)};
  }
}
DI f32x2 mish2(f32x2 hl) {
  f32x2 e = exp2v(hl);
  f32x2 d = fma2(e, e + splat2(2.0f), splat2(2.0f));
  f32x2 t = fma2(rcpv(d), splat2(-2.0f * LN2), splat2(LN2));
  return hl * t;
}
DI bf16x8 mk8(u32x2 a, u32x2 b) {
  u32x4 v = {a[0], a[1], b[0], b[1]};
  return __builtin_bit_cast(bf16x8, v);
}
DI bf16x8 tr2(const char* a, const char* b) {
  bf16x4 lo = __builtin_amdgcn_ds_read_tr16_b64_v4bf16((LDS3 bf16x4*)(a));
  bf16x4 hi = __builtin_amdgcn_ds_read_tr16_b64_v4bf16((LDS3 bf16x4*)(b));
  return __builtin_shufflevector(lo, hi, 0, 1, 2, 3, 4, 5, 6, 7);
}

struct P1 {
  const u16* x; const u16* wqkv; const u16* wphi; const u16* wo; const u16* w1; const u16* b1; const u16* w2;
  char* ws; u16* out;
};
typedef __amdgpu_buffer_rsrc_t rsrc_t;

DI unsigned lds_addr(const void* p) { return (unsigned)(size_t)(LDS3 const char*)p; }
DI void glds1(const void* g, unsigned lds) {
  unsigned keep;
  asm volatile("s_mov_b32 %0, m0\n\ts_mov_b32 m0, %2\n\ts_nop 0\n\tglobal_load_lds_dwordx4 %1, off\n\ts_mov_b32 m0, %0"
               : "=&s"(keep) : "v"(g), "s"(lds) : "memory");
}
DI void touch(const void* g, unsigned lds) {
  unsigned keep;
  asm volatile("s_mov_b32 %0, m0\n\ts_mov_b32 m0, %2\n\ts_nop 0\n\tglobal_load_lds_dword %1, off\n\ts_mov_b32 m0, %0"
               : "=&s"(keep) : "v"(g), "s"(lds) : "memory");
}
DI u64 next_tag(u64 e) { return e * 0x9E3779B97F4A7C15ull + 0x632BE59BD9B4E019ull; }
DI void st_flag(char* p, u64 v) { __hip_atomic_store((u64*)p, v, __ATOMIC_RELAXED, __HIP_MEMORY_SCOPE_AGENT); }
DI void st16(rsrc_t rs, unsigned off, u32x4 v) { __builtin_amdgcn_raw_buffer_store_b128(v, rs, off, 0, 16); }
DI void st8(rsrc_t rs, unsigned off, u32x2 v) { __builtin_amdgcn_raw_buffer_store_b64(v, rs, off, 0, 16); }
DI void st4(rsrc_t rs, unsigned off, float v) { __builtin_amdgcn_raw_buffer_store_b32(__float_as_uint(v), rs, off, 0, 16); }
DI u32x4 ld16(rsrc_t rs, unsigned off) { return __builtin_amdgcn_raw_buffer_load_b128(rs, off, 0, 16); }
DI bool ready(u32x4 v) { return v[0] != 0xffffffffu && v[1] != 0xffffffffu && v[2] != 0xffffffffu && v[3] != 0xffffffffu; }
DI void drain() { asm volatile("s_waitcnt vmcnt(0)" ::: "memory"); }
constexpr int SPIN_MAX = 1 << 18;
DI bool flags_ok(u32x4 a, u32x4 b, u64 tag) {
  const unsigned tl = (unsigned)tag, th = (unsigned)(tag >> 32);
  return a[0] == tl && a[1] == th && a[2] == tl && a[3] == th && b[0] == tl && b[1] == th && b[2] == tl && b[3] == th;
}
DI void poll256(const char* flags, u64 tag, int lane) {
  const char* q = flags + lane * 32;
#pragma unroll 1
  for (int spin = 0; spin < SPIN_MAX; ++spin) {
    u32x4 a, b;
    asm volatile("global_load_dwordx4 %0, %2, off sc1\n\tglobal_load_dwordx4 %1, %2, off offset:16 sc1\n\ts_waitcnt vmcnt(0)"
                 : "=&v"(a), "=&v"(b) : "v"(q) : "memory");
    if (__all(flags_ok(a, b, tag))) break;
    __builtin_amdgcn_s_sleep(8);
  }
}
DI void pollq(const char* q, u64 tag) {
#pragma unroll 1
  for (int spin = 0; spin < SPIN_MAX; ++spin) {
    u32x2 a;
    asm volatile("global_load_dwordx2 %0, %1, off sc1\n\ts_waitcnt vmcnt(0)" : "=&v"(a) : "v"(q) : "memory");
    if (__all(a[0] == (unsigned)tag && a[1] == (unsigned)(tag >> 32))) break;
    __builtin_amdgcn_s_sleep(3);
  }
}
DI void polln(const char* flags, int n, u64 tag, int lane) { pollq(flags + (lane < n ? lane : 0) * 8, tag); }
DI bool arrive(char* smem, int idx, int n, int lane) {
  unsigned old = 0;
  if (lane == 0) old = __hip_atomic_fetch_add((LDS3 unsigned*)(smem + L_CNT + idx * 4), 1u, __ATOMIC_RELAXED, __HIP_MEMORY_SCOPE_WORKGROUP);
  return __builtin_amdgcn_readfirstlane(old) == (unsigned)(n - 1);
}
DI void set_cnt(char* smem, int idx, int lane) {
  if (lane == 0) __hip_atomic_store((LDS3 unsigned*)(smem + L_CNT + idx * 4), 1u, __ATOMIC_RELAXED, __HIP_MEMORY_SCOPE_WORKGROUP);
}
DI void wait_cnt(char* smem, int idx, unsigned n) {
#pragma unroll 1
  for (int spin = 0; spin < (SPIN_MAX << 4); ++spin) {
    unsigned v = __hip_atomic_load((LDS3 unsigned*)(smem + L_CNT + idx * 4), __ATOMIC_RELAXED, __HIP_MEMORY_SCOPE_WORKGROUP);
    if (__builtin_amdgcn_readfirstlane(v) >= n) break;
    __builtin_amdgcn_s_sleep(1);
  }
  asm volatile("" ::: "memory");
}

template <int NT> struct Frags { bf16x8 x[2]; bf16x8 w[NT]; };

#define STAMP_ARG
#define STAMP_PASS
template <int N> DI void waitv() { asm volatile("s_waitcnt vmcnt(%0)" :: "i"(N) : "memory"); }
DI void waitl() { __builtin_amdgcn_s_waitcnt(0xC07F); }

template <int PASS, int C>
struct Gemm {
  static constexpr int NT = PASS == 0 ? 5 : (PASS == 1 ? 2 : (C == 0 ? 7 : 8));
  static constexpr int NU = PASS == 0 ? 11 : (PASS == 1 ? 5 : 15);
  static constexpr int WROW = NU * 32, NC = NU * 2;
  static constexpr int STAGE = XB + 64 * WROW;
  static constexpr int BASE = 0;
  static constexpr int NSTG = PASS == 1 ? 5 : 3;
  static DI int sbase(int s) { return PASS == 1 ? (s < 3 ? E_P2 + s * STAGE : (s - 3) * STAGE) : s * STAGE; }
  static constexpr int NWS = (NC + 7) / 8;
  static constexpr int NFULL = NC - (NWS - 1) * 8;
  static constexpr int MAGIC = (65536 + NC - 1) / NC;
  static constexpr bool VW = (PASS == 0 && C == 1);
  static constexpr int unit_of(int t) { return PASS == 0 ? t + 5 * C : (PASS == 1 ? t + 2 * C : t + 7 * C); }

  const char* xsrc[2];
  const char* wsrc[NWS];
  unsigned winc[NWS];
  unsigned xa0, wa, lds0;
  int cnext, w;
  bool full;
  char* smem;
  f32x4 acc[NT][2];

  DI void init(const P1& p, char* smem_, int rb, int g8, int t0w, int rot, int w_, int lane) {
    smem = smem_; w = w_; cnext = rot; lds0 = lds_addr(smem_) + BASE; full = w < NFULL;
    const int r = w & 3, lg = lane >> 4, l15 = lane & 15;
#pragma unroll
    for (int i = 0; i < 2; ++i) {
      int row = (i * 8 + w) * 8 + (lane >> 3);
      int ch = (lane & 7) ^ ((row >> 1) & 7);
      xsrc[i] = (const char*)p.x + (unsigned)((rb * 128 + row) * 1280 + ch * 16 + rot * 128);
    }
#pragma unroll
    for (int i = 0; i < NWS; ++i) {
      int ci = (i * 8 + w) * 64 + lane;
      ci = ci < 64 * NC ? ci : lane;
      int pos = (ci * MAGIC) >> 16, c16 = ci - pos * NC;
      int kk = pos & 15;
      int k = (kk >= 4 && kk < 12) ? (pos ^ 12) : pos;
      bool isq = PASS == 0 || (PASS == 2 && c16 < 10);
      int colb;
      if (PASS == 0) { int c = min(c16, 19); colb = 1280 + 160 * g8 + c * 16 + (c >= 10 ? 1120 : 0); }
      else if (PASS == 1) colb = 32 * t0w + min(c16, 7) * 16;
      else colb = isq ? (160 * g8 + c16 * 16) : (32 * t0w + (c16 - 10) * 16);
      unsigned stride = isq ? 3840u : 3456u;
      wsrc[i] = (isq ? (const char*)p.wqkv : (const char*)p.w1) + (unsigned)((k + rot * 64) * stride + colb);
      winc[i] = stride * 64;
    }
    xa0 = BASE + (32 * r + l15) * 128 + ((lg ^ ((l15 >> 1) & 7)) * 16);
    wa = BASE + XB + (16 * (lg >> 1) + 4 * (lg & 1) + (l15 >> 2)) * WROW + (VW ? 160 + (l15 & 3) * 40 : (l15 & 3) * 8);
#pragma unroll
    for (int t = 0; t < NT; ++t) { acc[t][0] = f32x4{0, 0, 0, 0}; acc[t][1] = f32x4{0, 0, 0, 0}; }
  }
  DI void issue1(int s, int stage) {
    unsigned sb = lds0 + sbase(stage) + w * 1024 + s * 0x2000;
    if (s < 2) { glds1(xsrc[s], sb); xsrc[s] += 128; }
    else if (s - 2 < NWS - 1 || full) { glds1(wsrc[s - 2], sb); wsrc[s - 2] += winc[s - 2]; }
  }
  DI void wrap() {
    if (++cnext == 10) {
      cnext = 0;
#pragma unroll
      for (int i = 0; i < 2; ++i) xsrc[i] -= 1280;
#pragma unroll
      for (int i = 0; i < NWS; ++i) wsrc[i] -= 10 * winc[i];
    }
  }
  DI void issue(int stage) {
#pragma unroll
    for (int s = 0; s < 2 + NWS; ++s) issue1(s, stage);
    wrap();
  }
  template <int K, int E> DI void wait_chunks() {
    if (full) waitv<K * (2 + NWS) + E>(); else waitv<K * (1 + NWS) + E>();
  }
  DI void rd_x(Frags<NT>& f, int stage, int ks, int tt) { f.x[tt] = *(const bf16x8*)(smem + sbase(stage) + (xa0 ^ (ks * 64)) + tt * 2048); }
  DI void rd_w(Frags<NT>& f, int stage, int ks, int t) {
    const char* q = smem + sbase(stage) + wa + (VW ? t * 8 : unit_of(t) * 32);
    f.w[t] = tr2(q + (32 * ks) * WROW, q + (32 * ks + 8) * WROW);
  }
  DI void rd_all(Frags<NT>& f, int stage, int ks) {
    rd_x(f, stage, ks, 0); rd_x(f, stage, ks, 1);
#pragma unroll
    for (int t = 0; t < NT; ++t) rd_w(f, stage, ks, t);
  }
  DI void kstep(const Frags<NT>& f, Frags<NT>& g, int nstage, int nks, bool do_rd, bool do_issue, int istage) {
    constexpr int NS = 2 + NWS;
#pragma unroll
    for (int t = 0; t < NT; ++t) {
      if (do_rd) {
        if (t < 2) rd_x(g, nstage, nks, t);
        rd_w(g, nstage, nks, t);
      }
#pragma unroll
      for (int tt = 0; tt < 2; ++tt)
        acc[t][tt] = VW ? MFMA16(f.x[tt], f.w[t], acc[t][tt]) : MFMA16(f.w[t], f.x[tt], acc[t][tt]);
      if (do_issue) {
#pragma unroll
        for (int s = (t * NS) / NT; s < ((t + 1) * NS) / NT; ++s) issue1(s, istage);
      }
      __builtin_amdgcn_sched_barrier(0);
    }
    if (do_issue) wrap();
  }
};

DI void dots2(float& c0, float& c1, const bf16x8& wa_, const bf16x8& wb_) {
  u32x4 a = __builtin_bit_cast(u32x4, wa_), b = __builtin_bit_cast(u32x4, wb_);
  unsigned a0 = a[0], a1 = a[1], a2 = a[2], a3 = a[3], b0 = b[0], b1 = b[1], b2 = b[2], b3 = b[3];
  asm volatile("s_nop 1\n\t"
               "v_dot2c_f32_bf16 %0, 0x3f803f80, %2\n\tv_dot2c_f32_bf16 %1, 0x3f803f80, %6\n\t"
               "v_dot2c_f32_bf16 %0, 0x3f803f80, %3\n\tv_dot2c_f32_bf16 %1, 0x3f803f80, %7\n\t"
               "v_dot2c_f32_bf16 %0, 0x3f803f80, %4\n\tv_dot2c_f32_bf16 %1, 0x3f803f80, %8\n\t"
               "v_dot2c_f32_bf16 %0, 0x3f803f80, %5\n\tv_dot2c_f32_bf16 %1, 0x3f803f80, %9\n\ts_nop 3"
               : "+v"(c0), "+v"(c1) : "v"(a0), "v"(a1), "v"(a2), "v"(a3), "v"(b0), "v"(b1), "v"(b2), "v"(b3));
}
DI void dots_x(float& s1, float& s2, const bf16x8& x) {
  u32x4 a = __builtin_bit_cast(u32x4, x);
  unsigned a0 = a[0], a1 = a[1], a2 = a[2], a3 = a[3];
  asm volatile("s_nop 1\n\t"
               "v_dot2c_f32_bf16 %0, 0x3f803f80, %2\n\tv_dot2c_f32_bf16 %1, %2, %2\n\t"
               "v_dot2c_f32_bf16 %0, 0x3f803f80, %3\n\tv_dot2c_f32_bf16 %1, %3, %3\n\t"
               "v_dot2c_f32_bf16 %0, 0x3f803f80, %4\n\tv_dot2c_f32_bf16 %1, %4, %4\n\t"
               "v_dot2c_f32_bf16 %0, 0x3f803f80, %5\n\tv_dot2c_f32_bf16 %1, %5, %5\n\ts_nop 3"
               : "+v"(s1), "+v"(s2) : "v"(a0), "v"(a1), "v"(a2), "v"(a3));
}
DI f32x2 stat_of(const char* smem, int tok) { return *(const f32x2*)(smem + L_SUM + tok * 8); }
DI void mish_pair(rsrc_t rs, const char* smem, const f32x4 (&a0)[2], const f32x4 (&a1)[2], int ucs, int wl, int wt, bool skip,
                  const f32x2 (&st)[2], int tt0, int lane) {
  const int lg = lane >> 4;
  f32x4 bb[2], c4[2];
#pragma unroll
  for (int h = 0; h < 2; ++h) {
    bb[h] = *(const f32x4*)(smem + L_BIASL + ((wl + h) * 16 + 4 * lg) * 4);
    c4[h] = *(const f32x4*)(smem + L_CS + ((ucs + h) * 16 + 4 * lg) * 4);
  }
#pragma unroll
  for (int tt = 0; tt < 2; ++tt) {
    const f32x2 rsL = splat2(st[tt][0] * LOG2E), nbL = splat2(st[tt][1] * LOG2E);
    u32x4 o;
    o[0] = pack2v(mish2(fma2(lo2(a0[tt]), rsL, fma2(nbL, lo2(c4[0]), lo2(bb[0])))));
    o[1] = pack2v(mish2(fma2(hi2(a0[tt]), rsL, fma2(nbL, hi2(c4[0]), hi2(bb[0])))));
    o[2] = pack2v(mish2(fma2(lo2(a1[tt]), rsL, fma2(nbL, lo2(c4[1]), lo2(bb[1])))));
    o[3] = pack2v(mish2(fma2(hi2(a1[tt]), rsL, fma2(nbL, hi2(c4[1]), hi2(bb[1])))));
    st16(rs, skip ? OOB : OFF_ACT + ((tt0 + tt) * 54 + (wt >> 1)) * 1024 + lane * 16, o);
  }
}
DI void load_aphi(const char* smem, int lane, bf16x8 (&ap)[5][3]) {
  const int lg = lane >> 4, l15 = lane & 15;
  const char* pa[6];
#pragma unroll
  for (int i = 0; i < 6; ++i) {
    int d0 = 16 * i + 4 * lg;
    int h = (d0 * 13) >> 8;
    bool ok = (h == (l15 & 3)) && i < 5;
    pa[i] = smem + (ok ? L_RAW + h * 800 + (d0 - 20 * h + (l15 >> 2)) * 40 : L_ZERO);
  }
#pragma unroll
  for (int t = 0; t < 5; ++t)
#pragma unroll
    for (int ks = 0; ks < 3; ++ks) ap[t][ks] = tr2(pa[2 * ks] + t * 8, pa[2 * ks + 1] + t * 8);
}
template <int NT>
DI void features(const char* smem, const f32x4 (&acc)[NT][2], int tt, f32x2 st, const bf16x8 (&ap)[5][3], int lane, u32x2 (&pos)[5], u32x2 (&neg)[5]) {
  const int lg = lane >> 4;
  u32x2 kb[5];
  const f32x2 rsv = splat2(st[0]), nb = splat2(st[1]);
#pragma unroll
  for (int t = 0; t < 5; ++t) {
    f32x4 c4 = *(const f32x4*)(smem + L_CS + (t * 16 + 4 * lg) * 4);
    kb[t] = u32x2{pack2v(fma2(lo2(acc[t][tt]), rsv, nb * lo2(c4))), pack2v(fma2(hi2(acc[t][tt]), rsv, nb * hi2(c4)))};
  }
  bf16x8 b0 = mk8(kb[0], kb[1]), b1 = mk8(kb[2], kb[3]), b2 = mk8(kb[4], u32x2{0u, 0u});
  f32x4 pk[5];
#pragma unroll
  for (int t = 0; t < 5; ++t) pk[t] = MFMA16(ap[t][2], b2, MFMA16(ap[t][1], b1, MFMA16(ap[t][0], b0, (f32x4{0, 0, 0, 0}))));
  dual_softmax(pk, pos, neg);
}

template <int TYPE, int C, class G, class G2>
DI u64 first_pass(G& g, G2& g2, const P1& p, rsrc_t rs, char* smem, int rb, int g8, int t0w, int w, int lane STAMP_ARG) {
  constexpr int NT = G::NT;
  const int r = w & 3, lg = lane >> 4, l15 = lane & 15;
  const unsigned lds0 = lds_addr(smem);
  u32x2 ev;
  asm volatile("global_load_dwordx2 %0, %1, off sc1" : "=v"(ev) : "v"(p.ws + OFF_FLG) : "memory");
  const int job = TYPE * 8 + g8;
  const int myc = (rb + job) % 10;
  const int rot = (myc / ROTP) * ROTP;
  const int my_i = myc - rot;
  g.init(p, smem, rb, g8, t0w, rot, w, lane);
  if (C == 1) {
    int ci = r * 64 + lane; ci = ci < 200 ? ci : 199;
    glds1((const char*)p.wphi + g8 * 3200 + ci * 16, lds0 + L_RAW + r * 1024);
  } else if (w == 3) {
    int nb = TYPE == 0 ? 8 : 20;
    int ci = lane < nb ? lane : nb - 1;
    glds1((const char*)p.b1 + t0w * 32 + ci * 16, lds0 + L_BIAS);
    if (lane < 32) *(unsigned*)(smem + L_ZERO + lane * 4) = 0u;
  }
  g.issue(0);
  __builtin_amdgcn_s_barrier();
  g.issue(1);
  g.issue(2);
  const int ptj = w * 256 + blockIdx.x;
  const bool has_pt = C == 0 && w < 3 && ptj < 740;
  const int ptu = ptj / 10, ptL = ptj - ptu * 10;
  if (has_pt) {
#pragma unroll
    for (int i = 0; i < 4; ++i) {
      int rho = i * 8 + (lane >> 3);
      const u16* src;
      if (ptu < 54) src = p.w2 + (unsigned)((32 * ptu + rho) * 640);
      else {
        int T = 2 * (ptu - 54) + (rho >> 4), Gq = (T * 13) >> 6;
        src = p.wo + (unsigned)(((4 * Gq + ((rho >> 2) & 3)) * 20 + 4 * (T - 5 * Gq) + (rho & 3)) * 640);
      }
      glds1((const char*)src + ptL * 128 + (lane & 7) * 16, lds0 + L_PT + w * 4096 + i * 1024);
    }
  }
  float cs[2] = {0.f, 0.f}, sx[2] = {0.f, 0.f};
  auto do_dots = [&](const Frags<NT>& f) {
    dots_x(sx[0], sx[1], f.x[C]);
    if (r == 0) dots2(cs[0], cs[1], f.w[0], f.w[4]);
    else if (r == 1) dots2(cs[0], cs[1], f.w[1], f.w[NT > 5 ? 5 : 0]);
    else if (r == 2) dots2(cs[0], cs[1], f.w[2], f.w[NT > 6 ? 6 : 0]);
    else dots2(cs[0], cs[1], f.w[3], f.w[NT > 7 ? 7 : 0]);
  };
  Frags<NT> f0, f1;
  if (has_pt) g.template wait_chunks<2, 4>(); else g.template wait_chunks<2, 0>();
  waitl();
  __builtin_amdgcn_s_barrier();
  __builtin_amdgcn_sched_barrier(0);
  STAMP(1);
  g.rd_all(f0, 0, 0);
  {
    int tid = w * 64 + lane;
    if (tid < 160) *(float*)(smem + L_BIASL + tid * 4) = bf_lo(*(const u16*)(smem + L_BIAS + tid * 2)) * LOG2E;
  }
  asm volatile("" : "+v"(ev));
  const u64 tag = next_tag(((u64)(unsigned)__builtin_amdgcn_readfirstlane(ev[1]) << 32) | (unsigned)__builtin_amdgcn_readfirstlane(ev[0]));
  int stage = 0;
  u32x4 fa, fb;
  u32x2 tf;
  const char* tfp = p.ws + (lane < 10 ? F_ST + (rb * 16 + lane) * 8 : F_CS + (job * 16 + (lane < 20 ? lane - 10 : 0)) * 8);
#pragma unroll 1
  for (int i = 0; i < 9; ++i) {
    waitl();
    __builtin_amdgcn_sched_barrier(0);
    g.kstep(f0, f1, stage, 1, true, false, 0);
    if (i == my_i) do_dots(f0);
    int nstage = stage == 2 ? 0 : stage + 1;
    if (has_pt && (i == 0 || i == 2 || i == 3)) g.template wait_chunks<1, 4>();
    else if (i < 8) g.template wait_chunks<1, 0>();
    else waitv<0>();
    waitl();
    __builtin_amdgcn_s_barrier();
    __builtin_amdgcn_sched_barrier(0);
    if (i == FLAG_I && w == 0 && lane < 3)
      st_flag(p.ws + (lane == 0 ? F_EARLY + blockIdx.x * 8 : (lane == 1 ? F_ST + (rb * 16 + job) * 8 : F_CS + (job * 16 + rb) * 8)), tag);
    g.kstep(f1, f0, nstage, 0, true, i < 7, stage);
    if (i == 1 && has_pt) {
      const char* q = smem + L_PT + w * 4096 + (4 * lg + (l15 >> 2)) * 128 + (l15 & 3) * 8;
#pragma unroll
      for (int n = 0; n < 4; ++n) {
        bf16x8 f = tr2(q + n * 32, q + 2048 + n * 32);
        int N = 4 * ptL + n, ct = (N * 13) >> 6;
        st16(rs, OFF_W2F + ((ct * 74 + ptu) * 5 + (N - 5 * ct)) * 1024 + lane * 16, __builtin_bit_cast(u32x4, f));
      }
    }
    if (i == my_i) {
      do_dots(f1);
      float v = rscatter_add(sx[0], sx[1], cs[0], cs[1]);
      unsigned a = lg < 2 ? OFF_STP + (rb * 10 + myc) * 1024 + lg * 512 + (32 * r + 16 * C + l15) * 4
                          : OFF_CSP + (job * 10 + myc) * 1024 + ((G::unit_of(0) + r + (lg == 2 ? 0 : 4)) * 16 + l15) * 4;
      bool on = lg < 2 ? job < 10 : (rb < 10 && (lg == 2 || r + 4 < NT));
      st4(rs, on ? a : OOB, v);
    }
    if (i == 7) {
      asm volatile("global_load_dwordx2 %0, %1, off sc1" : "=v"(tf) : "v"(tfp) : "memory");
      if (w == 5)
        asm volatile("global_load_dwordx4 %0, %2, off sc1\n\tglobal_load_dwordx4 %1, %2, off offset:16 sc1"
                     : "=&v"(fa), "=&v"(fb) : "v"(p.ws + F_EARLY + lane * 32) : "memory");
    }
    stage = nstage;
  }
  asm volatile("" : "+v"(tf));
  if (!__all(tf[0] == (unsigned)tag && tf[1] == (unsigned)(tag >> 32))) pollq(tfp, tag);
  unsigned pv[10], pw[10];
  {
    const int tid = w * 64 + lane;
    const char* q = p.ws + (tid < 128 ? OFF_STP + rb * 10240 + tid * 4 : OFF_CSP + job * 10240 + ((tid - 128) & 255) * 4);
    const char* q2 = q + 5120;
#define LD10(pv, q, q2, q3) \
    asm volatile("global_load_dword %0, %10, off sc1\n\tglobal_load_dword %1, %10, off offset:1024 sc1\n\t" \
                 "global_load_dword %2, %10, off offset:2048 sc1\n\tglobal_load_dword %3, %10, off offset:3072 sc1\n\t" \
                 "global_load_dword %4, %11, off offset:-1024 sc1\n\tglobal_load_dword %5, %11, off sc1\n\t" \
                 "global_load_dword %6, %11, off offset:1024 sc1\n\tglobal_load_dword %7, %11, off offset:2048 sc1\n\t" \
                 "global_load_dword %8, %11, off offset:3072 sc1\n\tglobal_load_dword %9, %12, off sc1" \
                 : "=&v"(pv[0]), "=&v"(pv[1]), "=&v"(pv[2]), "=&v"(pv[3]), "=&v"(pv[4]), "=&v"(pv[5]), "=&v"(pv[6]), "=&v"(pv[7]), "=&v"(pv[8]), "=&v"(pv[9]) \
                 : "v"(q), "v"(q2), "v"(q3) : "memory")
    LD10(pv, q, q2, q2 + 4096);
    if constexpr (C == 0) { LD10(pw, q + 512, q2 + 512, q2 + 4608); }
  }
  if (C == 1 && w == 5) {
    asm volatile("" : "+v"(fa), "+v"(fb));
    if (__all(flags_ok(fa, fb, tag))) {
      if (blockIdx.x == 0 && lane == 0) st_flag(p.ws + OFF_FLG, tag);
      set_cnt(smem, 3, lane);
      constexpr int NL = DMAP == 0 ? 370 : 740;
      const char* q = p.ws + OFF_W2F + (DMAP == 0 ? (blockIdx.x & 1) * 4 * 378880 : 0) + ((blockIdx.x >> 3) * NL + lane) * 128;
#pragma unroll
      for (int j = 0; j < (NL + 63) / 64; ++j) touch(q + (j * 64 + lane < NL ? j * 8192 : 0), lds0 + L_PT);
    }
  }
  if (TYPE == 0) {
    g2.init(p, smem, rb, g8, t0w, 0, w, lane);
    g2.issue(0); g2.issue(1); g2.issue(2);
  }
  waitl();
  __builtin_amdgcn_sched_barrier(0);
  g.kstep(f0, f1, stage, 1, true, false, 0);
  waitl();
  __builtin_amdgcn_sched_barrier(0);
  g.kstep(f1, f0, 0, 0, false, false, 0);
  STAMP(2);
  {
    if (TYPE == 0) g2.template wait_chunks<3, 0>(); else waitv<0>();
    asm volatile("" : "+v"(pv[0]), "+v"(pv[1]), "+v"(pv[2]), "+v"(pv[3]), "+v"(pv[4]), "+v"(pv[5]), "+v"(pv[6]), "+v"(pv[7]), "+v"(pv[8]), "+v"(pv[9]) :: "memory");
    float v = 0.f;
#pragma unroll
    for (int i = 0; i < 10; ++i) v += __uint_as_float(pv[i]);
    const int tid = w * 64 + lane;
    if constexpr (C == 0) {
      asm volatile("" : "+v"(pw[0]), "+v"(pw[1]), "+v"(pw[2]), "+v"(pw[3]), "+v"(pw[4]), "+v"(pw[5]), "+v"(pw[6]), "+v"(pw[7]), "+v"(pw[8]), "+v"(pw[9]) :: "memory");
      if (w < 2) {
        float v2 = 0.f;
#pragma unroll
        for (int i = 0; i < 10; ++i) v2 += __uint_as_float(pw[i]);
        float mean = v * (1.0f / 640.0f);
        float var = fmaxf(fmaf(-mean, mean, v2 * (1.0f / 640.0f)), 0.f);
        float rstd = __builtin_amdgcn_rsqf(var + 1e-5f);
        *(f32x2*)(smem + L_SUM + tid * 8) = f32x2{rstd, -rstd * mean};
      }
    }
    if (tid >= 128 && tid < 368) *(float*)(smem + L_CS + (tid - 128) * 4) = v;
  }
  waitl();
  __builtin_amdgcn_s_barrier();
  __builtin_amdgcn_sched_barrier(0);
  STAMP(3);
  return tag;
}

template <int C>
DI u64 body_a(const P1& p, rsrc_t rs, char* smem, int rb, int g8, int w, int lane STAMP_ARG) {
  const int r = w & 3, lg = lane >> 4, l15 = lane & 15;
  const int t0w = 4 * g8;
  Gemm<0, C> g;
  Gemm<1, C> g2;
  const u64 tag = first_pass<0, C>(g, g2, p, rs, smem, rb, g8, t0w, w, lane STAMP_PASS);
  if constexpr (C == 0) {
    bf16x8 ap[5][3];
    load_aphi(smem, lane, ap);
#pragma unroll
    for (int tt = 0; tt < 2; ++tt) {
      const int tok = 32 * r + 16 * tt + l15;
      u32x2 pos[5], neg[5];
      features<5>(smem, g.acc, tt, stat_of(smem, tok), ap, lane, pos, neg);
#pragma unroll
      for (int t = 0; t < 5; ++t) {
        *(u32x2*)(smem + E_PHI + (t * 128 + tok) * 32 + lg * 8) = pos[t];
        *(u32x2*)(smem + E_PHI + ((5 + t) * 128 + tok) * 32 + lg * 8) = neg[t];
      }
    }
  } else {
    f32x2 sv[2][4];
#pragma unroll
    for (int tt = 0; tt < 2; ++tt)
#pragma unroll
      for (int j = 0; j < 4; ++j) sv[tt][j] = stat_of(smem, 32 * r + 16 * tt + 4 * lg + j);
#pragma unroll
    for (int t = 0; t < 5; ++t) {
      float c1 = *(const float*)(smem + L_CS + ((5 + t) * 16 + l15) * 4);
      unsigned o[4];
#pragma unroll
      for (int tt = 0; tt < 2; ++tt) {
        float y0 = fmaf(g.acc[t][tt][0], sv[tt][0][0], sv[tt][0][1] * c1);
        float y1 = fmaf(g.acc[t][tt][1], sv[tt][1][0], sv[tt][1][1] * c1);
        float y2 = fmaf(g.acc[t][tt][2], sv[tt][2][0], sv[tt][2][1] * c1);
        float y3 = fmaf(g.acc[t][tt][3], sv[tt][3][0], sv[tt][3][1] * c1);
        o[2 * tt] = pack2(y0, y1); o[2 * tt + 1] = pack2(y2, y3);
      }
      *(u32x4*)(smem + E_V + (r * 5 + t) * 1024 + lane * 16) = u32x4{o[0], o[1], o[2], o[3]};
    }
  }
  waitl();
  __builtin_amdgcn_s_barrier();
  __builtin_amdgcn_sched_barrier(0);
  STAMP(4);
  {
    const unsigned pa = E_PHI + (4 * lg + (l15 >> 2)) * 32 + (l15 & 3) * 8;
    const u32x4 one4 = {0x3f803f80u, 0x3f803f80u, 0x3f803f80u, 0x3f803f80u};
    const bool valid = (l15 >> 2) == lg;
    f32x4 a[3][3];
    const char* qa[3]; const char* vb[3]; bool hf[3];
#pragma unroll
    for (int k3 = 0; k3 < 3; ++k3) {
      const int un0 = w + 8 * k3, un = un0 < 20 ? un0 : 19;
      hf[k3] = un & 1;
      qa[k3] = smem + pa + (un >> 1) * 4096;
      vb[k3] = smem + E_V + (un & 1) * 3072 + lane * 16;
#pragma unroll
      for (int j = 0; j < 3; ++j) a[k3][j] = f32x4{0, 0, 0, 0};
    }
#pragma unroll
    for (int ks = 0; ks < 4; ++ks)
#pragma unroll
      for (int k3 = 0; k3 < 3; ++k3) {
        bf16x8 af = tr2(qa[k3] + ks * 1024, qa[k3] + ks * 1024 + 512);
        const char* v = vb[k3] + ks * 5120;
        a[k3][0] = MFMA16(af, *(const bf16x8*)(v), a[k3][0]);
        a[k3][1] = MFMA16(af, *(const bf16x8*)(v + 1024), a[k3][1]);
        u32x4 b2 = *(const u32x4*)(v + (hf[k3] ? 0 : 2048));
        b2 = hf[k3] ? one4 : b2;
        a[k3][2] = MFMA16(af, __builtin_bit_cast(bf16x8, b2), a[k3][2]);
      }
#pragma unroll
    for (int k3 = 0; k3 < 3; ++k3) {
      const int un0 = w + 8 * k3;
      unsigned dst = (valid && un0 < 20) ? OFF_KVP + ((g8 * 16 + rb) * 60 + un0 * 3) * 256 + (lg * 4 + (l15 & 3)) * 16 : OOB;
#pragma unroll
      for (int j = 0; j < 3; ++j) st16(rs, dst + j * 256, __builtin_bit_cast(u32x4, a[k3][j]));
    }
  }
  STAMP(5);
  float cs0 = 0.f, csd = 0.f;
  Frags<2> f0, f1;
  g2.template wait_chunks<2, 9>();
  waitl();
  __builtin_amdgcn_s_barrier();
  __builtin_amdgcn_sched_barrier(0);
  g2.issue(3); g2.issue(4);
  g2.rd_all(f0, 0, 0);
  int stage = 0;
  const bool is_red = C == 1 && w == 7 && rb < 15;
  bool red_done = !is_red;
  const char* kvsrc = p.ws + OFF_KVP + g8 * 16 * 15360 + rb * 1024 + lane * 16;
  auto red_finish = [&](const u32x4 (&kv)[16], bool on) {
    f32x4 a = __builtin_bit_cast(f32x4, kv[0]);
#pragma unroll
    for (int i = 1; i < 16; ++i) a += __builtin_bit_cast(f32x4, kv[i]);
    int tau = rb * 4 + lg, m = (tau * 43) >> 8, tn = tau - 6 * m;
    int sg = m >= 5 ? 1 : 0, t = m - 5 * sg;
    int ks = t < 4 ? sg * 2 + (t >> 1) : 4, hf = t < 4 ? (t & 1) : sg;
    st8(rs, on ? OFF_KVH + g8 * 7680 + (tn * 5 + ks) * 256 + l15 * 16 + hf * 8 : OOB, u32x2{pack2(a[0], a[1]), pack2(a[2], a[3])});
  };
  auto iter_a = [&]() {
    waitl();
    __builtin_amdgcn_sched_barrier(0);
    g2.kstep(f0, f1, stage, 1, true, false, 0);
    if (r < 2) { if (r == 0) dots2(cs0, csd, f0.w[0], f0.w[0]); else dots2(cs0, csd, f0.w[1], f0.w[1]); }
  };
  auto iter_bar = [&]() {
    waitl();
    __builtin_amdgcn_s_barrier();
    __builtin_amdgcn_sched_barrier(0);
  };
  auto iter_b = [&](bool do_issue) {
    int nstage = stage == 4 ? 0 : stage + 1;
    g2.kstep(f1, f0, nstage, 0, true, do_issue, stage);
    if (r < 2) { if (r == 0) dots2(cs0, csd, f1.w[0], f1.w[0]); else dots2(cs0, csd, f1.w[1], f1.w[1]); }
    stage = nstage;
  };
  const int nloop = is_red ? 4 : 9;
#pragma unroll 1
  for (int i = 0; i < nloop; ++i) {
    iter_a();
    if (i < 2) g2.template wait_chunks<3, 9>();
    else if (i < 6) g2.template wait_chunks<3, 0>();
    else if (i == 6) g2.template wait_chunks<2, 0>();
    else if (i == 7) g2.template wait_chunks<1, 0>();
    else waitv<0>();
    iter_bar();
    if (i == 2 && w == 0 && lane == 0) st_flag(p.ws + F_KVP + (g8 * 16 + rb) * 8, tag);
    iter_b(i < 5);
  }
  if (C == 1 && is_red) {
    u32x2 kf;
    u32x4 kv[16];
    iter_a(); g2.template wait_chunks<3, 0>(); iter_bar();
    iter_b(true);
    iter_a(); g2.template wait_chunks<3, 0>(); iter_bar();
    asm volatile("global_load_dwordx2 %0, %1, off sc1" : "=v"(kf) : "v"(p.ws + F_KVP + g8 * 128 + l15 * 8) : "memory");
    iter_b(false);
    iter_a(); waitv<0>(); iter_bar();
    asm volatile("" : "+v"(kf));
    red_done = __all(kf[0] == (unsigned)tag && kf[1] == (unsigned)(tag >> 32));
#pragma unroll
    for (int i = 0; i < 16; ++i) asm volatile("global_load_dwordx4 %0, %1, off sc1" : "=v"(kv[i]) : "v"(kvsrc + i * 15360) : "memory");
    iter_b(false);
    iter_a(); iter_bar();
    iter_b(false);
    iter_a(); waitv<0>(); iter_bar();
#pragma unroll
    for (int i = 0; i < 16; ++i) asm volatile("" : "+v"(kv[i]));
    red_finish(kv, red_done);
    iter_b(false);
  }
  waitl();
  __builtin_amdgcn_sched_barrier(0);
  g2.kstep(f0, f1, stage, 1, true, false, 0);
  if (r < 2) { if (r == 0) dots2(cs0, csd, f0.w[0], f0.w[0]); else dots2(cs0, csd, f0.w[1], f0.w[1]); }
  waitl();
  __builtin_amdgcn_sched_barrier(0);
  g2.kstep(f1, f0, 0, 0, false, false, 0);
  if (r < 2) { if (r == 0) dots2(cs0, csd, f1.w[0], f1.w[0]); else dots2(cs0, csd, f1.w[1], f1.w[1]); }
  {
    float v = rscatter_add(cs0, 0.f, 0.f, 0.f);
    if (r < 2 && lg == 0) *(float*)(smem + L_CS + ((10 + 2 * C + r) * 16 + l15) * 4) = v;
  }
  waitl();
  __builtin_amdgcn_s_barrier();
  __builtin_amdgcn_sched_barrier(0);
  STAMP(6);
  {
    f32x2 st[2] = {stat_of(smem, 32 * r + l15), stat_of(smem, 32 * r + 16 + l15)};
    mish_pair(rs, smem, g2.acc[0], g2.acc[1], 10 + 2 * C, 2 * C, t0w + 2 * C, false, st, rb * 8 + 2 * r, lane);
  }
  if (C == 1 && is_red && red_done) {
    drain();
    if (lane == 0) st_flag(p.ws + F_KVH + (g8 * 16 + rb) * 8, tag);
  }
  if (C == 1 && !red_done) {
    polln(p.ws + F_KVP + g8 * 128, 16, tag, lane);
    u32x4 kv[16];
#pragma unroll
    for (int i = 0; i < 16; ++i) kv[i] = ld16(rs, OFF_KVP + (g8 * 16 + i) * 15360 + rb * 1024 + lane * 16);
    red_finish(kv, true);
    drain();
    if (lane == 0) st_flag(p.ws + F_KVH + (g8 * 16 + rb) * 8, tag);
  }
  drain();
  if (arrive(smem, 0, 8, lane) && lane == 0) st_flag(p.ws + F_ACT + (rb * 16 + g8) * 8, tag);
  return tag;
}

template <int C>
DI u64 body_b(const P1& p, rsrc_t rs, char* smem, int rb, int g8, int w, int lane STAMP_ARG) {
  const int r = w & 3, lg = lane >> 4, l15 = lane & 15;
  const int t0w = min(32 + 10 * g8, 98);
  Gemm<2, C> g;
  Gemm<1, C> g2;
  const u64 tag = first_pass<1, C>(g, g2, p, rs, smem, rb, g8, t0w, w, lane STAMP_PASS);
  f32x2 st[2] = {stat_of(smem, 32 * r + l15), stat_of(smem, 32 * r + 16 + l15)};
  if constexpr (C == 1) {
    __builtin_amdgcn_s_setprio(2);
#pragma unroll
    for (int pp = 0; pp < 4; ++pp)
      mish_pair(rs, smem, g.acc[2 * pp], g.acc[2 * pp + 1], 7 + 2 * pp, 2 + 2 * pp, t0w + 2 + 2 * pp, g8 == 7 && pp == 0, st, rb * 8 + 2 * r, lane);
    __builtin_amdgcn_s_setprio(0);
    STAMP(4);
    drain();
    if (arrive(smem, 0, 8, lane) && lane == 0) st_flag(p.ws + F_ACT + (rb * 16 + 8 + g8) * 8, tag);
    STAMP(5);
  } else {
    mish_pair(rs, smem, g.acc[5], g.acc[6], 5, 0, t0w, g8 == 7, st, rb * 8 + 2 * r, lane);
    STAMP(4);
    if (w == 0 && lane < 30) *(u32x4*)(smem + E_KV + lane * 272 + 256) = u32x4{0u, 0u, 0u, 0u};
    bf16x8 ap[5][3];
    load_aphi(smem, lane, ap);
    bf16x8 ph[2][5];
#pragma unroll
    for (int tt = 0; tt < 2; ++tt) {
      u32x2 pos[5], neg[5];
      features<7>(smem, g.acc, tt, st[tt], ap, lane, pos, neg);
      ph[tt][0] = mk8(pos[0], pos[1]); ph[tt][1] = mk8(pos[2], pos[3]);
      ph[tt][2] = mk8(neg[0], neg[1]); ph[tt][3] = mk8(neg[2], neg[3]); ph[tt][4] = mk8(pos[4], neg[4]);
      if (tt == 0) {
        drain();
        if (arrive(smem, 0, 8, lane) && lane == 0) st_flag(p.ws + F_ACT + (rb * 16 + 8 + g8) * 8, tag);
      }
    }
    STAMP(5);
    if (w == 0) { polln(p.ws + F_KVH + g8 * 128, 15, tag, lane); set_cnt(smem, 4, lane); }
    else wait_cnt(smem, 4, 1);
    {
      int e0 = w * 120 + lane, e1 = e0 + 64;
      u32x4 a = ld16(rs, OFF_KVH + g8 * 7680 + e0 * 16);
      u32x4 b = ld16(rs, lane < 56 ? OFF_KVH + g8 * 7680 + e1 * 16 : OOB);
      *(u32x4*)(smem + E_KV + (e0 >> 4) * 272 + (e0 & 15) * 16) = a;
      if (lane < 56) *(u32x4*)(smem + E_KV + (e1 >> 4) * 272 + (e1 & 15) * 16) = b;
    }
    waitl();
    asm volatile("" ::: "memory");
    (void)arrive(smem, 1, 4, lane);
    wait_cnt(smem, 1, 4);
    STAMP(6);
    const char* ks_ = smem + E_KV + (((l15 >> 2) == lg) ? (lg * 4 + (l15 & 3)) * 16 : 256);
    f32x4 n[2][6];
#pragma unroll
    for (int tn = 0; tn < 6; ++tn) {
      n[0][tn] = f32x4{0, 0, 0, 0}; n[1][tn] = f32x4{0, 0, 0, 0};
#pragma unroll
      for (int ks = 0; ks < 5; ++ks) {
        bf16x8 ka = *(const bf16x8*)(ks_ + (tn * 5 + ks) * 272);
        n[0][tn] = MFMA16(ka, ph[0][ks], n[0][tn]);
        n[1][tn] = MFMA16(ka, ph[1][ks], n[1][tn]);
      }
    }
#pragma unroll
    for (int tt = 0; tt < 2; ++tt) {
      const f32x2 rc = splat2(__builtin_amdgcn_rcpf(n[tt][5][0] + 1e-6f));
      u32x2 o[5];
#pragma unroll
      for (int t = 0; t < 5; ++t) o[t] = u32x2{pack2v(lo2(n[tt][t]) * rc), pack2v(hi2(n[tt][t]) * rc)};
      const unsigned ob = OFF_O + ((rb * 8 + 2 * r + tt) * 20 + ((5 * g8) >> 1)) * 1024 + lane * 16;
      if ((g8 & 1) == 0) {
        st16(rs, ob, u32x4{o[0][0], o[0][1], o[1][0], o[1][1]});
        st16(rs, ob + 1024, u32x4{o[2][0], o[2][1], o[3][0], o[3][1]});
        st8(rs, ob + 2048, o[4]);
      } else {
        st8(rs, ob + 8, o[0]);
        st16(rs, ob + 1024, u32x4{o[1][0], o[1][1], o[2][0], o[2][1]});
        st16(rs, ob + 2048, u32x4{o[3][0], o[3][1], o[4][0], o[4][1]});
      }
    }
    drain();
    if (arrive(smem, 2, 4, lane) && lane == 0) st_flag(p.ws + F_O + (rb * 16 + g8) * 8, tag);
  }
  return tag;
}

#define GLD(dst, ptr, off) asm volatile("global_load_dwordx4 %0, %1, off offset:" #off " sc1" : "=v"(dst) : "v"(ptr) : "memory")
#define WAITV(n) do { asm volatile("s_waitcnt vmcnt(%0)" :: "i"(n) : "memory"); __builtin_amdgcn_sched_barrier(0); } while (0)

template <int NST, int NA, int NB, int S>
DI void p2_steps(const char*& pa, const char*& pa1, const char*& pa2, const char*& pa3, const char*& pb,
                 u32x4 (&A)[NA][4], u32x4 (&B)[NB][5], f32x4 (&acc)[5][4]) {
  if constexpr (S < NST) {
    constexpr int ja = S - NA, jb = S - NB;
    auto cnt = [](int j0, bool skipA) constexpr {
      int n = 0;
      for (int j = j0; j < S; ++j) {
        if (j + NA < NST && !(skipA && j == j0)) n += 4;
        if (j + NB < NST) n += 5;
      }
      return n;
    };
    constexpr int ya = ja < 0 ? 4 * ((NA < NST ? NA : NST) - 1 - S) + cnt(0, false) : cnt(ja, true);
    constexpr int yb = jb < 0 ? 1000 : cnt(jb + 1, false);
    constexpr int y = ya < yb ? ya : yb;
    WAITV(y);
#pragma unroll
    for (int i = 0; i < 4; ++i) asm volatile("" : "+v"(A[S % NA][i]));
#pragma unroll
    for (int i = 0; i < 5; ++i) asm volatile("" : "+v"(B[S % NB][i]));
#pragma unroll
    for (int n = 0; n < 5; ++n)
#pragma unroll
      for (int t = 0; t < 4; ++t)
        acc[n][t] = MFMA16(__builtin_bit_cast(bf16x8, B[S % NB][n]), __builtin_bit_cast(bf16x8, A[S % NA][t]), acc[n][t]);
    __builtin_amdgcn_sched_barrier(0);
    if constexpr (S + NA < NST) {
      GLD(A[S % NA][0], pa, 0); GLD(A[S % NA][1], pa1, 0); GLD(A[S % NA][2], pa2, 0); GLD(A[S % NA][3], pa3, 0);
      pa += 1024; pa1 += 1024; pa2 += 1024; pa3 += 1024;
    }
    if constexpr (S + NB < NST) {
      GLD(B[S % NB][0], pb, -2048); GLD(B[S % NB][1], pb, -1024); GLD(B[S % NB][2], pb, 0); GLD(B[S % NB][3], pb, 1024); GLD(B[S % NB][4], pb, 2048);
      pb += 5120;
    }
    p2_steps<NST, NA, NB, S + 1>(pa, pa1, pa2, pa3, pb, A, B, acc);
  }
}
#define ISSUE_A(A, s) do { GLD(A[s][0], pa, 0); GLD(A[s][1], pa1, 0); GLD(A[s][2], pa2, 0); GLD(A[s][3], pa3, 0); \
    pa += 1024; pa1 += 1024; pa2 += 1024; pa3 += 1024; } while (0)
#define ISSUE_B(B, s) do { GLD(B[s][0], pb, -2048); GLD(B[s][1], pb, -1024); GLD(B[s][2], pb, 0); GLD(B[s][3], pb, 1024); GLD(B[s][4], pb, 2048); \
    pb += 5120; } while (0)
DI void wait_flags(char* smem, int cnt, bool poller, const char* flags, int n, u64 tag, int lane) {
  if (poller) { polln(flags, n, tag, lane); set_cnt(smem, cnt, lane); }
  else { wait_cnt(smem, cnt, 1); drain(); }
}
template <int NST, int NA, int NB>
DI void stream(const char* pa, unsigned astr, const char* pb, char* smem, int cnt, bool poller, const char* flags, int nflags, u64 tag, int lane,
               f32x4 (&acc)[5][4]) {
  const char* pa1 = pa + astr; const char* pa2 = pa1 + astr; const char* pa3 = pa2 + astr;
  u32x4 A[NA][4], B[NB][5];
#pragma unroll
  for (int s = 0; s < NB; ++s) ISSUE_B(B, s);
  wait_flags(smem, cnt, poller, flags, nflags, tag, lane);
#pragma unroll
  for (int s = 0; s < NA; ++s) ISSUE_A(A, s);
  p2_steps<NST, NA, NB, 0>(pa, pa1, pa2, pa3, pb, A, B, acc);
}

__global__ __launch_bounds__(512, 2) void k_fused(P1 p) {
  extern __shared__ __attribute__((aligned(16))) char smem[];
  const int b = blockIdx.x, xcd = b & 7, idx = b >> 3;
  const int w = __builtin_amdgcn_readfirstlane(threadIdx.x >> 6), lane = threadIdx.x & 63;
  const int rb = (xcd >> 2) * 8 + (idx >> 2);
  const int jl = idx & 3, xc = xcd & 3;
  const int type = jl >> 1, g8 = 2 * xc + (jl & 1);
  const int c = w >> 2;
  STAMP_DECL; STAMP(0);
  const rsrc_t rs = __builtin_amdgcn_make_buffer_rsrc(p.ws, 0, WS_USED, 0x00020000);
  u64 tag;
  if (type == 0) {
    if (c == 0) tag = body_a<0>(p, rs, smem, rb, g8, w, lane STAMP_PASS);
    else tag = body_a<1>(p, rs, smem, rb, g8, w, lane STAMP_PASS);
  } else {
    if (c == 0) tag = body_b<0>(p, rs, smem, rb, g8, w, lane STAMP_PASS);
    else tag = body_b<1>(p, rs, smem, rb, g8, w, lane STAMP_PASS);
  }
  const int lg = lane >> 4, l15 = lane & 15;
  STAMP(7);
  if (w == 5) {
    unsigned v = __hip_atomic_load((LDS3 unsigned*)(smem + L_CNT + 12), __ATOMIC_RELAXED, __HIP_MEMORY_SCOPE_WORKGROUP);
    if (__builtin_amdgcn_readfirstlane(v) == 0) {
      poll256(p.ws + F_EARLY, tag, lane);
      if (blockIdx.x == 0 && lane == 0) st_flag(p.ws + OFF_FLG, tag);
      set_cnt(smem, 3, lane);
    }
  } else wait_cnt(smem, 3, 1);
  const int rt = DMAP == 0 ? (xcd >> 1) * 8 + (idx >> 2) : xcd * 4 + (idx >> 3);
  const int ct = DMAP == 0 ? (xcd & 1) * 4 + (idx & 3) : (idx & 7);
  u32x2 xv[3];
  const unsigned o0 = (unsigned)((rt * 64 + (w & 3) * 16 + l15) * 640 + ct * 80 + (w >> 2) * 16 + 4 * lg);
#pragma unroll
  for (int m = 0; m < 3; ++m) xv[m] = *(const u32x2*)(p.x + o0 + ((m < 2 || w < 4) ? 32 * m : 0));
#pragma unroll
  for (int m = 0; m < 3; ++m) asm volatile("" : "+v"(xv[m]));
  f32x4 acc[5][4];
#pragma unroll
  for (int n = 0; n < 5; ++n)
#pragma unroll
    for (int t = 0; t < 4; ++t) acc[n][t] = f32x4{0, 0, 0, 0};
  const char* fact = p.ws + F_ACT + (rt >> 1) * 128;
  if (w >= 4) {
    const char* pa = p.ws + OFF_ACT + (rt * 4) * 54 * 1024 + lane * 16;
    const char* pb = p.ws + OFF_W2F + ct * 74 * 5120 + lane * 16 + 2048;
    const int j = w - 4;
    const int u1 = 16 + 10 * j - (j == 3 ? 1 : 0);
    if (j < 2) stream<10, 5, 3>(pa + u1 * 1024, 54 * 1024, pb + u1 * 5120, smem, 5, w == 4, fact + 64, 8, tag, lane, acc);
    else stream<9, 5, 3>(pa + u1 * 1024, 54 * 1024, pb + u1 * 5120, smem, 5, false, fact + 64, 8, tag, lane, acc);
    STAMP(8);
    stream<4, 4, 4>(pa + 4 * j * 1024, 54 * 1024, pb + 4 * j * 5120, smem, 6, w == 4, fact, 8, tag, lane, acc);
  } else {
    stream<5, 4, 4>(p.ws + OFF_O + ((rt * 4) * 20 + 5 * w) * 1024 + lane * 16, 20 * 1024,
                    p.ws + OFF_W2F + (ct * 74 + 54 + 5 * w) * 5120 + lane * 16 + 2048, smem, 7, w == 0, p.ws + F_O + (rt >> 1) * 128, 8, tag, lane, acc);
    if (type == 1) wait_cnt(smem, 2, 4);
  }
  STAMP(9);
  {
    char* dst = smem + w * 18432 + lane * 16;
#pragma unroll
    for (int tile = 0; tile < 20; ++tile) {
      int pos = tile - (tile > w) - (tile > w + 8) - (tile > w + 16);
      if ((tile & 7) != w) *(f32x4*)(dst + pos * 1024) = acc[tile >> 2][tile & 3];
    }
  }
  f32x4 own[3];
#pragma unroll
  for (int m = 0; m < 3; ++m) {
    own[m] = f32x4{0, 0, 0, 0};
#pragma unroll
    for (int k = 0; k < 8; ++k)
      if (8 * m + k < 20 && k == w) own[m] = acc[(8 * m + k) >> 2][(8 * m + k) & 3];
  }
  __syncthreads();
  STAMP(10);
#pragma unroll
  for (int m = 0; m < 3; ++m) {
    const int tile = w + 8 * m;
    if (tile < 20) {
      f32x4 part[8];
#pragma unroll
      for (int v = 0; v < 8; ++v) {
        int pos = tile - (tile > v) - (tile > v + 8) - (tile > v + 16);
        part[v] = *(const f32x4*)(smem + v * 18432 + pos * 1024 + lane * 16);
        if (v == w) part[v] = own[m];
      }
      f32x4 ssum = (part[0] + part[1]) + (part[2] + part[3]);
      f32x4 bsum = (part[4] + part[5]) + (part[6] + part[7]);
      float xs[4] = {bf_lo(xv[m][0]), bf_hi(xv[m][0]), bf_lo(xv[m][1]), bf_hi(xv[m][1])};
      float r[4];
#pragma unroll
      for (int j = 0; j < 4; ++j) {
        float bb = bsum[j];
        float g = bb * __builtin_amdgcn_rcpf(1.0f + __builtin_amdgcn_exp2f(-bb * LOG2E));
        r[j] = fmaf(ssum[j], g, xs[j]);
      }
      *(u32x2*)(p.out + o0 + 32 * m) = u32x2{pack2(r[0], r[1]), pack2(r[2], r[3])};
    }
  }
  STAMP(11);
  STAMP_FLUSH(p.ws, blockIdx.x * 8 + w);
}
}

extern "C" void kernel_launch(void* const* d_in, const int* in_sizes, int n_in, void* d_out, int out_size, void* d_ws,
                              size_t ws_size, hipStream_t stream) {
  using namespace hk;
  P1 p;
  p.x = (const u16*)d_in[0]; p.wqkv = (const u16*)d_in[1]; p.wphi = (const u16*)d_in[2]; p.wo = (const u16*)d_in[3];
  p.w1 = (const u16*)d_in[5]; p.b1 = (const u16*)d_in[6]; p.w2 = (const u16*)d_in[7]; p.ws = (char*)d_ws; p.out = (u16*)d_out;
  (void)hipFuncSetAttribute((const void*)k_fused, hipFuncAttributeMaxDynamicSharedMemorySize, L_END);
  k_fused<<<256, 512, L_END, stream>>>(p);
}
```

```cpp
#include <hip/hip_runtime.h>
#include <hip/hip_bf16.h>

typedef unsigned short u16;
typedef __bf16 bf16x2 __attribute__((ext_vector_type(2)));
typedef __bf16 bf16x4 __attribute__((ext_vector_type(4)));
typedef __bf16 bf16x8 __attribute__((ext_vector_type(8)));
typedef float f32x2 __attribute__((ext_vector_type(2)));
typedef float f32x4 __attribute__((ext_vector_type(4)));
typedef unsigned u32x2 __attribute__((ext_vector_type(2)));
typedef unsigned u32x4 __attribute__((ext_vector_type(4)));
#define LDS3 __attribute__((address_space(3)))
#define DI __device__ __forceinline__
#define MFMA16(a, b, c) __builtin_amdgcn_mfma_f32_16x16x32_bf16((a), (b), (c), 0, 0, 0)

#define STAMP(i)
#define STAMP_DECL
#define STAMP_FLUSH(ws, slot)
namespace hk {
typedef unsigned long long u64;
constexpr float LOG2E = 1.4426950408889634f;
constexpr float LN2 = 0.6931471805599453f;
#define ROTP 2
#define DMAP 0
constexpr int CODE_KB = 80, CODE_W = 7;
constexpr int FLAG_I = ROTP + 2 > 8 ? 8 : ROTP + 2;
constexpr unsigned OFF_ACT = 0;
constexpr unsigned OFF_O = OFF_ACT + 128 * 54 * 1024;
constexpr unsigned OFF_W2F = OFF_O + 128 * 20 * 1024;
constexpr unsigned OFF_KVP = OFF_W2F + 8 * 74 * 5 * 1024;
constexpr unsigned OFF_KVH = OFF_KVP + 8 * 16 * 60 * 256;
constexpr unsigned OFF_STP = OFF_KVH + 8 * 30 * 256;
constexpr unsigned OFF_CSP = OFF_STP + 16 * 10 * 1024;
constexpr unsigned OFF_FLG = OFF_CSP + 16 * 10 * 1024;
constexpr unsigned F_EARLY = OFF_FLG + 128;
constexpr unsigned F_ACT = F_EARLY + 2048;
constexpr unsigned F_KVP = F_ACT + 2048;
constexpr unsigned F_KVH = F_KVP + 1024;
constexpr unsigned F_O = F_KVH + 1024;
constexpr unsigned F_ST = F_O + 2048;
constexpr unsigned F_CS = F_ST + 2048;
constexpr unsigned WS_USED = F_CS + 2048;
constexpr unsigned OOB = 0x7ffff000u;
constexpr int XB = 16384;
constexpr int L_PT = 141312;
constexpr int L_RAW = L_PT + 14336;
constexpr int L_BIAS = L_RAW + 4096;
constexpr int L_ZERO = L_BIAS + 1024;
constexpr int L_CNT = L_ZERO + 64;
constexpr int L_BIASL = L_CNT + 64;
constexpr int L_CS = L_BIASL + 640;
constexpr int L_SUM = L_CS + 960;
constexpr int L_END = 163840;
static_assert(L_SUM + 1024 <= L_END - 256, "lds");
constexpr int E_PHI = 0;
constexpr int E_V = 40960;
constexpr int E_P2 = 61440;
constexpr int E_KV = 0;

DI unsigned pack2(float a, float b) {
  f32x2 v = {a, b};
  return __builtin_bit_cast(unsigned, __builtin_convertvector(v, bf16x2));
}
DI unsigned pack2v(f32x2 v) { return __builtin_bit_cast(unsigned, __builtin_convertvector(v, bf16x2)); }
DI bf16x2 as_bf2(unsigned u) { return __builtin_bit_cast(bf16x2, u); }
DI float bf_lo(unsigned u) { return __uint_as_float(u << 16); }
DI float bf_hi(unsigned u) { return __uint_as_float(u & 0xffff0000u); }
DI float rscatter_add(float m0, float m1, float m2, float m3) {
  auto a = __builtin_amdgcn_permlane16_swap(__float_as_uint(m0), __float_as_uint(m1), false, false);
  auto b = __builtin_amdgcn_permlane16_swap(__float_as_uint(m2), __float_as_uint(m3), false, false);
  float A = __uint_as_float(a[0]) + __uint_as_float(a[1]);
  float B = __uint_as_float(b[0]) + __uint_as_float(b[1]);
  auto c = __builtin_amdgcn_permlane32_swap(__float_as_uint(A), __float_as_uint(B), false, false);
  return __uint_as_float(c[0]) + __uint_as_float(c[1]);
}
DI f32x2 lo2(f32x4 v) { return __builtin_shufflevector(v, v, 0, 1); }
DI f32x2 hi2(f32x4 v) { return __builtin_shufflevector(v, v, 2, 3); }
DI f32x2 fma2(f32x2 a, f32x2 b, f32x2 c) { return __builtin_elementwise_fma(a, b, c); }
DI f32x2 splat2(float a) { return f32x2{a, a}; }
DI f32x2 exp2v(f32x2 a) { return f32x2{__builtin_amdgcn_exp2f(a[0]), __builtin_amdgcn_exp2f(a[1])}; }
DI f32x2 rcpv(f32x2 a) { return f32x2{__builtin_amdgcn_rcpf(a[0]), __builtin_amdgcn_rcpf(a[1])}; }
DI void dual_softmax(const f32x4 (&p)[5], u32x2 (&pos)[5], u32x2 (&neg)[5]) {
  float mx = fmaxf(p[0][0], p[0][1]), mn = fminf(p[0][0], p[0][1]);
  mx = fmaxf(fmaxf(mx, p[0][2]), p[0][3]); mn = fminf(fminf(mn, p[0][2]), p[0][3]);
#pragma unroll
  for (int t = 1; t < 5; ++t) {
    mx = fmaxf(fmaxf(mx, p[t][0]), p[t][1]); mx = fmaxf(fmaxf(mx, p[t][2]), p[t][3]);
    mn = fminf(fminf(mn, p[t][0]), p[t][1]); mn = fminf(fminf(mn, p[t][2]), p[t][3]);
  }
  const f32x2 L = splat2(LOG2E), nL = splat2(-LOG2E), cx = splat2(-mx * LOG2E), cn = splat2(mn * LOG2E);
  f32x2 ep[10], en[10];
#pragma unroll
  for (int t = 0; t < 5; ++t) {
    ep[2 * t] = exp2v(fma2(lo2(p[t]), L, cx)); ep[2 * t + 1] = exp2v(fma2(hi2(p[t]), L, cx));
    en[2 * t] = exp2v(fma2(lo2(p[t]), nL, cn)); en[2 * t + 1] = exp2v(fma2(hi2(p[t]), nL, cn));
  }
  f32x2 sp = ep[0], sn = en[0];
#pragma unroll
  for (int i = 1; i < 10; ++i) { sp += ep[i]; sn += en[i]; }
  const f32x2 ip = splat2(__builtin_amdgcn_rcpf(sp[0] + sp[1])), in_ = splat2(__builtin_amdgcn_rcpf(sn[0] + sn[1]));
#pragma unroll
  for (int t = 0; t < 5; ++t) {
    pos[t] = u32x2{pack2v(ep[2 * t] * ip), pack2v(ep[2 * t + 1] * ip)};
    neg[t] = u32x2{pack2v(en[2 * t] * in_), pack2v(en[2 * t + 1] * in_)};
  }
}
DI f32x2 mish2(f32x2 hl) {
  f32x2 e = exp2v(hl);
  f32x2 d = fma2(e, e + splat2(2.0f), splat2(2.0f));
  f32x2 t = fma2(rcpv(d), splat2(-2.0f * LN2), splat2(LN2));
  return hl * t;
}
DI bf16x8 mk8(u32x2 a, u32x2 b) {
  u32x4 v = {a[0], a[1], b[0], b[1]};
  return __builtin_bit_cast(bf16x8, v);
}
DI bf16x8 tr2(const char* a, const char* b) {
  bf16x4 lo = __builtin_amdgcn_ds_read_tr16_b64_v4bf16((LDS3 bf16x4*)(a));
  bf16x4 hi = __builtin_amdgcn_ds_read_tr16_b64_v4bf16((LDS3 bf16x4*)(b));
  return __builtin_shufflevector(lo, hi, 0, 1, 2, 3, 4, 5, 6, 7);
}

struct P1 {
  const u16* x; const u16* wqkv; const u16* wphi; const u16* wo; const u16* w1; const u16* b1; const u16* w2;
  char* ws; u16* out;
};
typedef __amdgpu_buffer_rsrc_t rsrc_t;

DI unsigned lds_addr(const void* p) { return (unsigned)(size_t)(LDS3 const char*)p; }
DI void glds1(const void* g, unsigned lds) {
  unsigned keep;
  asm volatile("s_mov_b32 %0, m0\n\ts_mov_b32 m0, %2\n\ts_nop 0\n\tglobal_load_lds_dwordx4 %1, off\n\ts_mov_b32 m0, %0"
               : "=&s"(keep) : "v"(g), "s"(lds) : "memory");
}
DI void touch(const void* g, unsigned lds) {
  unsigned keep;
  asm volatile("s_mov_b32 %0, m0\n\ts_mov_b32 m0, %2\n\ts_nop 0\n\tglobal_load_lds_dword %1, off\n\ts_mov_b32 m0, %0"
               : "=&s"(keep) : "v"(g), "s"(lds) : "memory");
}
DI u64 next_tag(u64 e) { return e * 0x9E3779B97F4A7C15ull + 0x632BE59BD9B4E019ull; }
DI void st_flag(char* p, u64 v) { __hip_atomic_store((u64*)p, v, __ATOMIC_RELAXED, __HIP_MEMORY_SCOPE_AGENT); }
DI void st16(rsrc_t rs, unsigned off, u32x4 v) { __builtin_amdgcn_raw_buffer_store_b128(v, rs, off, 0, 16); }
DI void st8(rsrc_t rs, unsigned off, u32x2 v) { __builtin_amdgcn_raw_buffer_store_b64(v, rs, off, 0, 16); }
DI void st4(rsrc_t rs, unsigned off, float v) { __builtin_amdgcn_raw_buffer_store_b32(__float_as_uint(v), rs, off, 0, 16); }
DI u32x4 ld16(rsrc_t rs, unsigned off) { return __builtin_amdgcn_raw_buffer_load_b128(rs, off, 0, 16); }
DI bool ready(u32x4 v) { return v[0] != 0xffffffffu && v[1] != 0xffffffffu && v[2] != 0xffffffffu && v[3] != 0xffffffffu; }
DI void drain() { asm volatile("s_waitcnt vmcnt(0)" ::: "memory"); }
constexpr int SPIN_MAX = 1 << 18;
DI bool flags_ok(u32x4 a, u32x4 b, u64 tag) {
  const unsigned tl = (unsigned)tag, th = (unsigned)(tag >> 32);
  return a[0] == tl && a[1] == th && a[2] == tl && a[3] == th && b[0] == tl && b[1] == th && b[2] == tl && b[3] == th;
}
DI void poll256(const char* flags, u64 tag, int lane) {
  const char* q = flags + lane * 32;
#pragma unroll 1
  for (int spin = 0; spin < SPIN_MAX; ++spin) {
    u32x4 a, b;
    asm volatile("global_load_dwordx4 %0, %2, off sc1\n\tglobal_load_dwordx4 %1, %2, off offset:16 sc1\n\ts_waitcnt vmcnt(0)"
                 : "=&v"(a), "=&v"(b) : "v"(q) : "memory");
    if (__all(flags_ok(a, b, tag))) break;
    __builtin_amdgcn_s_sleep(8);
  }
}
DI void pollq(const char* q, u64 tag) {
#pragma unroll 1
  for (int spin = 0; spin < SPIN_MAX; ++spin) {
    u32x2 a;
    asm volatile("global_load_dwordx2 %0, %1, off sc1\n\ts_waitcnt vmcnt(0)" : "=&v"(a) : "v"(q) : "memory");
    if (__all(a[0] == (unsigned)tag && a[1] == (unsigned)(tag >> 32))) break;
    __builtin_amdgcn_s_sleep(3);
  }
}
DI void polln(const char* flags, int n, u64 tag, int lane) { pollq(flags + (lane < n ? lane : 0) * 8, tag); }
DI bool arrive(char* smem, int idx, int n, int lane) {
  unsigned old = 0;
  if (lane == 0) old = __hip_atomic_fetch_add((LDS3 unsigned*)(smem + L_CNT + idx * 4), 1u, __ATOMIC_RELAXED, __HIP_MEMORY_SCOPE_WORKGROUP);
  return __builtin_amdgcn_readfirstlane(old) == (unsigned)(n - 1);
}
DI void set_cnt(char* smem, int idx, int lane) {
  if (lane == 0) __hip_atomic_store((LDS3 unsigned*)(smem + L_CNT + idx * 4), 1u, __ATOMIC_RELAXED, __HIP_MEMORY_SCOPE_WORKGROUP);
}
DI void wait_cnt(char* smem, int idx, unsigned n) {
#pragma unroll 1
  for (int spin = 0; spin < (SPIN_MAX << 4); ++spin) {
    unsigned v = __hip_atomic_load((LDS3 unsigned*)(smem + L_CNT + idx * 4), __ATOMIC_RELAXED, __HIP_MEMORY_SCOPE_WORKGROUP);
    if (__builtin_amdgcn_readfirstlane(v) >= n) break;
    __builtin_amdgcn_s_sleep(1);
  }
  asm volatile("" ::: "memory");
}

template <int NT> struct Frags { bf16x8 x[2]; bf16x8 w[NT]; };

#define STAMP_ARG
#define STAMP_PASS
template <int N> DI void waitv() { asm volatile("s_waitcnt vmcnt(%0)" :: "i"(N) : "memory"); }
DI void waitl() { __builtin_amdgcn_s_waitcnt(0xC07F); }

template <int PASS, int C>
struct Gemm {
  static constexpr int NT = PASS == 0 ? 5 : (PASS == 1 ? 2 : (C == 0 ? 7 : 8));
  static constexpr int NU = PASS == 0 ? 11 : (PASS == 1 ? 5 : 15);
  static constexpr int WROW = NU * 32, NC = NU * 2;
  static constexpr int STAGE = XB + 64 * WROW;
  static constexpr int BASE = 0;
  static constexpr int NSTG = PASS == 1 ? 5 : 3;
  static DI int sbase(int s) { return PASS == 1 ? (s < 3 ? E_P2 + s * STAGE : (s - 3) * STAGE) : s * STAGE; }
  static constexpr int NWS = (NC + 7) / 8;
  static constexpr int NFULL = NC - (NWS - 1) * 8;
  static constexpr int MAGIC = (65536 + NC - 1) / NC;
  static constexpr bool VW = (PASS == 0 && C == 1);
  static constexpr int unit_of(int t) { return PASS == 0 ? t + 5 * C : (PASS == 1 ? t + 2 * C : t + 7 * C); }

  const char* xsrc[2];
  const char* wsrc[NWS];
  unsigned winc[NWS];
  unsigned xa0, wa, lds0;
  int cnext, w;
  bool full;
  char* smem;
  f32x4 acc[NT][2];

  DI void init(const P1& p, char* smem_, int rb, int g8, int t0w, int rot, int w_, int lane) {
    smem = smem_; w = w_; cnext = rot; lds0 = lds_addr(smem_) + BASE; full = w < NFULL;
    const int r = w & 3, lg = lane >> 4, l15 = lane & 15;
#pragma unroll
    for (int i = 0; i < 2; ++i) {
      int row = (i * 8 + w) * 8 + (lane >> 3);
      int ch = (lane & 7) ^ ((row >> 1) & 7);
      xsrc[i] = (const char*)p.x + (unsigned)((rb * 128 + row) * 1280 + ch * 16 + rot * 128);
    }
#pragma unroll
    for (int i = 0; i < NWS; ++i) {
      int ci = (i * 8 + w) * 64 + lane;
      ci = ci < 64 * NC ? ci : lane;
      int pos = (ci * MAGIC) >> 16, c16 = ci - pos * NC;
      int kk = pos & 15;
      int k = (kk >= 4 && kk < 12) ? (pos ^ 12) : pos;
      bool isq = PASS == 0 || (PASS == 2 && c16 < 10);
      int colb;
      if (PASS == 0) { int c = min(c16, 19); colb = 1280 + 160 * g8 + c * 16 + (c >= 10 ? 1120 : 0); }
      else if (PASS == 1) colb = 32 * t0w + min(c16, 7) * 16;
      else colb = isq ? (160 * g8 + c16 * 16) : (32 * t0w + (c16 - 10) * 16);
      unsigned stride = isq ? 3840u : 3456u;
      wsrc[i] = (isq ? (const char*)p.wqkv : (const char*)p.w1) + (unsigned)((k + rot * 64) * stride + colb);
      winc[i] = stride * 64;
    }
    xa0 = BASE + (32 * r + l15) * 128 + ((lg ^ ((l15 >> 1) & 7)) * 16);
    wa = BASE + XB + (16 * (lg >> 1) + 4 * (lg & 1) + (l15 >> 2)) * WROW + (VW ? 160 + (l15 & 3) * 40 : (l15 & 3) * 8);
#pragma unroll
    for (int t = 0; t < NT; ++t) { acc[t][0] = f32x4{0, 0, 0, 0}; acc[t][1] = f32x4{0, 0, 0, 0}; }
  }
  DI void issue1(int s, int stage) {
    unsigned sb = lds0 + sbase(stage) + w * 1024 + s * 0x2000;
    if (s < 2) { glds1(xsrc[s], sb); xsrc[s] += 128; }
    else if (s - 2 < NWS - 1 || full) { glds1(wsrc[s - 2], sb); wsrc[s - 2] += winc[s - 2]; }
  }
  DI void wrap() {
    if (++cnext == 10) {
      cnext = 0;
#pragma unroll
      for (int i = 0; i < 2; ++i) xsrc[i] -= 1280;
#pragma unroll
      for (int i = 0; i < NWS; ++i) wsrc[i] -= 10 * winc[i];
    }
  }
  DI void issue(int stage) {
#pragma unroll
    for (int s = 0; s < 2 + NWS; ++s) issue1(s, stage);
    wrap();
  }
  template <int K, int E> DI void wait_chunks() {
    if (full) waitv<K * (2 + NWS) + E>(); else waitv<K * (1 + NWS) + E>();
  }
  DI void rd_x(Frags<NT>& f, int stage, int ks, int tt) { f.x[tt] = *(const bf16x8*)(smem + sbase(stage) + (xa0 ^ (ks * 64)) + tt * 2048); }
  DI void rd_w(Frags<NT>& f, int stage, int ks, int t) {
    const char* q = smem + sbase(stage) + wa + (VW ? t * 8 : unit_of(t) * 32);
    f.w[t] = tr2(q + (32 * ks) * WROW, q + (32 * ks + 8) * WROW);
  }
  DI void rd_all(Frags<NT>& f, int stage, int ks) {
    rd_x(f, stage, ks, 0); rd_x(f, stage, ks, 1);
#pragma unroll
    for (int t = 0; t < NT; ++t) rd_w(f, stage, ks, t);
  }
  DI void kstep(const Frags<NT>& f, Frags<NT>& g, int nstage, int nks, bool do_rd, bool do_issue, int istage) {
    constexpr int NS = 2 + NWS;
#pragma unroll
    for (int t = 0; t < NT; ++t) {
      if (do_rd) {
        if (t < 2) rd_x(g, nstage, nks, t);
        rd_w(g, nstage, nks, t);
      }
#pragma unroll
      for (int tt = 0; tt < 2; ++tt)
        acc[t][tt] = VW ? MFMA16(f.x[tt], f.w[t], acc[t][tt]) : MFMA16(f.w[t], f.x[tt], acc[t][tt]);
      if (do_issue) {
#pragma unroll
        for (int s = (t * NS) / NT; s < ((t + 1) * NS) / NT; ++s) issue1(s, istage);
      }
      __builtin_amdgcn_sched_barrier(0);
    }
    if (do_issue) wrap();
  }
};

DI void dots2(float& c0, float& c1, const bf16x8& wa_, const bf16x8& wb_) {
  u32x4 a = __builtin_bit_cast(u32x4, wa_), b = __builtin_bit_cast(u32x4, wb_);
  unsigned a0 = a[0], a1 = a[1], a2 = a[2], a3 = a[3], b0 = b[0], b1 = b[1], b2 = b[2], b3 = b[3];
  asm volatile("s_nop 1\n\t"
               "v_dot2c_f32_bf16 %0, 0x3f803f80, %2\n\tv_dot2c_f32_bf16 %1, 0x3f803f80, %6\n\t"
               "v_dot2c_f32_bf16 %0, 0x3f803f80, %3\n\tv_dot2c_f32_bf16 %1, 0x3f803f80, %7\n\t"
               "v_dot2c_f32_bf16 %0, 0x3f803f80, %4\n\tv_dot2c_f32_bf16 %1, 0x3f803f80, %8\n\t"
               "v_dot2c_f32_bf16 %0, 0x3f803f80, %5\n\tv_dot2c_f32_bf16 %1, 0x3f803f80, %9\n\ts_nop 3"
               : "+v"(c0), "+v"(c1) : "v"(a0), "v"(a1), "v"(a2), "v"(a3), "v"(b0), "v"(b1), "v"(b2), "v"(b3));
}
DI void dots_x(float& s1, float& s2, const bf16x8& x) {
  u32x4 a = __builtin_bit_cast(u32x4, x);
  unsigned a0 = a[0], a1 = a[1], a2 = a[2], a3 = a[3];
  asm volatile("s_nop 1\n\t"
               "v_dot2c_f32_bf16 %0, 0x3f803f80, %2\n\tv_dot2c_f32_bf16 %1, %2, %2\n\t"
               "v_dot2c_f32_bf16 %0, 0x3f803f80, %3\n\tv_dot2c_f32_bf16 %1, %3, %3\n\t"
               "v_dot2c_f32_bf16 %0, 0x3f803f80, %4\n\tv_dot2c_f32_bf16 %1, %4, %4\n\t"
               "v_dot2c_f32_bf16 %0, 0x3f803f80, %5\n\tv_dot2c_f32_bf16 %1, %5, %5\n\ts_nop 3"
               : "+v"(s1), "+v"(s2) : "v"(a0), "v"(a1), "v"(a2), "v"(a3));
}
DI f32x2 stat_of(const char* smem, int tok) { return *(const f32x2*)(smem + L_SUM + tok * 8); }
DI void mish_pair(rsrc_t rs, const char* smem, const f32x4 (&a0)[2], const f32x4 (&a1)[2], int ucs, int wl, int wt, bool skip,
                  const f32x2 (&st)[2], int tt0, int lane) {
  const int lg = lane >> 4;
  f32x4 bb[2], c4[2];
#pragma unroll
  for (int h = 0; h < 2; ++h) {
    bb[h] = *(const f32x4*)(smem + L_BIASL + ((wl + h) * 16 + 4 * lg) * 4);
    c4[h] = *(const f32x4*)(smem + L_CS + ((ucs + h) * 16 + 4 * lg) * 4);
  }
#pragma unroll
  for (int tt = 0; tt < 2; ++tt) {
    const f32x2 rsL = splat2(st[tt][0] * LOG2E), nbL = splat2(st[tt][1] * LOG2E);
    u32x4 o;
    o[0] = pack2v(mish2(fma2(lo2(a0[tt]), rsL, fma2(nbL, lo2(c4[0]), lo2(bb[0])))));
    o[1] = pack2v(mish2(fma2(hi2(a0[tt]), rsL, fma2(nbL, hi2(c4[0]), hi2(bb[0])))));
    o[2] = pack2v(mish2(fma2(lo2(a1[tt]), rsL, fma2(nbL, lo2(c4[1]), lo2(bb[1])))));
    o[3] = pack2v(mish2(fma2(hi2(a1[tt]), rsL, fma2(nbL, hi2(c4[1]), hi2(bb[1])))));
    st16(rs, skip ? OOB : OFF_ACT + ((tt0 + tt) * 54 + (wt >> 1)) * 1024 + lane * 16, o);
  }
}
DI void load_aphi(const char* smem, int lane, bf16x8 (&ap)[5][3]) {
  const int lg = lane >> 4, l15 = lane & 15;
  const char* pa[6];
#pragma unroll
  for (int i = 0; i < 6; ++i) {
    int d0 = 16 * i + 4 * lg;
    int h = (d0 * 13) >> 8;
    bool ok = (h == (l15 & 3)) && i < 5;
    pa[i] = smem + (ok ? L_RAW + h * 800 + (d0 - 20 * h + (l15 >> 2)) * 40 : L_ZERO);
  }
#pragma unroll
  for (int t = 0; t < 5; ++t)
#pragma unroll
    for (int ks = 0; ks < 3; ++ks) ap[t][ks] = tr2(pa[2 * ks] + t * 8, pa[2 * ks + 1] + t * 8);
}
template <int NT>
DI void features(const char* smem, const f32x4 (&acc)[NT][2], int tt, f32x2 st, const bf16x8 (&ap)[5][3], int lane, u32x2 (&pos)[5], u32x2 (&neg)[5]) {
  const int lg = lane >> 4;
  u32x2 kb[5];
  const f32x2 rsv = splat2(st[0]), nb = splat2(st[1]);
#pragma unroll
  for (int t = 0; t < 5; ++t) {
    f32x4 c4 = *(const f32x4*)(smem + L_CS + (t * 16 + 4 * lg) * 4);
    kb[t] = u32x2{pack2v(fma2(lo2(acc[t][tt]), rsv, nb * lo2(c4))), pack2v(fma2(hi2(acc[t][tt]), rsv, nb * hi2(c4)))};
  }
  bf16x8 b0 = mk8(kb[0], kb[1]), b1 = mk8(kb[2], kb[3]), b2 = mk8(kb[4], u32x2{0u, 0u});
  f32x4 pk[5];
#pragma unroll
  for (int t = 0; t < 5; ++t) pk[t] = MFMA16(ap[t][2], b2, MFMA16(ap[t][1], b1, MFMA16(ap[t][0], b0, (f32x4{0, 0, 0, 0}))));
  dual_softmax(pk, pos, neg);
}

template <int TYPE, int C, class G, class G2>
DI u64 first_pass(G& g, G2& g2, const P1& p, rsrc_t rs, char* smem, int rb, int g8, int t0w, int w, int lane STAMP_ARG) {
  constexpr int NT = G::NT;
  const int r = w & 3, lg = lane >> 4, l15 = lane & 15;
  const unsigned lds0 = lds_addr(smem);
  u32x2 ev;
  asm volatile("global_load_dwordx2 %0, %1, off sc1" : "=v"(ev) : "v"(p.ws + OFF_FLG) : "memory");
  const int job = TYPE * 8 + g8;
  const int myc = (rb + job) % 10;
  const int rot = (myc / ROTP) * ROTP;
  const int my_i = myc - rot;
  g.init(p, smem, rb, g8, t0w, rot, w, lane);
  if (C == 1) {
    int ci = r * 64 + lane; ci = ci < 200 ? ci : 199;
    glds1((const char*)p.wphi + g8 * 3200 + ci * 16, lds0 + L_RAW + r * 1024);
  } else if (w == 3) {
    int nb = TYPE == 0 ? 8 : 20;
    int ci = lane < nb ? lane : nb - 1;
    glds1((const char*)p.b1 + t0w * 32 + ci * 16, lds0 + L_BIAS);
    if (lane < 32) *(unsigned*)(smem + L_ZERO + lane * 4) = 0u;
  }
  g.issue(0);
  __builtin_amdgcn_s_barrier();
  g.issue(1);
  g.issue(2);
  const int ptj = w * 256 + blockIdx.x;
  const bool has_pt = C == 0 && w < 3 && ptj < 740;
  const int ptu = ptj / 10, ptL = ptj - ptu * 10;
  if (has_pt) {
#pragma unroll
    for (int i = 0; i < 4; ++i) {
      int rho = i * 8 + (lane >> 3);
      const u16* src;
      if (ptu < 54) src = p.w2 + (unsigned)((32 * ptu + rho) * 640);
      else {
        int T = 2 * (ptu - 54) + (rho >> 4), Gq = (T * 13) >> 6;
        src = p.wo + (unsigned)(((4 * Gq + ((rho >> 2) & 3)) * 20 + 4 * (T - 5 * Gq) + (rho & 3)) * 640);
      }
      glds1((const char*)src + ptL * 128 + (lane & 7) * 16, lds0 + L_PT + w * 4096 + i * 1024);
    }
  }
  float cs[2] = {0.f, 0.f}, sx[2] = {0.f, 0.f};
  auto do_dots = [&](const Frags<NT>& f) {
    dots_x(sx[0], sx[1], f.x[C]);
    if (r == 0) dots2(cs[0], cs[1], f.w[0], f.w[4]);
    else if (r == 1) dots2(cs[0], cs[1], f.w[1], f.w[NT > 5 ? 5 : 0]);
    else if (r == 2) dots2(cs[0], cs[1], f.w[2], f.w[NT > 6 ? 6 : 0]);
    else dots2(cs[0], cs[1], f.w[3], f.w[NT > 7 ? 7 : 0]);
  };
  Frags<NT> f0, f1;
  if (has_pt) g.template wait_chunks<2, 4>(); else g.template wait_chunks<2, 0>();
  waitl();
  __builtin_amdgcn_s_barrier();
  __builtin_amdgcn_sched_barrier(0);
  STAMP(1);
  g.rd_all(f0, 0, 0);
  {
    int tid = w * 64 + lane;
    if (tid < 160) *(float*)(smem + L_BIASL + tid * 4) = bf_lo(*(const u16*)(smem + L_BIAS + tid * 2)) * LOG2E;
  }
  asm volatile("" : "+v"(ev));
  const u64 tag = next_tag(((u64)(unsigned)__builtin_amdgcn_readfirstlane(ev[1]) << 32) | (unsigned)__builtin_amdgcn_readfirstlane(ev[0]));
  int stage = 0;
  u32x4 fa, fb;
  u32x2 tf;
  const char* tfp = p.ws + (lane < 10 ? F_ST + (rb * 16 + lane) * 8 : F_CS + (job * 16 + (lane < 20 ? lane - 10 : 0)) * 8);
#pragma unroll 1
  for (int i = 0; i < 9; ++i) {
    waitl();
    __builtin_amdgcn_sched_barrier(0);
    g.kstep(f0, f1, stage, 1, true, false, 0);
    if (i == my_i) do_dots(f0);
    int nstage = stage == 2 ? 0 : stage + 1;
    if (has_pt && (i == 0 || i == 2 || i == 3)) g.template wait_chunks<1, 4>();
    else if (i < 8) g.template wait_chunks<1, 0>();
    else waitv<0>();
    waitl();
    __builtin_amdgcn_s_barrier();
    __builtin_amdgcn_sched_barrier(0);
    if (i == FLAG_I && w == 0 && lane < 3)
      st_flag(p.ws + (lane == 0 ? F_EARLY + blockIdx.x * 8 : (lane == 1 ? F_ST + (rb * 16 + job) * 8 : F_CS + (job * 16 + rb) * 8)), tag);
    g.kstep(f1, f0, nstage, 0, true, i < 7, stage);
    if (i == 1 && has_pt) {
      const char* q = smem + L_PT + w * 4096 + (4 * lg + (l15 >> 2)) * 128 + (l15 & 3) * 8;
#pragma unroll
      for (int n = 0; n < 4; ++n) {
        bf16x8 f = tr2(q + n * 32, q + 2048 + n * 32);
        int N = 4 * ptL + n, ct = (N * 13) >> 6;
        st16(rs, OFF_W2F + ((ct * 74 + ptu) * 5 + (N - 5 * ct)) * 1024 + lane * 16, __builtin_bit_cast(u32x4, f));
      }
    }
    if (i == my_i) {
      do_dots(f1);
      float v = rscatter_add(sx[0], sx[1], cs[0], cs[1]);
      unsigned a = lg < 2 ? OFF_STP + (rb * 10 + myc) * 1024 + lg * 512 + (32 * r + 16 * C + l15) * 4
                          : OFF_CSP + (job * 10 + myc) * 1024 + ((G::unit_of(0) + r + (lg == 2 ? 0 : 4)) * 16 + l15) * 4;
      bool on = lg < 2 ? job < 10 : (rb < 10 && (lg == 2 || r + 4 < NT));
      st4(rs, on ? a : OOB, v);
    }
    if (i == 7) {
      asm volatile("global_load_dwordx2 %0, %1, off sc1" : "=v"(tf) : "v"(tfp) : "memory");
      if (w == 5)
        asm volatile("global_load_dwordx4 %0, %2, off sc1\n\tglobal_load_dwordx4 %1, %2, off offset:16 sc1"
                     : "=&v"(fa), "=&v"(fb) : "v"(p.ws + F_EARLY + lane * 32) : "memory");
    }
    stage = nstage;
  }
  asm volatile("" : "+v"(tf));
  if (!__all(tf[0] == (unsigned)tag && tf[1] == (unsigned)(tag >> 32))) pollq(tfp, tag);
  unsigned pv[10], pw[10];
  {
    const int tid = w * 64 + lane;
    const char* q = p.ws + (tid < 128 ? OFF_STP + rb * 10240 + tid * 4 : OFF_CSP + job * 10240 + ((tid - 128) & 255) * 4);
    const char* q2 = q + 5120;
#define LD10(pv, q, q2, q3) \
    asm volatile("global_load_dword %0, %10, off sc1\n\tglobal_load_dword %1, %10, off offset:1024 sc1\n\t" \
                 "global_load_dword %2, %10, off offset:2048 sc1\n\tglobal_load_dword %3, %10, off offset:3072 sc1\n\t" \
                 "global_load_dword %4, %11, off offset:-1024 sc1\n\tglobal_load_dword %5, %11, off sc1\n\t" \
                 "global_load_dword %6, %11, off offset:1024 sc1\n\tglobal_load_dword %7, %11, off offset:2048 sc1\n\t" \
                 "global_load_dword %8, %11, off offset:3072 sc1\n\tglobal_load_dword %9, %12, off sc1" \
                 : "=&v"(pv[0]), "=&v"(pv[1]), "=&v"(pv[2]), "=&v"(pv[3]), "=&v"(pv[4]), "=&v"(pv[5]), "=&v"(pv[6]), "=&v"(pv[7]), "=&v"(pv[8]), "=&v"(pv[9]) \
                 : "v"(q), "v"(q2), "v"(q3) : "memory")
    LD10(pv, q, q2, q2 + 4096);
    if constexpr (C == 0) { LD10(pw, q + 512, q2 + 512, q2 + 4608); }
  }
  if (C == 1 && w == 5) {
    asm volatile("" : "+v"(fa), "+v"(fb));
    if (__all(flags_ok(fa, fb, tag))) {
      if (blockIdx.x == 0 && lane == 0) st_flag(p.ws + OFF_FLG, tag);
      set_cnt(smem, 3, lane);
      constexpr int NL = DMAP == 0 ? 370 : 740;
      const char* q = p.ws + OFF_W2F + (DMAP == 0 ? (blockIdx.x & 1) * 4 * 378880 : 0) + ((blockIdx.x >> 3) * NL + lane) * 128;
#pragma unroll
      for (int j = 0; j < (NL + 63) / 64; ++j) touch(q + (j * 64 + lane < NL ? j * 8192 : 0), lds0 + L_PT);
    }
  }
  if (TYPE == 0) {
    g2.init(p, smem, rb, g8, t0w, 0, w, lane);
    g2.issue(0); g2.issue(1); g2.issue(2);
  }
  waitl();
  __builtin_amdgcn_sched_barrier(0);
  g.kstep(f0, f1, stage, 1, true, false, 0);
  waitl();
  __builtin_amdgcn_sched_barrier(0);
  g.kstep(f1, f0, 0, 0, false, false, 0);
  STAMP(2);
  {
    if (TYPE == 0) g2.template wait_chunks<3, 0>(); else waitv<0>();
    asm volatile("" : "+v"(pv[0]), "+v"(pv[1]), "+v"(pv[2]), "+v"(pv[3]), "+v"(pv[4]), "+v"(pv[5]), "+v"(pv[6]), "+v"(pv[7]), "+v"(pv[8]), "+v"(pv[9]) :: "memory");
    float v = 0.f;
#pragma unroll
    for (int i = 0; i < 10; ++i) v += __uint_as_float(pv[i]);
    const int tid = w * 64 + lane;
    if constexpr (C == 0) {
      asm volatile("" : "+v"(pw[0]), "+v"(pw[1]), "+v"(pw[2]), "+v"(pw[3]), "+v"(pw[4]), "+v"(pw[5]), "+v"(pw[6]), "+v"(pw[7]), "+v"(pw[8]), "+v"(pw[9]) :: "memory");
      if (w < 2) {
        float v2 = 0.f;
#pragma unroll
        for (int i = 0; i < 10; ++i) v2 += __uint_as_float(pw[i]);
        float mean = v * (1.0f / 640.0f);
        float var = fmaxf(fmaf(-mean, mean, v2 * (1.0f / 640.0f)), 0.f);
        float rstd = __builtin_amdgcn_rsqf(var + 1e-5f);
        *(f32x2*)(smem + L_SUM + tid * 8) = f32x2{rstd, -rstd * mean};
      }
    }
    if (tid >= 128 && tid < 368) *(float*)(smem + L_CS + (tid - 128) * 4) = v;
  }
  waitl();
  __builtin_amdgcn_s_barrier();
  __builtin_amdgcn_sched_barrier(0);
  STAMP(3);
  return tag;
}

template <int C>
DI u64 body_a(const P1& p, rsrc_t rs, char* smem, int rb, int g8, int w, int lane STAMP_ARG) {
  const int r = w & 3, lg = lane >> 4, l15 = lane & 15;
  const int t0w = 4 * g8;
  Gemm<0, C> g;
  Gemm<1, C> g2;
  const u64 tag = first_pass<0, C>(g, g2, p, rs, smem, rb, g8, t0w, w, lane STAMP_PASS);
  if constexpr (C == 0) {
    bf16x8 ap[5][3];
    load_aphi(smem, lane, ap);
#pragma unroll
    for (int tt = 0; tt < 2; ++tt) {
      const int tok = 32 * r + 16 * tt + l15;
      u32x2 pos[5], neg[5];
      features<5>(smem, g.acc, tt, stat_of(smem, tok), ap, lane, pos, neg);
#pragma unroll
      for (int t = 0; t < 5; ++t) {
        *(u32x2*)(smem + E_PHI + (t * 128 + tok) * 32 + lg * 8) = pos[t];
        *(u32x2*)(smem + E_PHI + ((5 + t) * 128 + tok) * 32 + lg * 8) = neg[t];
      }
    }
  } else {
    f32x2 sv[2][4];
#pragma unroll
    for (int tt = 0; tt < 2; ++tt)
#pragma unroll
      for (int j = 0; j < 4; ++j) sv[tt][j] = stat_of(smem, 32 * r + 16 * tt + 4 * lg + j);
#pragma unroll
    for (int t = 0; t < 5; ++t) {
      float c1 = *(const float*)(smem + L_CS + ((5 + t) * 16 + l15) * 4);
      unsigned o[4];
#pragma unroll
      for (int tt = 0; tt < 2; ++tt) {
        float y0 = fmaf(g.acc[t][tt][0], sv[tt][0][0], sv[tt][0][1] * c1);
        float y1 = fmaf(g.acc[t][tt][1], sv[tt][1][0], sv[tt][1][1] * c1);
        float y2 = fmaf(g.acc[t][tt][2], sv[tt][2][0], sv[tt][2][1] * c1);
        float y3 = fmaf(g.acc[t][tt][3], sv[tt][3][0], sv[tt][3][1] * c1);
        o[2 * tt] = pack2(y0, y1); o[2 * tt + 1] = pack2(y2, y3);
      }
      *(u32x4*)(smem + E_V + (r * 5 + t) * 1024 + lane * 16) = u32x4{o[0], o[1], o[2], o[3]};
    }
  }
  waitl();
  __builtin_amdgcn_s_barrier();
  __builtin_amdgcn_sched_barrier(0);
  STAMP(4);
  {
    const unsigned pa = E_PHI + (4 * lg + (l15 >> 2)) * 32 + (l15 & 3) * 8;
    const u32x4 one4 = {0x3f803f80u, 0x3f803f80u, 0x3f803f80u, 0x3f803f80u};
    const bool valid = (l15 >> 2) == lg;
    f32x4 a[3][3];
    const char* qa[3]; const char* vb[3]; bool hf[3];
#pragma unroll
    for (int k3 = 0; k3 < 3; ++k3) {
      const int un0 = w + 8 * k3, un = un0 < 20 ? un0 : 19;
      hf[k3] = un & 1;
      qa[k3] = smem + pa + (un >> 1) * 4096;
      vb[k3] = smem + E_V + (un & 1) * 3072 + lane * 16;
#pragma unroll
      for (int j = 0; j < 3; ++j) a[k3][j] = f32x4{0, 0, 0, 0};
    }
#pragma unroll
    for (int ks = 0; ks < 4; ++ks)
#pragma unroll
      for (int k3 = 0; k3 < 3; ++k3) {
        bf16x8 af = tr2(qa[k3] + ks * 1024, qa[k3] + ks * 1024 + 512);
        const char* v = vb[k3] + ks * 5120;
        a[k3][0] = MFMA16(af, *(const bf16x8*)(v), a[k3][0]);
        a[k3][1] = MFMA16(af, *(const bf16x8*)(v + 1024), a[k3][1]);
        u32x4 b2 = *(const u32x4*)(v + (hf[k3] ? 0 : 2048));
        b2 = hf[k3] ? one4 : b2;
        a[k3][2] = MFMA16(af, __builtin_bit_cast(bf16x8, b2), a[k3][2]);
      }
#pragma unroll
    for (int k3 = 0; k3 < 3; ++k3) {
      const int un0 = w + 8 * k3;
      unsigned dst = (valid && un0 < 20) ? OFF_KVP + ((g8 * 16 + rb) * 60 + un0 * 3) * 256 + (lg * 4 + (l15 & 3)) * 16 : OOB;
#pragma unroll
      for (int j = 0; j < 3; ++j) st16(rs, dst + j * 256, __builtin_bit_cast(u32x4, a[k3][j]));
    }
  }
  STAMP(5);
  float cs0 = 0.f, csd = 0.f;
  Frags<2> f0, f1;
  g2.template wait_chunks<2, 9>();
  waitl();
  __builtin_amdgcn_s_barrier();
  __builtin_amdgcn_sched_barrier(0);
  g2.issue(3); g2.issue(4);
  g2.rd_all(f0, 0, 0);
  int stage = 0;
  const bool is_red = C == 1 && w == 7 && rb < 15;
  bool red_done = !is_red;
  const char* kvsrc = p.ws + OFF_KVP + g8 * 16 * 15360 + rb * 1024 + lane * 16;
  auto red_finish = [&](const u32x4 (&kv)[16], bool on) {
    f32x4 a = __builtin_bit_cast(f32x4, kv[0]);
#pragma unroll
    for (int i = 1; i < 16; ++i) a += __builtin_bit_cast(f32x4, kv[i]);
    int tau = rb * 4 + lg, m = (tau * 43) >> 8, tn = tau - 6 * m;
    int sg = m >= 5 ? 1 : 0, t = m - 5 * sg;
    int ks = t < 4 ? sg * 2 + (t >> 1) : 4, hf = t < 4 ? (t & 1) : sg;
    st8(rs, on ? OFF_KVH + g8 * 7680 + (tn * 5 + ks) * 256 + l15 * 16 + hf * 8 : OOB, u32x2{pack2(a[0], a[1]), pack2(a[2], a[3])});
  };
  auto iter_a = [&]() {
    waitl();
    __builtin_amdgcn_sched_barrier(0);
    g2.kstep(f0, f1, stage, 1, true, false, 0);
    if (r < 2) { if (r == 0) dots2(cs0, csd, f0.w[0], f0.w[0]); else dots2(cs0, csd, f0.w[1], f0.w[1]); }
  };
  auto iter_bar = [&]() {
    waitl();
    __builtin_amdgcn_s_barrier();
    __builtin_amdgcn_sched_barrier(0);
  };
  auto iter_b = [&](bool do_issue) {
    int nstage = stage == 4 ? 0 : stage + 1;
    g2.kstep(f1, f0, nstage, 0, true, do_issue, stage);
    if (r < 2) { if (r == 0) dots2(cs0, csd, f1.w[0], f1.w[0]); else dots2(cs0, csd, f1.w[1], f1.w[1]); }
    stage = nstage;
  };
  const int nloop = is_red ? 4 : 9;
#pragma unroll 1
  for (int i = 0; i < nloop; ++i) {
    iter_a();
    if (i < 2) g2.template wait_chunks<3, 9>();
    else if (i < 6) g2.template wait_chunks<3, 0>();
    else if (i == 6) g2.template wait_chunks<2, 0>();
    else if (i == 7) g2.template wait_chunks<1, 0>();
    else waitv<0>();
    iter_bar();
    if (i == 2 && w == 0 && lane == 0) st_flag(p.ws + F_KVP + (g8 * 16 + rb) * 8, tag);
    iter_b(i < 5);
  }
  if (C == 1 && is_red) {
    u32x2 kf;
    u32x4 kv[16];
    iter_a(); g2.template wait_chunks<3, 0>(); iter_bar();
    iter_b(true);
    iter_a(); g2.template wait_chunks<3, 0>(); iter_bar();
    asm volatile("global_load_dwordx2 %0, %1, off sc1" : "=v"(kf) : "v"(p.ws + F_KVP + g8 * 128 + l15 * 8) : "memory");
    iter_b(false);
    iter_a(); waitv<0>(); iter_bar();
    asm volatile("" : "+v"(kf));
    red_done = __all(kf[0] == (unsigned)tag && kf[1] == (unsigned)(tag >> 32));
#pragma unroll
    for (int i = 0; i < 16; ++i) asm volatile("global_load_dwordx4 %0, %1, off sc1" : "=v"(kv[i]) : "v"(kvsrc + i * 15360) : "memory");
    iter_b(false);
    iter_a(); iter_bar();
    iter_b(false);
    iter_a(); waitv<0>(); iter_bar();
#pragma unroll
    for (int i = 0; i < 16; ++i) asm volatile("" : "+v"(kv[i]));
    red_finish(kv, red_done);
    iter_b(false);
  }
  waitl();
  __builtin_amdgcn_sched_barrier(0);
  g2.kstep(f0, f1, stage, 1, true, false, 0);
  if (r < 2) { if (r == 0) dots2(cs0, csd, f0.w[0], f0.w[0]); else dots2(cs0, csd, f0.w[1], f0.w[1]); }
  waitl();
  __builtin_amdgcn_sched_barrier(0);
  g2.kstep(f1, f0, 0, 0, false, false, 0);
  if (r < 2) { if (r == 0) dots2(cs0, csd, f1.w[0], f1.w[0]); else dots2(cs0, csd, f1.w[1], f1.w[1]); }
  {
    float v = rscatter_add(cs0, 0.f, 0.f, 0.f);
    if (r < 2 && lg == 0) *(float*)(smem + L_CS + ((10 + 2 * C + r) * 16 + l15) * 4) = v;
  }
  waitl();
  __builtin_amdgcn_s_barrier();
  __builtin_amdgcn_sched_barrier(0);
  STAMP(6);
  {
    f32x2 st[2] = {stat_of(smem, 32 * r + l15), stat_of(smem, 32 * r + 16 + l15)};
    mish_pair(rs, smem, g2.acc[0], g2.acc[1], 10 + 2 * C, 2 * C, t0w + 2 * C, false, st, rb * 8 + 2 * r, lane);
  }
  if (C == 1 && is_red && red_done) {
    drain();
    if (lane == 0) st_flag(p.ws + F_KVH + (g8 * 16 + rb) * 8, tag);
  }
  if (C == 1 && !red_done) {
    polln(p.ws + F_KVP + g8 * 128, 16, tag, lane);
    u32x4 kv[16];
#pragma unroll
    for (int i = 0; i < 16; ++i) kv[i] = ld16(rs, OFF_KVP + (g8 * 16 + i) * 15360 + rb * 1024 + lane * 16);
    red_finish(kv, true);
    drain();
    if (lane == 0) st_flag(p.ws + F_KVH + (g8 * 16 + rb) * 8, tag);
  }
  drain();
  if (arrive(smem, 0, 8, lane) && lane == 0) st_flag(p.ws + F_ACT + (rb * 16 + g8) * 8, tag);
  return tag;
}

template <int C>
DI u64 body_b(const P1& p, rsrc_t rs, char* smem, int rb, int g8, int w, int lane STAMP_ARG) {
  const int r = w & 3, lg = lane >> 4, l15 = lane & 15;
  const int t0w = min(32 + 10 * g8, 98);
  Gemm<2, C> g;
  Gemm<1, C> g2;
  const u64 tag = first_pass<1, C>(g, g2, p, rs, smem, rb, g8, t0w, w, lane STAMP_PASS);
  f32x2 st[2] = {stat_of(smem, 32 * r + l15), stat_of(smem, 32 * r + 16 + l15)};
  if constexpr (C == 1) {
    __builtin_amdgcn_s_setprio(2);
#pragma unroll
    for (int pp = 0; pp < 4; ++pp)
      mish_pair(rs, smem, g.acc[2 * pp], g.acc[2 * pp + 1], 7 + 2 * pp, 2 + 2 * pp, t0w + 2 + 2 * pp, g8 == 7 && pp == 0, st, rb * 8 + 2 * r, lane);
    __builtin_amdgcn_s_setprio(0);
    STAMP(4);
    drain();
    if (arrive(smem, 0, 8, lane) && lane == 0) st_flag(p.ws + F_ACT + (rb * 16 + 8 + g8) * 8, tag);
    STAMP(5);
  } else {
    mish_pair(rs, smem, g.acc[5], g.acc[6], 5, 0, t0w, g8 == 7, st, rb * 8 + 2 * r, lane);
    STAMP(4);
    if (w == 0 && lane < 30) *(u32x4*)(smem + E_KV + lane * 272 + 256) = u32x4{0u, 0u, 0u, 0u};
    bf16x8 ap[5][3];
    load_aphi(smem, lane, ap);
    bf16x8 ph[2][5];
#pragma unroll
    for (int tt = 0; tt < 2; ++tt) {
      u32x2 pos[5], neg[5];
      features<7>(smem, g.acc, tt, st[tt], ap, lane, pos, neg);
      ph[tt][0] = mk8(pos[0], pos[1]); ph[tt][1] = mk8(pos[2], pos[3]);
      ph[tt][2] = mk8(neg[0], neg[1]); ph[tt][3] = mk8(neg[2], neg[3]); ph[tt][4] = mk8(pos[4], neg[4]);
      if (tt == 0) {
        drain();
        if (arrive(smem, 0, 8, lane) && lane == 0) st_flag(p.ws + F_ACT + (rb * 16 + 8 + g8) * 8, tag);
      }
    }
    STAMP(5);
    if (w == 0) { polln(p.ws + F_KVH + g8 * 128, 15, tag, lane); set_cnt(smem, 4, lane); }
    else wait_cnt(smem, 4, 1);
    {
      int e0 = w * 120 + lane, e1 = e0 + 64;
      u32x4 a = ld16(rs, OFF_KVH + g8 * 7680 + e0 * 16);
      u32x4 b = ld16(rs, lane < 56 ? OFF_KVH + g8 * 7680 + e1 * 16 : OOB);
      *(u32x4*)(smem + E_KV + (e0 >> 4) * 272 + (e0 & 15) * 16) = a;
      if (lane < 56) *(u32x4*)(smem + E_KV + (e1 >> 4) * 272 + (e1 & 15) * 16) = b;
    }
    waitl();
    asm volatile("" ::: "memory");
    (void)arrive(smem, 1, 4, lane);
    wait_cnt(smem, 1, 4);
    STAMP(6);
    const char* ks_ = smem + E_KV + (((l15 >> 2) == lg) ? (lg * 4 + (l15 & 3)) * 16 : 256);
    f32x4 n[2][6];
#pragma unroll
    for (int tn = 0; tn < 6; ++tn) {
      n[0][tn] = f32x4{0, 0, 0, 0}; n[1][tn] = f32x4{0, 0, 0, 0};
#pragma unroll
      for (int ks = 0; ks < 5; ++ks) {
        bf16x8 ka = *(const bf16x8*)(ks_ + (tn * 5 + ks) * 272);
        n[0][tn] = MFMA16(ka, ph[0][ks], n[0][tn]);
        n[1][tn] = MFMA16(ka, ph[1][ks], n[1][tn]);
      }
    }
#pragma unroll
    for (int tt = 0; tt < 2; ++tt) {
      const f32x2 rc = splat2(__builtin_amdgcn_rcpf(n[tt][5][0] + 1e-6f));
      u32x2 o[5];
#pragma unroll
      for (int t = 0; t < 5; ++t) o[t] = u32x2{pack2v(lo2(n[tt][t]) * rc), pack2v(hi2(n[tt][t]) * rc)};
      const unsigned ob = OFF_O + ((rb * 8 + 2 * r + tt) * 20 + ((5 * g8) >> 1)) * 1024 + lane * 16;
      if ((g8 & 1) == 0) {
        st16(rs, ob, u32x4{o[0][0], o[0][1], o[1][0], o[1][1]});
        st16(rs, ob + 1024, u32x4{o[2][0], o[2][1], o[3][0], o[3][1]});
        st8(rs, ob + 2048, o[4]);
      } else {
        st8(rs, ob + 8, o[0]);
        st16(rs, ob + 1024, u32x4{o[1][0], o[1][1], o[2][0], o[2][1]});
        st16(rs, ob + 2048, u32x4{o[3][0], o[3][1], o[4][0], o[4][1]});
      }
    }
    drain();
    if (arrive(smem, 2, 4, lane) && lane == 0) st_flag(p.ws + F_O + (rb * 16 + g8) * 8, tag);
  }
  return tag;
}

#define GLD(dst, ptr, off) asm volatile("global_load_dwordx4 %0, %1, off offset:" #off " sc1" : "=v"(dst) : "v"(ptr) : "memory")
#define WAITV(n) do { asm volatile("s_waitcnt vmcnt(%0)" :: "i"(n) : "memory"); __builtin_amdgcn_sched_barrier(0); } while (0)

template <int NST, int NA, int NB, int S>
DI void p2_steps(const char*& pa, const char*& pa1, const char*& pa2, const char*& pa3, const char*& pb,
                 u32x4 (&A)[NA][4], u32x4 (&B)[NB][5], f32x4 (&acc)[5][4]) {
  if constexpr (S < NST) {
    constexpr int ja = S - NA, jb = S - NB;
    auto cnt = [](int j0, bool skipA) constexpr {
      int n = 0;
      for (int j = j0; j < S; ++j) {
        if (j + NA < NST && !(skipA && j == j0)) n += 4;
        if (j + NB < NST) n += 5;
      }
      return n;
    };
    constexpr int ya = ja < 0 ? 4 * ((NA < NST ? NA : NST) - 1 - S) + cnt(0, false) : cnt(ja, true);
    constexpr int yb = jb < 0 ? 1000 : cnt(jb + 1, false);
    constexpr int y = ya < yb ? ya : yb;
    WAITV(y);
#pragma unroll
    for (int i = 0; i < 4; ++i) asm volatile("" : "+v"(A[S % NA][i]));
#pragma unroll
    for (int i = 0; i < 5; ++i) asm volatile("" : "+v"(B[S % NB][i]));
#pragma unroll
    for (int n = 0; n < 5; ++n)
#pragma unroll
      for (int t = 0; t < 4; ++t)
        acc[n][t] = MFMA16(__builtin_bit_cast(bf16x8, B[S % NB][n]), __builtin_bit_cast(bf16x8, A[S % NA][t]), acc[n][t]);
    __builtin_amdgcn_sched_barrier(0);
    if constexpr (S + NA < NST) {
      GLD(A[S % NA][0], pa, 0); GLD(A[S % NA][1], pa1, 0); GLD(A[S % NA][2], pa2, 0); GLD(A[S % NA][3], pa3, 0);
      pa += 1024; pa1 += 1024; pa2 += 1024; pa3 += 1024;
    }
    if constexpr (S + NB < NST) {
      GLD(B[S % NB][0], pb, -2048); GLD(B[S % NB][1], pb, -1024); GLD(B[S % NB][2], pb, 0); GLD(B[S % NB][3], pb, 1024); GLD(B[S % NB][4], pb, 2048);
      pb += 5120;
    }
    p2_steps<NST, NA, NB, S + 1>(pa, pa1, pa2, pa3, pb, A, B, acc);
  }
}
#define ISSUE_A(A, s) do { GLD(A[s][0], pa, 0); GLD(A[s][1], pa1, 0); GLD(A[s][2], pa2, 0); GLD(A[s][3], pa3, 0); \
    pa += 1024; pa1 += 1024; pa2 += 1024; pa3 += 1024; } while (0)
#define ISSUE_B(B, s) do { GLD(B[s][0], pb, -2048); GLD(B[s][1], pb, -1024); GLD(B[s][2], pb, 0); GLD(B[s][3], pb, 1024); GLD(B[s][4], pb, 2048); \
    pb += 5120; } while (0)
DI void wait_flags(char* smem, int cnt, bool poller, const char* flags, int n, u64 tag, int lane) {
  if (poller) { polln(flags, n, tag, lane); set_cnt(smem, cnt, lane); }
  else { wait_cnt(smem, cnt, 1); drain(); }
}
template <int NST, int NA, int NB>
DI void stream(const char* pa, unsigned astr, const char* pb, char* smem, int cnt, bool poller, const char* flags, int nflags, u64 tag, int lane,
               f32x4 (&acc)[5][4]) {
  const char* pa1 = pa + astr; const char* pa2 = pa1 + astr; const char* pa3 = pa2 + astr;
  u32x4 A[NA][4], B[NB][5];
#pragma unroll
  for (int s = 0; s < NB; ++s) ISSUE_B(B, s);
  wait_flags(smem, cnt, poller, flags, nflags, tag, lane);
#pragma unroll
  for (int s = 0; s < NA; ++s) ISSUE_A(A, s);
  p2_steps<NST, NA, NB, 0>(pa, pa1, pa2, pa3, pb, A, B, acc);
}

__global__ __launch_bounds__(512, 2) void k_fused(P1 p) {
  extern __shared__ __attribute__((aligned(16))) char smem[];
  const int b = blockIdx.x, xcd = b & 7, idx = b >> 3;
  const int w = __builtin_amdgcn_readfirstlane(threadIdx.x >> 6), lane = threadIdx.x & 63;
  const int rb = (xcd >> 2) * 8 + (idx >> 2);
  const int jl = idx & 3, xc = xcd & 3;
  const int type = jl >> 1, g8 = 2 * xc + (jl & 1);
  const int c = w >> 2;
  STAMP_DECL; STAMP(0);
  const rsrc_t rs = __builtin_amdgcn_make_buffer_rsrc(p.ws, 0, WS_USED, 0x00020000);
  if (w == CODE_W) {
    u64 pc;
    asm volatile("s_getpc_b64 %0" : "=s"(pc));
    const char* q = (const char*)(pc & ~127ull) + lane * 128;
#pragma unroll
    for (int j = 0; j < CODE_KB / 8; ++j) touch(q + j * 8192, lds_addr(smem) + L_PT + 12288);
  }
  u64 tag;
  if (type == 0) {
    if (c == 0) tag = body_a<0>(p, rs, smem, rb, g8, w, lane STAMP_PASS);
    else tag = body_a<1>(p, rs, smem, rb, g8, w, lane STAMP_PASS);
  } else {
    if (c == 0) tag = body_b<0>(p, rs, smem, rb, g8, w, lane STAMP_PASS);
    else tag = body_b<1>(p, rs, smem, rb, g8, w, lane STAMP_PASS);
  }
  const int lg = lane >> 4, l15 = lane & 15;
  STAMP(7);
  if (w == 5) {
    unsigned v = __hip_atomic_load((LDS3 unsigned*)(smem + L_CNT + 12), __ATOMIC_RELAXED, __HIP_MEMORY_SCOPE_WORKGROUP);
    if (__builtin_amdgcn_readfirstlane(v) == 0) {
      poll256(p.ws + F_EARLY, tag, lane);
      if (blockIdx.x == 0 && lane == 0) st_flag(p.ws + OFF_FLG, tag);
      set_cnt(smem, 3, lane);
    }
  } else wait_cnt(smem, 3, 1);
  const int rt = DMAP == 0 ? (xcd >> 1) * 8 + (idx >> 2) : xcd * 4 + (idx >> 3);
  const int ct = DMAP == 0 ? (xcd & 1) * 4 + (idx & 3) : (idx & 7);
  u32x2 xv[3];
  const unsigned o0 = (unsigned)((rt * 64 + (w & 3) * 16 + l15) * 640 + ct * 80 + (w >> 2) * 16 + 4 * lg);
#pragma unroll
  for (int m = 0; m < 3; ++m) xv[m] = *(const u32x2*)(p.x + o0 + ((m < 2 || w < 4) ? 32 * m : 0));
#pragma unroll
  for (int m = 0; m < 3; ++m) asm volatile("" : "+v"(xv[m]));
  f32x4 acc[5][4];
#pragma unroll
  for (int n = 0; n < 5; ++n)
#pragma unroll
    for (int t = 0; t < 4; ++t) acc[n][t] = f32x4{0, 0, 0, 0};
  const char* fact = p.ws + F_ACT + (rt >> 1) * 128;
  if (w >= 4) {
    const char* pa = p.ws + OFF_ACT + (rt * 4) * 54 * 1024 + lane * 16;
    const char* pb = p.ws + OFF_W2F + ct * 74 * 5120 + lane * 16 + 2048;
    const int j = w - 4;
    const int u1 = 16 + 10 * j - (j == 3 ? 1 : 0);
    if (j < 2) stream<10, 5, 3>(pa + u1 * 1024, 54 * 1024, pb + u1 * 5120, smem, 5, w == 4, fact + 64, 8, tag, lane, acc);
    else stream<9, 5, 3>(pa + u1 * 1024, 54 * 1024, pb + u1 * 5120, smem, 5, false, fact + 64, 8, tag, lane, acc);
    STAMP(8);
    stream<4, 4, 4>(pa + 4 * j * 1024, 54 * 1024, pb + 4 * j * 5120, smem, 6, w == 4, fact, 8, tag, lane, acc);
  } else {
    stream<5, 4, 4>(p.ws + OFF_O + ((rt * 4) * 20 + 5 * w) * 1024 + lane * 16, 20 * 1024,
                    p.ws + OFF_W2F + (ct * 74 + 54 + 5 * w) * 5120 + lane * 16 + 2048, smem, 7, w == 0, p.ws + F_O + (rt >> 1) * 128, 8, tag, lane, acc);
    if (type == 1) wait_cnt(smem, 2, 4);
  }
  STAMP(9);
  {
    char* dst = smem + w * 18432 + lane * 16;
#pragma unroll
    for (int tile = 0; tile < 20; ++tile) {
      int pos = tile - (tile > w) - (tile > w + 8) - (tile > w + 16);
      if ((tile & 7) != w) *(f32x4*)(dst + pos * 1024) = acc[tile >> 2][tile & 3];
    }
  }
  f32x4 own[3];
#pragma unroll
  for (int m = 0; m < 3; ++m) {
    own[m] = f32x4{0, 0, 0, 0};
#pragma unroll
    for (int k = 0; k < 8; ++k)
      if (8 * m + k < 20 && k == w) own[m] = acc[(8 * m + k) >> 2][(8 * m + k) & 3];
  }
  __syncthreads();
  STAMP(10);
#pragma unroll
  for (int m = 0; m < 3; ++m) {
    const int tile = w + 8 * m;
    if (tile < 20) {
      f32x4 part[8];
#pragma unroll
      for (int v = 0; v < 8; ++v) {
        int pos = tile - (tile > v) - (tile > v + 8) - (tile > v + 16);
        part[v] = *(const f32x4*)(smem + v * 18432 + pos * 1024 + lane * 16);
        if (v == w) part[v] = own[m];
      }
      f32x4 ssum = (part[0] + part[1]) + (part[2] + part[3]);
      f32x4 bsum = (part[4] + part[5]) + (part[6] + part[7]);
      float xs[4] = {bf_lo(xv[m][0]), bf_hi(xv[m][0]), bf_lo(xv[m][1]), bf_hi(xv[m][1])};
      float r[4];
#pragma unroll
      for (int j = 0; j < 4; ++j) {
        float bb = bsum[j];
        float g = bb * __builtin_amdgcn_rcpf(1.0f + __builtin_amdgcn_exp2f(-bb * LOG2E));
        r[j] = fmaf(ssum[j], g, xs[j]);
      }
      *(u32x2*)(p.out + o0 + 32 * m) = u32x2{pack2(r[0], r[1]), pack2(r[2], r[3])};
    }
  }
  STAMP(11);
  STAMP_FLUSH(p.ws, blockIdx.x * 8 + w);
}
}

extern "C" void kernel_launch(void* const* d_in, const int* in_sizes, int n_in, void* d_out, int out_size, void* d_ws,
                              size_t ws_size, hipStream_t stream) {
  using namespace hk;
  P1 p;
  p.x = (const u16*)d_in[0]; p.wqkv = (const u16*)d_in[1]; p.wphi = (const u16*)d_in[2]; p.wo = (const u16*)d_in[3];
  p.w1 = (const u16*)d_in[5]; p.b1 = (const u16*)d_in[6]; p.w2 = (const u16*)d_in[7]; p.ws = (char*)d_ws; p.out = (u16*)d_out;
  (void)hipFuncSetAttribute((const void*)k_fused, hipFuncAttributeMaxDynamicSharedMemorySize, L_END);
  k_fused<<<256, 512, L_END, stream>>>(p);
}
```

```cpp
#include <hip/hip_runtime.h>
#include <hip/hip_bf16.h>

typedef unsigned short u16;
typedef __bf16 bf16x2 __attribute__((ext_vector_type(2)));
typedef __bf16 bf16x4 __attribute__((ext_vector_type(4)));
typedef __bf16 bf16x8 __attribute__((ext_vector_type(8)));
typedef float f32x2 __attribute__((ext_vector_type(2)));
typedef float f32x4 __attribute__((ext_vector_type(4)));
typedef unsigned u32x2 __attribute__((ext_vector_type(2)));
typedef unsigned u32x4 __attribute__((ext_vector_type(4)));
#define LDS3 __attribute__((address_space(3)))
#define DI __device__ __forceinline__
#define MFMA16(a, b, c) __builtin_amdgcn_mfma_f32_16x16x32_bf16((a), (b), (c), 0, 0, 0)

#define STAMP(i)
#define STAMP_DECL
#define STAMP_FLUSH(ws, slot)
namespace hk {
typedef unsigned long long u64;
constexpr float LOG2E = 1.4426950408889634f;
constexpr float LN2 = 0.6931471805599453f;
#define ROTP 2
#define DMAP 0
constexpr int FLAG_I = ROTP + 2 > 8 ? 8 : ROTP + 2;
constexpr unsigned OFF_ACT = 0;
constexpr unsigned OFF_O = OFF_ACT + 128 * 54 * 1024;
constexpr unsigned OFF_W2F = OFF_O + 128 * 20 * 1024;
constexpr unsigned OFF_KVP = OFF_W2F + 8 * 74 * 5 * 1024;
constexpr unsigned OFF_KVH = OFF_KVP + 8 * 16 * 60 * 256;
constexpr unsigned OFF_STP = OFF_KVH + 8 * 30 * 256;
constexpr unsigned OFF_CSP = OFF_STP + 16 * 12800;
constexpr unsigned OFF_FLG = OFF_CSP + 16 * 12800;
constexpr unsigned F_EARLY = OFF_FLG + 128;
constexpr unsigned F_ACT = F_EARLY + 2048;
constexpr unsigned F_KVP = F_ACT + 2048;
constexpr unsigned F_KVH = F_KVP + 1024;
constexpr unsigned F_O = F_KVH + 1024;
constexpr unsigned F_ST = F_O + 2048;
constexpr unsigned F_CS = F_ST + 2048;
constexpr unsigned WS_USED = F_CS + 2048;
constexpr unsigned OOB = 0x7ffff000u;
constexpr int XB = 16384, WB0 = 3 * XB;
constexpr int L_PT = 118784;
constexpr int L_DUMMY = 151552;
constexpr int L_RAW = 155648;
constexpr int L_BIAS = L_RAW + 3200;
constexpr int L_ZERO = L_BIAS + 448;
constexpr int L_CNT = L_ZERO + 64;
constexpr int L_BIASL = L_CNT + 64;
constexpr int L_CS = L_BIASL + 896;
constexpr int L_SUM = L_CS + 1216;
constexpr int L_END = 163840;
static_assert(L_SUM + 1024 <= L_END - 256, "lds");
constexpr int E_PHI = 0;
constexpr int E_V = 40960;
constexpr int E_KB = 61440;
constexpr int E_KV = 0;

DI unsigned pack2(float a, float b) {
  f32x2 v = {a, b};
  return __builtin_bit_cast(unsigned, __builtin_convertvector(v, bf16x2));
}
DI unsigned pack2v(f32x2 v) { return __builtin_bit_cast(unsigned, __builtin_convertvector(v, bf16x2)); }
DI bf16x2 as_bf2(unsigned u) { return __builtin_bit_cast(bf16x2, u); }
DI float bf_lo(unsigned u) { return __uint_as_float(u << 16); }
DI float bf_hi(unsigned u) { return __uint_as_float(u & 0xffff0000u); }
DI float rscatter_add(float m0, float m1, float m2, float m3) {
  auto a = __builtin_amdgcn_permlane16_swap(__float_as_uint(m0), __float_as_uint(m1), false, false);
  auto b = __builtin_amdgcn_permlane16_swap(__float_as_uint(m2), __float_as_uint(m3), false, false);
  float A = __uint_as_float(a[0]) + __uint_as_float(a[1]);
  float B = __uint_as_float(b[0]) + __uint_as_float(b[1]);
  auto c = __builtin_amdgcn_permlane32_swap(__float_as_uint(A), __float_as_uint(B), false, false);
  return __uint_as_float(c[0]) + __uint_as_float(c[1]);
}
DI f32x2 lo2(f32x4 v) { return __builtin_shufflevector(v, v, 0, 1); }
DI f32x2 hi2(f32x4 v) { return __builtin_shufflevector(v, v, 2, 3); }
DI f32x2 fma2(f32x2 a, f32x2 b, f32x2 c) { return __builtin_elementwise_fma(a, b, c); }
DI f32x2 splat2(float a) { return f32x2{a, a}; }
DI f32x2 exp2v(f32x2 a) { return f32x2{__builtin_amdgcn_exp2f(a[0]), __builtin_amdgcn_exp2f(a[1])}; }
DI f32x2 rcpv(f32x2 a) { return f32x2{__builtin_amdgcn_rcpf(a[0]), __builtin_amdgcn_rcpf(a[1])}; }
DI void dual_softmax(const f32x4 (&p)[5], u32x2 (&pos)[5], u32x2 (&neg)[5]) {
  float mx = fmaxf(p[0][0], p[0][1]), mn = fminf(p[0][0], p[0][1]);
  mx = fmaxf(fmaxf(mx, p[0][2]), p[0][3]); mn = fminf(fminf(mn, p[0][2]), p[0][3]);
#pragma unroll
  for (int t = 1; t < 5; ++t) {
    mx = fmaxf(fmaxf(mx, p[t][0]), p[t][1]); mx = fmaxf(fmaxf(mx, p[t][2]), p[t][3]);
    mn = fminf(fminf(mn, p[t][0]), p[t][1]); mn = fminf(fminf(mn, p[t][2]), p[t][3]);
  }
  const f32x2 L = splat2(LOG2E), nL = splat2(-LOG2E), cx = splat2(-mx * LOG2E), cn = splat2(mn * LOG2E);
  f32x2 ep[10], en[10];
#pragma unroll
  for (int t = 0; t < 5; ++t) {
    ep[2 * t] = exp2v(fma2(lo2(p[t]), L, cx)); ep[2 * t + 1] = exp2v(fma2(hi2(p[t]), L, cx));
    en[2 * t] = exp2v(fma2(lo2(p[t]), nL, cn)); en[2 * t + 1] = exp2v(fma2(hi2(p[t]), nL, cn));
  }
  f32x2 sp = ep[0], sn = en[0];
#pragma unroll
  for (int i = 1; i < 10; ++i) { sp += ep[i]; sn += en[i]; }
  const f32x2 ip = splat2(__builtin_amdgcn_rcpf(sp[0] + sp[1])), in_ = splat2(__builtin_amdgcn_rcpf(sn[0] + sn[1]));
#pragma unroll
  for (int t = 0; t < 5; ++t) {
    pos[t] = u32x2{pack2v(ep[2 * t] * ip), pack2v(ep[2 * t + 1] * ip)};
    neg[t] = u32x2{pack2v(en[2 * t] * in_), pack2v(en[2 * t + 1] * in_)};
  }
}
DI f32x2 mish2(f32x2 hl) {
  f32x2 e = exp2v(hl);
  f32x2 d = fma2(e, e + splat2(2.0f), splat2(2.0f));
  f32x2 t = fma2(rcpv(d), splat2(-2.0f * LN2), splat2(LN2));
  return hl * t;
}
DI bf16x8 mk8(u32x2 a, u32x2 b) {
  u32x4 v = {a[0], a[1], b[0], b[1]};
  return __builtin_bit_cast(bf16x8, v);
}
DI bf16x8 tr2(const char* a, const char* b) {
  bf16x4 lo = __builtin_amdgcn_ds_read_tr16_b64_v4bf16((LDS3 bf16x4*)(a));
  bf16x4 hi = __builtin_amdgcn_ds_read_tr16_b64_v4bf16((LDS3 bf16x4*)(b));
  return __builtin_shufflevector(lo, hi, 0, 1, 2, 3, 4, 5, 6, 7);
}

struct P1 {
  const u16* x; const u16* wqkv; const u16* wphi; const u16* wo; const u16* w1; const u16* b1; const u16* w2;
  char* ws; u16* out;
};
typedef __amdgpu_buffer_rsrc_t rsrc_t;

DI unsigned lds_addr(const void* p) { return (unsigned)(size_t)(LDS3 const char*)p; }
DI void glds1(const void* g, unsigned lds) {
  unsigned keep;
  asm volatile("s_mov_b32 %0, m0\n\ts_mov_b32 m0, %2\n\ts_nop 0\n\tglobal_load_lds_dwordx4 %1, off\n\ts_mov_b32 m0, %0"
               : "=&s"(keep) : "v"(g), "s"(lds) : "memory");
}
DI void touch(const void* g, unsigned lds) {
  unsigned keep;
  asm volatile("s_mov_b32 %0, m0\n\ts_mov_b32 m0, %2\n\ts_nop 0\n\tglobal_load_lds_dword %1, off\n\ts_mov_b32 m0, %0"
               : "=&s"(keep) : "v"(g), "s"(lds) : "memory");
}
DI u64 next_tag(u64 e) { return e * 0x9E3779B97F4A7C15ull + 0x632BE59BD9B4E019ull; }
DI void st_flag(char* p, u64 v) { __hip_atomic_store((u64*)p, v, __ATOMIC_RELAXED, __HIP_MEMORY_SCOPE_AGENT); }
DI void st16(rsrc_t rs, unsigned off, u32x4 v) { __builtin_amdgcn_raw_buffer_store_b128(v, rs, off, 0, 16); }
DI void st8(rsrc_t rs, unsigned off, u32x2 v) { __builtin_amdgcn_raw_buffer_store_b64(v, rs, off, 0, 16); }
DI void st4(rsrc_t rs, unsigned off, float v) { __builtin_amdgcn_raw_buffer_store_b32(__float_as_uint(v), rs, off, 0, 16); }
DI u32x4 ld16(rsrc_t rs, unsigned off) { return __builtin_amdgcn_raw_buffer_load_b128(rs, off, 0, 16); }
DI bool ready(u32x4 v) { return v[0] != 0xffffffffu && v[1] != 0xffffffffu && v[2] != 0xffffffffu && v[3] != 0xffffffffu; }
DI void drain() { asm volatile("s_waitcnt vmcnt(0)" ::: "memory"); }
constexpr int SPIN_MAX = 1 << 18;
DI bool flags_ok(u32x4 a, u32x4 b, u64 tag) {
  const unsigned tl = (unsigned)tag, th = (unsigned)(tag >> 32);
  return a[0] == tl && a[1] == th && a[2] == tl && a[3] == th && b[0] == tl && b[1] == th && b[2] == tl && b[3] == th;
}
DI void poll256(const char* flags, u64 tag, int lane) {
  const char* q = flags + lane * 32;
#pragma unroll 1
  for (int spin = 0; spin < SPIN_MAX; ++spin) {
    u32x4 a, b;
    asm volatile("global_load_dwordx4 %0, %2, off sc1\n\tglobal_load_dwordx4 %1, %2, off offset:16 sc1\n\ts_waitcnt vmcnt(0)"
                 : "=&v"(a), "=&v"(b) : "v"(q) : "memory");
    if (__all(flags_ok(a, b, tag))) break;
    __builtin_amdgcn_s_sleep(8);
  }
}
DI void pollq(const char* q, u64 tag) {
#pragma unroll 1
  for (int spin = 0; spin < SPIN_MAX; ++spin) {
    u32x2 a;
    asm volatile("global_load_dwordx2 %0, %1, off sc1\n\ts_waitcnt vmcnt(0)" : "=&v"(a) : "v"(q) : "memory");
    if (__all(a[0] == (unsigned)tag && a[1] == (unsigned)(tag >> 32))) break;
    __builtin_amdgcn_s_sleep(3);
  }
}
DI void polln(const char* flags, int n, u64 tag, int lane) { pollq(flags + (lane < n ? lane : 0) * 8, tag); }
DI bool arrive(char* smem, int idx, int n, int lane) {
  unsigned old = 0;
  if (lane == 0) old = __hip_atomic_fetch_add((LDS3 unsigned*)(smem + L_CNT + idx * 4), 1u, __ATOMIC_RELAXED, __HIP_MEMORY_SCOPE_WORKGROUP);
  return __builtin_amdgcn_readfirstlane(old) == (unsigned)(n - 1);
}
DI void set_cnt(char* smem, int idx, int lane) {
  if (lane == 0) __hip_atomic_store((LDS3 unsigned*)(smem + L_CNT + idx * 4), 1u, __ATOMIC_RELAXED, __HIP_MEMORY_SCOPE_WORKGROUP);
}
DI void wait_cnt(char* smem, int idx, unsigned n) {
#pragma unroll 1
  for (int spin = 0; spin < (SPIN_MAX << 4); ++spin) {
    unsigned v = __hip_atomic_load((LDS3 unsigned*)(smem + L_CNT + idx * 4), __ATOMIC_RELAXED, __HIP_MEMORY_SCOPE_WORKGROUP);
    if (__builtin_amdgcn_readfirstlane(v) >= n) break;
    __builtin_amdgcn_s_sleep(1);
  }
  asm volatile("" ::: "memory");
}

template <int NT> struct Frags { bf16x8 x[2]; bf16x8 w[NT]; };

#define STAMP_ARG
#define STAMP_PASS
template <int N> DI void waitv() { asm volatile("s_waitcnt vmcnt(%0)" :: "i"(N) : "memory"); }
DI void waitl() { __builtin_amdgcn_s_waitcnt(0xC07F); }

template <int TYPE, int C>
struct Gemm {
  static constexpr int NT = TYPE == 0 ? 5 : (C == 0 ? 9 : 10);
  static constexpr int NU = TYPE == 0 ? 11 : 19;
  static constexpr int WROW = NU * 32, NC = NU * 2, HB = 32 * WROW;
  static constexpr int NWH = TYPE == 0 ? 6 : 5;
  static constexpr int NWS = (NU + 7) / 8;
  static constexpr int NFULL = NU - (NWS - 1) * 8;
  static constexpr int MAGIC = (65536 + NC - 1) / NC;
  static constexpr bool VW = (TYPE == 0 && C == 1);
  static constexpr int unit_of(int t) { return TYPE == 0 ? t + 5 * C : t + 9 * C; }

  const char* xsrc[2];
  const char* wsrc[NWS];
  unsigned winc[NWS];
  unsigned xa0, wa, lds0;
  int cx, hw, hslot, w;
  bool full;
  char* smem;
  f32x4 acc[NT][2];

  DI void init(const P1& p, char* smem_, int rb, int g8, int t0w, int rot, int w_, int lane) {
    smem = smem_; w = w_; cx = rot; hw = 2 * rot; hslot = 0; lds0 = lds_addr(smem_); full = w < NFULL;
    const int r = w & 3, lg = lane >> 4, l15 = lane & 15;
#pragma unroll
    for (int i = 0; i < 2; ++i) {
      int row = (i * 8 + w) * 8 + (lane >> 3);
      int ch = (lane & 7) ^ ((row >> 1) & 7);
      xsrc[i] = (const char*)p.x + (unsigned)((rb * 128 + row) * 1280 + ch * 16 + rot * 128);
    }
#pragma unroll
    for (int i = 0; i < NWS; ++i) {
      int ci = (i * 8 + w) * 64 + lane;
      ci = ci < 32 * NC ? ci : lane;
      int pos = (ci * MAGIC) >> 16, c16 = ci - pos * NC;
      int kk = pos & 15;
      int k = (kk >= 4 && kk < 12) ? (pos ^ 12) : pos;
      bool isq = TYPE == 0 || c16 < 10;
      int colb;
      if (TYPE == 0) { int c = min(c16, 19); colb = 1280 + 160 * g8 + c * 16 + (c >= 10 ? 1120 : 0); }
      else colb = isq ? (160 * g8 + c16 * 16) : (32 * t0w + (c16 - 10) * 16);
      unsigned stride = isq ? 3840u : 3456u;
      wsrc[i] = (isq ? (const char*)p.wqkv : (const char*)p.w1) + (unsigned)((k + rot * 64) * stride + colb);
      winc[i] = stride * 32;
    }
    xa0 = (32 * r + l15) * 128 + ((lg ^ ((l15 >> 1) & 7)) * 16);
    wa = WB0 + (16 * (lg >> 1) + 4 * (lg & 1) + (l15 >> 2)) * WROW + (VW ? 160 + (l15 & 3) * 40 : (l15 & 3) * 8);
#pragma unroll
    for (int t = 0; t < NT; ++t) { acc[t][0] = f32x4{0, 0, 0, 0}; acc[t][1] = f32x4{0, 0, 0, 0}; }
  }
  DI void issue_x1(int s, int xslot) {
    glds1(xsrc[s], lds0 + xslot * XB + w * 1024 + s * 0x2000);
    xsrc[s] += 128;
    if (s == 1 && ++cx == 10) { cx = 0; xsrc[0] -= 1280; xsrc[1] -= 1280; }
  }
  DI void issue_w1(int s) {
    if (s < NWS - 1 || full) { glds1(wsrc[s], lds0 + WB0 + hslot * HB + w * 1024 + s * 0x2000); wsrc[s] += winc[s]; }
    if (s == NWS - 1) {
      hslot = hslot == NWH - 1 ? 0 : hslot + 1;
      if (++hw == 20) {
        hw = 0;
#pragma unroll
        for (int i = 0; i < NWS; ++i) wsrc[i] -= 20 * winc[i];
      }
    }
  }
  DI void issue_x(int xslot) { issue_x1(0, xslot); issue_x1(1, xslot); }
  DI void issue_w() {
#pragma unroll
    for (int s = 0; s < NWS; ++s) issue_w1(s);
  }
  template <int NX, int NH, int E> DI void wait() {
    if (full) waitv<2 * NX + NH * NWS + E>(); else waitv<2 * NX + NH * (NWS - 1) + E>();
  }
  DI void rd_x(Frags<NT>& f, int xslot, int ks, int tt) { f.x[tt] = *(const bf16x8*)(smem + xslot * XB + (xa0 ^ (ks * 64)) + tt * 2048); }
  DI void rd_w(Frags<NT>& f, int hs, int t) {
    const char* q = smem + hs * HB + wa + (VW ? t * 8 : unit_of(t) * 32);
    f.w[t] = tr2(q, q + 8 * WROW);
  }
  DI void kstep(const Frags<NT>& f, Frags<NT>& g, int nxs, int nks, int nhs, bool do_rd, int nreq, int ixs) {
    constexpr int NS = 2 + 2 * NWS;
#pragma unroll
    for (int t = 0; t < NT; ++t) {
      if (do_rd) {
        if (t < 2) rd_x(g, nxs, nks, t);
        rd_w(g, nhs, t);
      }
#pragma unroll
      for (int tt = 0; tt < 2; ++tt)
        acc[t][tt] = VW ? MFMA16(f.x[tt], f.w[t], acc[t][tt]) : MFMA16(f.w[t], f.x[tt], acc[t][tt]);
#pragma unroll
      for (int s = (t * NS) / NT; s < ((t + 1) * NS) / NT; ++s) {
        if (s < NWS) { if (nreq > 0) issue_w1(s); }
        else if (s < NWS + 2) { if (nreq > 1) issue_x1(s - NWS, ixs); }
        else { if (nreq > 1) issue_w1(s - NWS - 2); }
      }
      __builtin_amdgcn_sched_barrier(0);
    }
  }
};

DI void dots2(float& c0, float& c1, const bf16x8& wa_, const bf16x8& wb_) {
  u32x4 a = __builtin_bit_cast(u32x4, wa_), b = __builtin_bit_cast(u32x4, wb_);
  unsigned a0 = a[0], a1 = a[1], a2 = a[2], a3 = a[3], b0 = b[0], b1 = b[1], b2 = b[2], b3 = b[3];
  asm volatile("s_nop 1\n\t"
               "v_dot2c_f32_bf16 %0, 0x3f803f80, %2\n\tv_dot2c_f32_bf16 %1, 0x3f803f80, %6\n\t"
               "v_dot2c_f32_bf16 %0, 0x3f803f80, %3\n\tv_dot2c_f32_bf16 %1, 0x3f803f80, %7\n\t"
               "v_dot2c_f32_bf16 %0, 0x3f803f80, %4\n\tv_dot2c_f32_bf16 %1, 0x3f803f80, %8\n\t"
               "v_dot2c_f32_bf16 %0, 0x3f803f80, %5\n\tv_dot2c_f32_bf16 %1, 0x3f803f80, %9\n\ts_nop 3"
               : "+v"(c0), "+v"(c1) : "v"(a0), "v"(a1), "v"(a2), "v"(a3), "v"(b0), "v"(b1), "v"(b2), "v"(b3));
}
DI void dots_x(float& s1, float& s2, const bf16x8& x) {
  u32x4 a = __builtin_bit_cast(u32x4, x);
  unsigned a0 = a[0], a1 = a[1], a2 = a[2], a3 = a[3];
  asm volatile("s_nop 1\n\t"
               "v_dot2c_f32_bf16 %0, 0x3f803f80, %2\n\tv_dot2c_f32_bf16 %1, %2, %2\n\t"
               "v_dot2c_f32_bf16 %0, 0x3f803f80, %3\n\tv_dot2c_f32_bf16 %1, %3, %3\n\t"
               "v_dot2c_f32_bf16 %0, 0x3f803f80, %4\n\tv_dot2c_f32_bf16 %1, %4, %4\n\t"
               "v_dot2c_f32_bf16 %0, 0x3f803f80, %5\n\tv_dot2c_f32_bf16 %1, %5, %5\n\ts_nop 3"
               : "+v"(s1), "+v"(s2) : "v"(a0), "v"(a1), "v"(a2), "v"(a3));
}
DI f32x2 stat_of(const char* smem, int tok) { return *(const f32x2*)(smem + L_SUM + tok * 8); }
DI void mish_pair(rsrc_t rs, const char* smem, const f32x4 (&a0)[2], const f32x4 (&a1)[2], int ucs, int wl, int wt, bool skip,
                  const f32x2 (&st)[2], int tt0, int lane) {
  const int lg = lane >> 4;
  f32x4 bb[2], c4[2];
#pragma unroll
  for (int h = 0; h < 2; ++h) {
    bb[h] = *(const f32x4*)(smem + L_BIASL + ((wl + h) * 16 + 4 * lg) * 4);
    c4[h] = *(const f32x4*)(smem + L_CS + ((ucs + h) * 16 + 4 * lg) * 4);
  }
#pragma unroll
  for (int tt = 0; tt < 2; ++tt) {
    const f32x2 rsL = splat2(st[tt][0] * LOG2E), nbL = splat2(st[tt][1] * LOG2E);
    u32x4 o;
    o[0] = pack2v(mish2(fma2(lo2(a0[tt]), rsL, fma2(nbL, lo2(c4[0]), lo2(bb[0])))));
    o[1] = pack2v(mish2(fma2(hi2(a0[tt]), rsL, fma2(nbL, hi2(c4[0]), hi2(bb[0])))));
    o[2] = pack2v(mish2(fma2(lo2(a1[tt]), rsL, fma2(nbL, lo2(c4[1]), lo2(bb[1])))));
    o[3] = pack2v(mish2(fma2(hi2(a1[tt]), rsL, fma2(nbL, hi2(c4[1]), hi2(bb[1])))));
    st16(rs, skip ? OOB : OFF_ACT + ((tt0 + tt) * 54 + (wt >> 1)) * 1024 + lane * 16, o);
  }
}
DI void load_aphi(const char* smem, int lane, bf16x8 (&ap)[5][3]) {
  const int lg = lane >> 4, l15 = lane & 15;
  const char* pa[6];
#pragma unroll
  for (int i = 0; i < 6; ++i) {
    int d0 = 16 * i + 4 * lg;
    int h = (d0 * 13) >> 8;
    bool ok = (h == (l15 & 3)) && i < 5;
    pa[i] = smem + (ok ? L_RAW + h * 800 + (d0 - 20 * h + (l15 >> 2)) * 40 : L_ZERO);
  }
#pragma unroll
  for (int t = 0; t < 5; ++t)
#pragma unroll
    for (int ks = 0; ks < 3; ++ks) ap[t][ks] = tr2(pa[2 * ks] + t * 8, pa[2 * ks + 1] + t * 8);
}
DI void pack_k(const char* smem, const f32x4& a, int t, f32x2 st, int lg, u32x2& kb) {
  f32x4 c4 = *(const f32x4*)(smem + L_CS + (t * 16 + 4 * lg) * 4);
  const f32x2 rsv = splat2(st[0]), nb = splat2(st[1]);
  kb = u32x2{pack2v(fma2(lo2(a), rsv, nb * lo2(c4))), pack2v(fma2(hi2(a), rsv, nb * hi2(c4)))};
}
DI void features(const u32x2 (&kb)[5], const bf16x8 (&ap)[5][3], u32x2 (&pos)[5], u32x2 (&neg)[5]) {
  bf16x8 b0 = mk8(kb[0], kb[1]), b1 = mk8(kb[2], kb[3]), b2 = mk8(kb[4], u32x2{0u, 0u});
  f32x4 pk[5];
#pragma unroll
  for (int t = 0; t < 5; ++t) pk[t] = MFMA16(ap[t][2], b2, MFMA16(ap[t][1], b1, MFMA16(ap[t][0], b0, (f32x4{0, 0, 0, 0}))));
  dual_softmax(pk, pos, neg);
}
DI void barrier() {
  waitl();
  __builtin_amdgcn_s_barrier();
  __builtin_amdgcn_sched_barrier(0);
}

template <int TYPE, int C, class G>
DI u64 first_pass(G& g, const P1& p, rsrc_t rs, char* smem, int rb, int g8, int t0w, int w, int lane STAMP_ARG) {
  constexpr int NT = G::NT, NWH = G::NWH;
  const int r = w & 3, lg = lane >> 4, l15 = lane & 15;
  const unsigned lds0 = lds_addr(smem);
  u32x2 ev;
  asm volatile("global_load_dwordx2 %0, %1, off sc1" : "=v"(ev) : "v"(p.ws + OFF_FLG) : "memory");
  const int job = TYPE * 8 + g8;
  const int myc = (rb + job) % 10;
  const int rot = (myc / ROTP) * ROTP;
  const int my_i = myc - rot;
  g.init(p, smem, rb, g8, t0w, rot, w, lane);
  if (C == 1) {
    int ci = r * 64 + lane;
    if (ci < 200) glds1((const char*)p.wphi + g8 * 3200 + ci * 16, lds0 + L_RAW + r * 1024);
  } else if (w == 3) {
    if (TYPE == 1 && lane < 28) glds1((const char*)p.b1 + t0w * 32 + lane * 16, lds0 + L_BIAS);
    if (lane < 32) *(unsigned*)(smem + L_ZERO + lane * 4) = 0u;
  }
  g.issue_w(); g.issue_x(0); g.issue_w();
  __builtin_amdgcn_s_barrier();
  g.issue_w(); g.issue_x(1); g.issue_w();
  g.issue_w(); g.issue_x(2);
  if (NWH == 6) g.issue_w();
  constexpr int AH = NWH - 4;
  const int ptj = w * 128 + rb * 8 + g8;
  const bool has_pt = TYPE == 0 && w < 6 && ptj < 740;
  const int ptu = ptj / 10, ptL = ptj - ptu * 10;
  if (has_pt) {
#pragma unroll
    for (int i = 0; i < 4; ++i) {
      int rho = i * 8 + (lane >> 3);
      const u16* src;
      if (ptu < 54) src = p.w2 + (unsigned)((32 * ptu + rho) * 640);
      else {
        int T = 2 * (ptu - 54) + (rho >> 4), Gq = (T * 13) >> 6;
        src = p.wo + (unsigned)(((4 * Gq + ((rho >> 2) & 3)) * 20 + 4 * (T - 5 * Gq) + (rho & 3)) * 640);
      }
      glds1((const char*)src + ptL * 128 + (lane & 7) * 16, lds0 + L_PT + w * 4096 + i * 1024);
    }
  }
  float cs[3] = {0.f, 0.f, 0.f}, sx[2] = {0.f, 0.f}, cd = 0.f;
  auto do_dots = [&](const Frags<NT>& f) {
    dots_x(sx[0], sx[1], f.x[C]);
    if (r == 0) { dots2(cs[0], cs[1], f.w[0], f.w[4]); if (NT > 8) dots2(cs[2], cd, f.w[NT > 8 ? 8 : 0], f.w[0]); }
    else if (r == 1) { dots2(cs[0], cs[1], f.w[1], f.w[NT > 5 ? 5 : 0]); if (NT > 9) dots2(cs[2], cd, f.w[NT > 9 ? 9 : 0], f.w[0]); }
    else if (r == 2) dots2(cs[0], cs[1], f.w[2], f.w[NT > 6 ? 6 : 0]);
    else dots2(cs[0], cs[1], f.w[3], f.w[NT > 7 ? 7 : 0]);
  };
  Frags<NT> f0, f1;
  if (has_pt) g.template wait<2, NWH - 2, 4>(); else g.template wait<2, NWH - 2, 0>();
  barrier();
  STAMP(1);
  g.rd_x(f0, 0, 0, 0); g.rd_x(f0, 0, 0, 1);
#pragma unroll
  for (int t = 0; t < NT; ++t) g.rd_w(f0, 0, t);
  if (TYPE == 1) {
    int tid = w * 64 + lane;
    if (tid < 224) *(float*)(smem + L_BIASL + tid * 4) = bf_lo(*(const u16*)(smem + L_BIAS + tid * 2)) * LOG2E;
  }
  asm volatile("" : "+v"(ev));
  const u64 tag = next_tag(((u64)(unsigned)__builtin_amdgcn_readfirstlane(ev[1]) << 32) | (unsigned)__builtin_amdgcn_readfirstlane(ev[0]));
  int xs = 0, hs = 0;
  u32x4 fa, fb;
  u32x2 tf;
  unsigned pv[10], pw[10];
  const char* tfp = p.ws + (lane < 10 ? F_ST + (rb * 16 + lane) * 8 : F_CS + (job * 16 + (lane < 20 ? lane - 10 : 0)) * 8);
  auto inc = [](int s) { return s == NWH - 1 ? 0 : s + 1; };
#define LD10(pv, q) \
    asm volatile("global_load_dword %0, %10, off sc1\n\tglobal_load_dword %1, %10, off offset:1280 sc1\n\t" \
                 "global_load_dword %2, %10, off offset:2560 sc1\n\tglobal_load_dword %3, %10, off offset:3840 sc1\n\t" \
                 "global_load_dword %4, %11, off sc1\n\tglobal_load_dword %5, %11, off offset:1280 sc1\n\t" \
                 "global_load_dword %6, %11, off offset:2560 sc1\n\tglobal_load_dword %7, %11, off offset:3840 sc1\n\t" \
                 "global_load_dword %8, %12, off sc1\n\tglobal_load_dword %9, %12, off offset:1280 sc1" \
                 : "=&v"(pv[0]), "=&v"(pv[1]), "=&v"(pv[2]), "=&v"(pv[3]), "=&v"(pv[4]), "=&v"(pv[5]), "=&v"(pv[6]), "=&v"(pv[7]), "=&v"(pv[8]), "=&v"(pv[9]) \
                 : "v"(q), "v"((q) + 5120), "v"((q) + 10240) : "memory")
#pragma unroll 1
  for (int i = 0; i < 8; ++i) {
    waitl();
    __builtin_amdgcn_sched_barrier(0);
    const int h1 = inc(hs), h2 = inc(h1);
    g.kstep(f0, f1, xs, 1, h1, true, 0, 0);
    if (i == my_i) do_dots(f0);
    const int nxs = xs == 2 ? 0 : xs + 1;
    if (has_pt && (i == 0 || i == 2 || i == 3)) g.template wait<1, AH, 4>();
    else if (i < 7) g.template wait<1, AH, 0>();
    else g.template wait<1, AH, 0>();
    barrier();
    if (i == FLAG_I && w == 0 && lane < 3)
      st_flag(p.ws + (lane == 0 ? F_EARLY + blockIdx.x * 8 : (lane == 1 ? F_ST + (rb * 16 + job) * 8 : F_CS + (job * 16 + rb) * 8)), tag);
    g.kstep(f1, f0, nxs, 0, h2, true, i < 7 ? 3 : (NWH == 5 ? 1 : 0), xs);
    if (i == 1 && has_pt) {
      const char* q = smem + L_PT + w * 4096 + (4 * lg + (l15 >> 2)) * 128 + (l15 & 3) * 8;
#pragma unroll
      for (int n = 0; n < 4; ++n) {
        bf16x8 f = tr2(q + n * 32, q + 2048 + n * 32);
        int N = 4 * ptL + n, ct = (N * 13) >> 6;
        st16(rs, OFF_W2F + ((ct * 74 + ptu) * 5 + (N - 5 * ct)) * 1024 + lane * 16, __builtin_bit_cast(u32x4, f));
      }
    }
    if (i == my_i) {
      do_dots(f1);
      float v = rscatter_add(sx[0], sx[1], cs[0], cs[1]);
      unsigned a = lg < 2 ? OFF_STP + (rb * 10 + myc) * 1280 + lg * 512 + (32 * r + 16 * C + l15) * 4
                          : OFF_CSP + (job * 10 + myc) * 1280 + ((G::unit_of(0) + r + (lg == 2 ? 0 : 4)) * 16 + l15) * 4;
      bool on = lg < 2 ? job < 10 : (rb < 10 && (lg == 2 || r + 4 < NT));
      st4(rs, on ? a : OOB, v);
      if (NT > 8) {
        float v2 = rscatter_add(cs[2], 0.f, 0.f, 0.f);
        st4(rs, (lg == 0 && rb < 10 && r + 8 < NT) ? OFF_CSP + (job * 10 + myc) * 1280 + ((G::unit_of(0) + r + 8) * 16 + l15) * 4 : OOB, v2);
      }
    }
    if (i == 6) {
      asm volatile("global_load_dwordx2 %0, %1, off sc1" : "=v"(tf) : "v"(tfp) : "memory");
      if (w == 5)
        asm volatile("global_load_dwordx4 %0, %2, off sc1\n\tglobal_load_dwordx4 %1, %2, off offset:16 sc1"
                     : "=&v"(fa), "=&v"(fb) : "v"(p.ws + F_EARLY + lane * 32) : "memory");
    }
    xs = nxs; hs = h2;
  }
  {
    waitl();
    __builtin_amdgcn_sched_barrier(0);
    const int h1 = inc(hs), h2 = inc(h1);
    g.kstep(f0, f1, xs, 1, h1, true, 0, 0);
    const int nxs = xs == 2 ? 0 : xs + 1;
    waitv<0>();
    barrier();
    asm volatile("" : "+v"(tf));
    if (!__all(tf[0] == (unsigned)tag && tf[1] == (unsigned)(tag >> 32))) pollq(tfp, tag);
    if (C == 1 && w == 5) {
      asm volatile("" : "+v"(fa), "+v"(fb));
      if (__all(flags_ok(fa, fb, tag))) {
        if (blockIdx.x == 0 && lane == 0) st_flag(p.ws + OFF_FLG, tag);
        set_cnt(smem, 3, lane);
        constexpr int NL = DMAP == 0 ? 370 : 740;
        const char* q2 = p.ws + OFF_W2F + (DMAP == 0 ? (blockIdx.x & 1) * 4 * 378880 : 0) + ((blockIdx.x >> 3) * NL + lane) * 128;
#pragma unroll
        for (int j = 0; j < (NL + 63) / 64; ++j) touch(q2 + (j * 64 + lane < NL ? j * 8192 : 0), lds0 + L_DUMMY);
      }
    }
    const int tid = w * 64 + lane;
    const char* q = p.ws + (tid < 128 ? OFF_STP + rb * 12800 + tid * 4 : OFF_CSP + job * 12800 + min(tid - 128, 303) * 4);
    LD10(pv, q);
    if constexpr (C == 0) { LD10(pw, q + 512); }
    g.kstep(f1, f0, nxs, 0, h2, true, 0, 0);
    xs = nxs; hs = h2;
  }
  waitl();
  __builtin_amdgcn_sched_barrier(0);
  g.kstep(f0, f1, xs, 1, inc(hs), true, 0, 0);
  waitl();
  __builtin_amdgcn_sched_barrier(0);
  g.kstep(f1, f0, 0, 0, 0, false, 0, 0);
  STAMP(2);
  {
    waitv<0>();
    asm volatile("" : "+v"(pv[0]), "+v"(pv[1]), "+v"(pv[2]), "+v"(pv[3]), "+v"(pv[4]), "+v"(pv[5]), "+v"(pv[6]), "+v"(pv[7]), "+v"(pv[8]), "+v"(pv[9]) :: "memory");
    float v = 0.f;
#pragma unroll
    for (int i = 0; i < 10; ++i) v += __uint_as_float(pv[i]);
    const int tid = w * 64 + lane;
    if constexpr (C == 0) {
      asm volatile("" : "+v"(pw[0]), "+v"(pw[1]), "+v"(pw[2]), "+v"(pw[3]), "+v"(pw[4]), "+v"(pw[5]), "+v"(pw[6]), "+v"(pw[7]), "+v"(pw[8]), "+v"(pw[9]) :: "memory");
      if (w < 2) {
        float v2 = 0.f;
#pragma unroll
        for (int i = 0; i < 10; ++i) v2 += __uint_as_float(pw[i]);
        float mean = v * (1.0f / 640.0f);
        float var = fmaxf(fmaf(-mean, mean, v2 * (1.0f / 640.0f)), 0.f);
        float rstd = __builtin_amdgcn_rsqf(var + 1e-5f);
        *(f32x2*)(smem + L_SUM + tid * 8) = f32x2{rstd, -rstd * mean};
      }
    }
    if (tid >= 128 && tid < 432) *(float*)(smem + L_CS + (tid - 128) * 4) = v;
  }
  barrier();
  STAMP(3);
  return tag;
}

template <int C>
DI u64 body_a(const P1& p, rsrc_t rs, char* smem, int rb, int g8, int w, int lane STAMP_ARG) {
  const int r = w & 3, lg = lane >> 4, l15 = lane & 15;
  Gemm<0, C> g;
  const u64 tag = first_pass<0, C>(g, p, rs, smem, rb, g8, 0, w, lane STAMP_PASS);
  u32x2 kb[5];
  bf16x8 ap[5][3];
  load_aphi(smem, lane, ap);
  if constexpr (C == 0) {
    const f32x2 s0 = stat_of(smem, 32 * r + l15), s1 = stat_of(smem, 32 * r + 16 + l15);
#pragma unroll
    for (int t = 0; t < 5; ++t) {
      u32x2 k1;
      pack_k(smem, g.acc[t][1], t, s1, lg, k1);
      *(u32x2*)(smem + E_KB + (r * 5 + t) * 512 + lane * 8) = k1;
      pack_k(smem, g.acc[t][0], t, s0, lg, kb[t]);
    }
  } else {
    f32x4 sa[2], sb[2];
#pragma unroll
    for (int tt = 0; tt < 2; ++tt) {
      sa[tt] = *(const f32x4*)(smem + L_SUM + (32 * r + 16 * tt + 4 * lg) * 8);
      sb[tt] = *(const f32x4*)(smem + L_SUM + (32 * r + 16 * tt + 4 * lg + 2) * 8);
    }
#pragma unroll
    for (int t = 0; t < 5; ++t) {
      float c1 = *(const float*)(smem + L_CS + ((5 + t) * 16 + l15) * 4);
      unsigned o[4];
#pragma unroll
      for (int tt = 0; tt < 2; ++tt) {
        float y0 = fmaf(g.acc[t][tt][0], sa[tt][0], sa[tt][1] * c1);
        float y1 = fmaf(g.acc[t][tt][1], sa[tt][2], sa[tt][3] * c1);
        float y2 = fmaf(g.acc[t][tt][2], sb[tt][0], sb[tt][1] * c1);
        float y3 = fmaf(g.acc[t][tt][3], sb[tt][2], sb[tt][3] * c1);
        o[2 * tt] = pack2(y0, y1); o[2 * tt + 1] = pack2(y2, y3);
      }
      *(u32x4*)(smem + E_V + (r * 5 + t) * 1024 + lane * 16) = u32x4{o[0], o[1], o[2], o[3]};
    }
  }
  barrier();
  if constexpr (C == 1) {
#pragma unroll
    for (int t = 0; t < 5; ++t) kb[t] = *(const u32x2*)(smem + E_KB + (r * 5 + t) * 512 + lane * 8);
  }
  {
    const int tok = 32 * r + 16 * C + l15;
    u32x2 pos[5], neg[5];
    features(kb, ap, pos, neg);
#pragma unroll
    for (int t = 0; t < 5; ++t) {
      *(u32x2*)(smem + E_PHI + (t * 128 + tok) * 32 + lg * 8) = pos[t];
      *(u32x2*)(smem + E_PHI + ((5 + t) * 128 + tok) * 32 + lg * 8) = neg[t];
    }
  }
  barrier();
  STAMP(4);
  {
    const unsigned pa = E_PHI + (4 * lg + (l15 >> 2)) * 32 + (l15 & 3) * 8;
    const u32x4 one4 = {0x3f803f80u, 0x3f803f80u, 0x3f803f80u, 0x3f803f80u};
    const bool valid = (l15 >> 2) == lg;
    f32x4 a[3][3];
    const char* qa[3]; const char* vb[3]; bool hf[3];
#pragma unroll
    for (int k3 = 0; k3 < 3; ++k3) {
      const int un0 = w + 8 * k3, un = un0 < 20 ? un0 : 19;
      hf[k3] = un & 1;
      qa[k3] = smem + pa + (un >> 1) * 4096;
      vb[k3] = smem + E_V + (un & 1) * 3072 + lane * 16;
#pragma unroll
      for (int j = 0; j < 3; ++j) a[k3][j] = f32x4{0, 0, 0, 0};
    }
#pragma unroll
    for (int ks = 0; ks < 4; ++ks)
#pragma unroll
      for (int k3 = 0; k3 < 3; ++k3) {
        bf16x8 af = tr2(qa[k3] + ks * 1024, qa[k3] + ks * 1024 + 512);
        const char* v = vb[k3] + ks * 5120;
        a[k3][0] = MFMA16(af, *(const bf16x8*)(v), a[k3][0]);
        a[k3][1] = MFMA16(af, *(const bf16x8*)(v + 1024), a[k3][1]);
        u32x4 b2 = *(const u32x4*)(v + (hf[k3] ? 0 : 2048));
        b2 = hf[k3] ? one4 : b2;
        a[k3][2] = MFMA16(af, __builtin_bit_cast(bf16x8, b2), a[k3][2]);
      }
#pragma unroll
    for (int k3 = 0; k3 < 3; ++k3) {
      const int un0 = w + 8 * k3;
      unsigned dst = (valid && un0 < 20) ? OFF_KVP + ((g8 * 16 + rb) * 60 + un0 * 3) * 256 + (lg * 4 + (l15 & 3)) * 16 : OOB;
#pragma unroll
      for (int j = 0; j < 3; ++j) st16(rs, dst + j * 256, __builtin_bit_cast(u32x4, a[k3][j]));
    }
  }
  drain();
  if (arrive(smem, 1, 8, lane) && lane == 0) st_flag(p.ws + F_KVP + (g8 * 16 + rb) * 8, tag);
  STAMP(5);
  if (rb < 15) {
    if (C == 1 && w == 6) {
      polln(p.ws + F_KVP + g8 * 128, 16, tag, lane);
      u32x4 kv[16];
#pragma unroll
      for (int i = 0; i < 16; ++i) kv[i] = ld16(rs, OFF_KVP + (g8 * 16 + i) * 15360 + rb * 1024 + lane * 16);
      f32x4 a = __builtin_bit_cast(f32x4, kv[0]);
#pragma unroll
      for (int i = 1; i < 16; ++i) a += __builtin_bit_cast(f32x4, kv[i]);
      int tau = rb * 4 + lg, m = (tau * 43) >> 8, tn = tau - 6 * m;
      int sg = m >= 5 ? 1 : 0, t = m - 5 * sg;
      int ks = t < 4 ? sg * 2 + (t >> 1) : 4, hf = t < 4 ? (t & 1) : sg;
      st8(rs, OFF_KVH + g8 * 7680 + (tn * 5 + ks) * 256 + l15 * 16 + hf * 8, u32x2{pack2(a[0], a[1]), pack2(a[2], a[3])});
      drain();
      if (lane == 0) st_flag(p.ws + F_KVH + (g8 * 16 + rb) * 8, tag);
      set_cnt(smem, 2, lane);
    } else wait_cnt(smem, 2, 1);
  } else wait_cnt(smem, 1, 8);
  STAMP(6);
  return tag;
}

template <int C>
DI u64 body_b(const P1& p, rsrc_t rs, char* smem, int rb, int g8, int w, int lane STAMP_ARG) {
  const int r = w & 3, lg = lane >> 4, l15 = lane & 15;
  const int t0w = min(14 * g8, 94);
  Gemm<1, C> g;
  const u64 tag = first_pass<1, C>(g, p, rs, smem, rb, g8, t0w, w, lane STAMP_PASS);
  f32x2 st[2] = {stat_of(smem, 32 * r + l15), stat_of(smem, 32 * r + 16 + l15)};
  if constexpr (C == 1) {
    __builtin_amdgcn_s_setprio(2);
#pragma unroll
    for (int pp = 0; pp < 5; ++pp)
      mish_pair(rs, smem, g.acc[2 * pp], g.acc[2 * pp + 1], 9 + 2 * pp, 4 + 2 * pp, t0w + 4 + 2 * pp, false, st, rb * 8 + 2 * r, lane);
    __builtin_amdgcn_s_setprio(0);
    STAMP(4);
    drain();
    if (arrive(smem, 0, 8, lane) && lane == 0) st_flag(p.ws + F_ACT + (rb * 16 + g8) * 8, tag);
    STAMP(5);
  } else {
    __builtin_amdgcn_s_setprio(2);
    mish_pair(rs, smem, g.acc[5], g.acc[6], 5, 0, t0w, g8 == 7, st, rb * 8 + 2 * r, lane);
    mish_pair(rs, smem, g.acc[7], g.acc[8], 7, 2, t0w + 2, g8 == 7, st, rb * 8 + 2 * r, lane);
    __builtin_amdgcn_s_setprio(0);
    STAMP(4);
    if (w == 0 && lane < 30) *(u32x4*)(smem + E_KV + lane * 272 + 256) = u32x4{0u, 0u, 0u, 0u};
    bf16x8 ap[5][3];
    load_aphi(smem, lane, ap);
    bf16x8 ph[2][5];
#pragma unroll
    for (int tt = 0; tt < 2; ++tt) {
      u32x2 kb[5], pos[5], neg[5];
#pragma unroll
      for (int t = 0; t < 5; ++t) pack_k(smem, g.acc[t][tt], t, st[tt], lg, kb[t]);
      if (tt == 0) {
        drain();
        if (arrive(smem, 0, 8, lane) && lane == 0) st_flag(p.ws + F_ACT + (rb * 16 + g8) * 8, tag);
      }
      features(kb, ap, pos, neg);
      ph[tt][0] = mk8(pos[0], pos[1]); ph[tt][1] = mk8(pos[2], pos[3]);
      ph[tt][2] = mk8(neg[0], neg[1]); ph[tt][3] = mk8(neg[2], neg[3]); ph[tt][4] = mk8(pos[4], neg[4]);
    }
    STAMP(5);
    if (w == 0) { polln(p.ws + F_KVH + g8 * 128, 15, tag, lane); set_cnt(smem, 4, lane); }
    else wait_cnt(smem, 4, 1);
    {
      int e0 = w * 120 + lane, e1 = e0 + 64;
      u32x4 a = ld16(rs, OFF_KVH + g8 * 7680 + e0 * 16);
      u32x4 b = ld16(rs, lane < 56 ? OFF_KVH + g8 * 7680 + e1 * 16 : OOB);
      *(u32x4*)(smem + E_KV + (e0 >> 4) * 272 + (e0 & 15) * 16) = a;
      if (lane < 56) *(u32x4*)(smem + E_KV + (e1 >> 4) * 272 + (e1 & 15) * 16) = b;
    }
    waitl();
    asm volatile("" ::: "memory");
    (void)arrive(smem, 1, 4, lane);
    wait_cnt(smem, 1, 4);
    STAMP(6);
    const char* ks_ = smem + E_KV + (((l15 >> 2) == lg) ? (lg * 4 + (l15 & 3)) * 16 : 256);
    f32x4 n[2][6];
#pragma unroll
    for (int tn = 0; tn < 6; ++tn) {
      n[0][tn] = f32x4{0, 0, 0, 0}; n[1][tn] = f32x4{0, 0, 0, 0};
#pragma unroll
      for (int ks = 0; ks < 5; ++ks) {
        bf16x8 ka = *(const bf16x8*)(ks_ + (tn * 5 + ks) * 272);
        n[0][tn] = MFMA16(ka, ph[0][ks], n[0][tn]);
        n[1][tn] = MFMA16(ka, ph[1][ks], n[1][tn]);
      }
    }
#pragma unroll
    for (int tt = 0; tt < 2; ++tt) {
      const f32x2 rc = splat2(__builtin_amdgcn_rcpf(n[tt][5][0] + 1e-6f));
      u32x2 o[5];
#pragma unroll
      for (int t = 0; t < 5; ++t) o[t] = u32x2{pack2v(lo2(n[tt][t]) * rc), pack2v(hi2(n[tt][t]) * rc)};
      const unsigned ob = OFF_O + ((rb * 8 + 2 * r + tt) * 20 + ((5 * g8) >> 1)) * 1024 + lane * 16;
      if ((g8 & 1) == 0) {
        st16(rs, ob, u32x4{o[0][0], o[0][1], o[1][0], o[1][1]});
        st16(rs, ob + 1024, u32x4{o[2][0], o[2][1], o[3][0], o[3][1]});
        st8(rs, ob + 2048, o[4]);
      } else {
        st8(rs, ob + 8, o[0]);
        st16(rs, ob + 1024, u32x4{o[1][0], o[1][1], o[2][0], o[2][1]});
        st16(rs, ob + 2048, u32x4{o[3][0], o[3][1], o[4][0], o[4][1]});
      }
    }
    drain();
    if (arrive(smem, 2, 4, lane) && lane == 0) st_flag(p.ws + F_O + (rb * 16 + g8) * 8, tag);
  }
  return tag;
}

#define GLD(dst, ptr, off) asm volatile("global_load_dwordx4 %0, %1, off offset:" #off " sc1" : "=v"(dst) : "v"(ptr) : "memory")
#define WAITV(n) do { asm volatile("s_waitcnt vmcnt(%0)" :: "i"(n) : "memory"); __builtin_amdgcn_sched_barrier(0); } while (0)

template <int NST, int NA, int NB, int S>
DI void p2_steps(const char*& pa, const char*& pa1, const char*& pa2, const char*& pa3, const char*& pb,
                 u32x4 (&A)[NA][4], u32x4 (&B)[NB][5], f32x4 (&acc)[5][4]) {
  if constexpr (S < NST) {
    constexpr int ja = S - NA, jb = S - NB;
    auto cnt = [](int j0, bool skipA) constexpr {
      int n = 0;
      for (int j = j0; j < S; ++j) {
        if (j + NA < NST && !(skipA && j == j0)) n += 4;
        if (j + NB < NST) n += 5;
      }
      return n;
    };
    constexpr int ya = ja < 0 ? 4 * ((NA < NST ? NA : NST) - 1 - S) + cnt(0, false) : cnt(ja, true);
    constexpr int yb = jb < 0 ? 1000 : cnt(jb + 1, false);
    constexpr int y = ya < yb ? ya : yb;
    WAITV(y);
#pragma unroll
    for (int i = 0; i < 4; ++i) asm volatile("" : "+v"(A[S % NA][i]));
#pragma unroll
    for (int i = 0; i < 5; ++i) asm volatile("" : "+v"(B[S % NB][i]));
#pragma unroll
    for (int n = 0; n < 5; ++n)
#pragma unroll
      for (int t = 0; t < 4; ++t)
        acc[n][t] = MFMA16(__builtin_bit_cast(bf16x8, B[S % NB][n]), __builtin_bit_cast(bf16x8, A[S % NA][t]), acc[n][t]);
    __builtin_amdgcn_sched_barrier(0);
    if constexpr (S + NA < NST) {
      GLD(A[S % NA][0], pa, 0); GLD(A[S % NA][1], pa1, 0); GLD(A[S % NA][2], pa2, 0); GLD(A[S % NA][3], pa3, 0);
      pa += 1024; pa1 += 1024; pa2 += 1024; pa3 += 1024;
    }
    if constexpr (S + NB < NST) {
      GLD(B[S % NB][0], pb, -2048); GLD(B[S % NB][1], pb, -1024); GLD(B[S % NB][2], pb, 0); GLD(B[S % NB][3], pb, 1024); GLD(B[S % NB][4], pb, 2048);
      pb += 5120;
    }
    p2_steps<NST, NA, NB, S + 1>(pa, pa1, pa2, pa3, pb, A, B, acc);
  }
}
#define ISSUE_A(A, s) do { GLD(A[s][0], pa, 0); GLD(A[s][1], pa1, 0); GLD(A[s][2], pa2, 0); GLD(A[s][3], pa3, 0); \
    pa += 1024; pa1 += 1024; pa2 += 1024; pa3 += 1024; } while (0)
#define ISSUE_B(B, s) do { GLD(B[s][0], pb, -2048); GLD(B[s][1], pb, -1024); GLD(B[s][2], pb, 0); GLD(B[s][3], pb, 1024); GLD(B[s][4], pb, 2048); \
    pb += 5120; } while (0)
DI void wait_flags(char* smem, int cnt, bool poller, const char* flags, int n, u64 tag, int lane) {
  if (poller) { polln(flags, n, tag, lane); set_cnt(smem, cnt, lane); }
  else { wait_cnt(smem, cnt, 1); drain(); }
}
template <int NST, int NA, int NB>
DI void stream(const char* pa, unsigned astr, const char* pb, char* smem, int cnt, bool poller, const char* flags, int nflags, u64 tag, int lane,
               f32x4 (&acc)[5][4]) {
  const char* pa1 = pa + astr; const char* pa2 = pa1 + astr; const char* pa3 = pa2 + astr;
  u32x4 A[NA][4], B[NB][5];
#pragma unroll
  for (int s = 0; s < NB; ++s) ISSUE_B(B, s);
  wait_flags(smem, cnt, poller, flags, nflags, tag, lane);
#pragma unroll
  for (int s = 0; s < NA; ++s) ISSUE_A(A, s);
  p2_steps<NST, NA, NB, 0>(pa, pa1, pa2, pa3, pb, A, B, acc);
}

__global__ __launch_bounds__(512, 2) void k_fused(P1 p) {
  extern __shared__ __attribute__((aligned(16))) char smem[];
  const int b = blockIdx.x, xcd = b & 7, idx = b >> 3;
  const int w = __builtin_amdgcn_readfirstlane(threadIdx.x >> 6), lane = threadIdx.x & 63;
  const int rb = (xcd >> 2) * 8 + (idx >> 2);
  const int jl = idx & 3, xc = xcd & 3;
  const int type = jl >> 1, g8 = 2 * xc + (jl & 1);
  const int c = w >> 2;
  STAMP_DECL; STAMP(0);
  const rsrc_t rs = __builtin_amdgcn_make_buffer_rsrc(p.ws, 0, WS_USED, 0x00020000);
  u64 tag;
  if (type == 0) {
    if (c == 0) tag = body_a<0>(p, rs, smem, rb, g8, w, lane STAMP_PASS);
    else tag = body_a<1>(p, rs, smem, rb, g8, w, lane STAMP_PASS);
  } else {
    if (c == 0) tag = body_b<0>(p, rs, smem, rb, g8, w, lane STAMP_PASS);
    else tag = body_b<1>(p, rs, smem, rb, g8, w, lane STAMP_PASS);
  }
  const int lg = lane >> 4, l15 = lane & 15;
  STAMP(7);
  if (w == 5) {
    unsigned v = __hip_atomic_load((LDS3 unsigned*)(smem + L_CNT + 12), __ATOMIC_RELAXED, __HIP_MEMORY_SCOPE_WORKGROUP);
    if (__builtin_amdgcn_readfirstlane(v) == 0) {
      poll256(p.ws + F_EARLY, tag, lane);
      if (blockIdx.x == 0 && lane == 0) st_flag(p.ws + OFF_FLG, tag);
      set_cnt(smem, 3, lane);
    }
  } else wait_cnt(smem, 3, 1);
  const int rt = DMAP == 0 ? (xcd >> 1) * 8 + (idx >> 2) : xcd * 4 + (idx >> 3);
  const int ct = DMAP == 0 ? (xcd & 1) * 4 + (idx & 3) : (idx & 7);
  u32x2 xv[3];
  const unsigned o0 = (unsigned)((rt * 64 + (w & 3) * 16 + l15) * 640 + ct * 80 + (w >> 2) * 16 + 4 * lg);
#pragma unroll
  for (int m = 0; m < 3; ++m) xv[m] = *(const u32x2*)(p.x + o0 + ((m < 2 || w < 4) ? 32 * m : 0));
#pragma unroll
  for (int m = 0; m < 3; ++m) asm volatile("" : "+v"(xv[m]));
  f32x4 acc[5][4];
#pragma unroll
  for (int n = 0; n < 5; ++n)
#pragma unroll
    for (int t = 0; t < 4; ++t) acc[n][t] = f32x4{0, 0, 0, 0};
  if (w >= 4) {
    const int u0 = (w - 4) * 14 - (w == 7 ? 1 : 0);
    const char* pa = p.ws + OFF_ACT + ((rt * 4) * 54 + u0) * 1024 + lane * 16;
    const char* pb = p.ws + OFF_W2F + (ct * 74 + u0) * 5120 + lane * 16 + 2048;
    const char* fact = p.ws + F_ACT + (rt >> 1) * 128;
    if (w < 6) stream<14, 5, 3>(pa, 54 * 1024, pb, smem, 5, w == 4, fact, 8, tag, lane, acc);
    else stream<13, 5, 3>(pa, 54 * 1024, pb, smem, 5, false, fact, 8, tag, lane, acc);
  } else {
    stream<5, 4, 4>(p.ws + OFF_O + ((rt * 4) * 20 + 5 * w) * 1024 + lane * 16, 20 * 1024,
                    p.ws + OFF_W2F + (ct * 74 + 54 + 5 * w) * 5120 + lane * 16 + 2048, smem, 7, w == 0, p.ws + F_O + (rt >> 1) * 128, 8, tag, lane, acc);
    if (type == 1) wait_cnt(smem, 2, 4);
  }
  STAMP(9);
  {
    char* dst = smem + w * 18432 + lane * 16;
#pragma unroll
    for (int tile = 0; tile < 20; ++tile) {
      int pos = tile - (tile > w) - (tile > w + 8) - (tile > w + 16);
      if ((tile & 7) != w) *(f32x4*)(dst + pos * 1024) = acc[tile >> 2][tile & 3];
    }
  }
  f32x4 own[3];
#pragma unroll
  for (int m = 0; m < 3; ++m) {
    own[m] = f32x4{0, 0, 0, 0};
#pragma unroll
    for (int k = 0; k < 8; ++k)
      if (8 * m + k < 20 && k == w) own[m] = acc[(8 * m + k) >> 2][(8 * m + k) & 3];
  }
  __syncthreads();
  STAMP(10);
#pragma unroll
  for (int m = 0; m < 3; ++m) {
    const int tile = w + 8 * m;
    if (tile < 20) {
      f32x4 part[8];
#pragma unroll
      for (int v = 0; v < 8; ++v) {
        int pos = tile - (tile > v) - (tile > v + 8) - (tile > v + 16);
        part[v] = *(const f32x4*)(smem + v * 18432 + pos * 1024 + lane * 16);
        if (v == w) part[v] = own[m];
      }
      f32x4 ssum = (part[0] + part[1]) + (part[2] + part[3]);
      f32x4 bsum = (part[4] + part[5]) + (part[6] + part[7]);
      float xs[4] = {bf_lo(xv[m][0]), bf_hi(xv[m][0]), bf_lo(xv[m][1]), bf_hi(xv[m][1])};
      float r[4];
#pragma unroll
      for (int j = 0; j < 4; ++j) {
        float bb = bsum[j];
        float g = bb * __builtin_amdgcn_rcpf(1.0f + __builtin_amdgcn_exp2f(-bb * LOG2E));
        r[j] = fmaf(ssum[j], g, xs[j]);
      }
      *(u32x2*)(p.out + o0 + 32 * m) = u32x2{pack2(r[0], r[1]), pack2(r[2], r[3])};
    }
  }
  STAMP(11);
  STAMP_FLUSH(p.ws, blockIdx.x * 8 + w);
}
}

extern "C" void kernel_launch(void* const* d_in, const int* in_sizes, int n_in, void* d_out, int out_size, void* d_ws,
                              size_t ws_size, hipStream_t stream) {
  using namespace hk;
  P1 p;
  p.x = (const u16*)d_in[0]; p.wqkv = (const u16*)d_in[1]; p.wphi = (const u16*)d_in[2]; p.wo = (const u16*)d_in[3];
  p.w1 = (const u16*)d_in[5]; p.b1 = (const u16*)d_in[6]; p.w2 = (const u16*)d_in[7]; p.ws = (char*)d_ws; p.out = (u16*)d_out;
  (void)hipFuncSetAttribute((const void*)k_fused, hipFuncAttributeMaxDynamicSharedMemorySize, L_END);
  k_fused<<<256, 512, L_END, stream>>>(p);
}
```

```cpp
#include <hip/hip_runtime.h>
#include <hip/hip_bf16.h>

typedef unsigned short u16;
typedef __bf16 bf16x2 __attribute__((ext_vector_type(2)));
typedef __bf16 bf16x4 __attribute__((ext_vector_type(4)));
typedef __bf16 bf16x8 __attribute__((ext_vector_type(8)));
typedef float f32x2 __attribute__((ext_vector_type(2)));
typedef float f32x4 __attribute__((ext_vector_type(4)));
typedef unsigned u32x2 __attribute__((ext_vector_type(2)));
typedef unsigned u32x4 __attribute__((ext_vector_type(4)));
#define LDS3 __attribute__((address_space(3)))
#define DI __device__ __forceinline__
#define MFMA16(a, b, c) __builtin_amdgcn_mfma_f32_16x16x32_bf16((a), (b), (c), 0, 0, 0)

#define STAMP(i)
#define STAMP_DECL
#define STAMP_FLUSH(ws, slot)
namespace hk {
typedef unsigned long long u64;
constexpr float LOG2E = 1.4426950408889634f;
constexpr float LN2 = 0.6931471805599453f;
#define ROTP 2
#define NA_ACT 3
#define DMAP 0
constexpr int FLAG_I = ROTP + 2 > 8 ? 8 : ROTP + 2;
#define APAD 256
constexpr unsigned AST = 54 * 1024 + APAD, OST = 20 * 1024 + APAD;
constexpr unsigned OFF_ACT = 0;
constexpr unsigned OFF_O = OFF_ACT + 128 * AST;
constexpr unsigned OFF_W2F = OFF_O + 128 * OST;
constexpr unsigned OFF_KVP = OFF_W2F + 8 * 74 * 5 * 1024;
constexpr unsigned OFF_KVH = OFF_KVP + 8 * 16 * 60 * 256;
constexpr unsigned OFF_STP = OFF_KVH + 8 * 30 * 256;
constexpr unsigned OFF_CSP = OFF_STP + 16 * 12800;
constexpr unsigned OFF_FLG = OFF_CSP + 16 * 12800;
constexpr unsigned F_EARLY = OFF_FLG + 128;
constexpr unsigned F_ACT = F_EARLY + 2048;
constexpr unsigned F_KVP = F_ACT + 2048;
constexpr unsigned F_KVH = F_KVP + 1024;
constexpr unsigned F_O = F_KVH + 1024;
constexpr unsigned F_ST = F_O + 2048;
constexpr unsigned F_CS = F_ST + 2048;
constexpr unsigned WS_USED = F_CS + 2048;
constexpr unsigned OOB = 0x7ffff000u;
constexpr int XB = 16384, WB0 = 3 * XB;
constexpr int L_PT = 118784;
constexpr int L_DUMMY = 151552;
constexpr int L_RAW = 155648;
constexpr int L_BIAS = L_RAW + 3200;
constexpr int L_ZERO = L_BIAS + 448;
constexpr int L_CNT = L_ZERO + 64;
constexpr int L_BIASL = L_CNT + 64;
constexpr int L_CS = L_BIASL + 896;
constexpr int L_SUM = L_CS + 1216;
constexpr int L_END = 163840;
static_assert(L_SUM + 1024 <= L_END - 256, "lds");
constexpr int E_PHI = 0;
constexpr int E_V = 40960;
constexpr int E_KB = 61440;
constexpr int E_KV = 0;

DI unsigned pack2(float a, float b) {
  f32x2 v = {a, b};
  return __builtin_bit_cast(unsigned, __builtin_convertvector(v, bf16x2));
}
DI unsigned pack2v(f32x2 v) { return __builtin_bit_cast(unsigned, __builtin_convertvector(v, bf16x2)); }
DI bf16x2 as_bf2(unsigned u) { return __builtin_bit_cast(bf16x2, u); }
DI float bf_lo(unsigned u) { return __uint_as_float(u << 16); }
DI float bf_hi(unsigned u) { return __uint_as_float(u & 0xffff0000u); }
DI float rscatter_add(float m0, float m1, float m2, float m3) {
  auto a = __builtin_amdgcn_permlane16_swap(__float_as_uint(m0), __float_as_uint(m1), false, false);
  auto b = __builtin_amdgcn_permlane16_swap(__float_as_uint(m2), __float_as_uint(m3), false, false);
  float A = __uint_as_float(a[0]) + __uint_as_float(a[1]);
  float B = __uint_as_float(b[0]) + __uint_as_float(b[1]);
  auto c = __builtin_amdgcn_permlane32_swap(__float_as_uint(A), __float_as_uint(B), false, false);
  return __uint_as_float(c[0]) + __uint_as_float(c[1]);
}
DI f32x2 lo2(f32x4 v) { return __builtin_shufflevector(v, v, 0, 1); }
DI f32x2 hi2(f32x4 v) { return __builtin_shufflevector(v, v, 2, 3); }
DI f32x2 fma2(f32x2 a, f32x2 b, f32x2 c) { return __builtin_elementwise_fma(a, b, c); }
DI f32x2 splat2(float a) { return f32x2{a, a}; }
DI f32x2 exp2v(f32x2 a) { return f32x2{__builtin_amdgcn_exp2f(a[0]), __builtin_amdgcn_exp2f(a[1])}; }
DI f32x2 rcpv(f32x2 a) { return f32x2{__builtin_amdgcn_rcpf(a[0]), __builtin_amdgcn_rcpf(a[1])}; }
DI void dual_softmax(const f32x4 (&p)[5], u32x2 (&pos)[5], u32x2 (&neg)[5]) {
  float mx = fmaxf(p[0][0], p[0][1]), mn = fminf(p[0][0], p[0][1]);
  mx = fmaxf(fmaxf(mx, p[0][2]), p[0][3]); mn = fminf(fminf(mn, p[0][2]), p[0][3]);
#pragma unroll
  for (int t = 1; t < 5; ++t) {
    mx = fmaxf(fmaxf(mx, p[t][0]), p[t][1]); mx = fmaxf(fmaxf(mx, p[t][2]), p[t][3]);
    mn = fminf(fminf(mn, p[t][0]), p[t][1]); mn = fminf(fminf(mn, p[t][2]), p[t][3]);
  }
  const f32x2 L = splat2(LOG2E), nL = splat2(-LOG2E), cx = splat2(-mx * LOG2E), cn = splat2(mn * LOG2E);
  f32x2 ep[10], en[10];
#pragma unroll
  for (int t = 0; t < 5; ++t) {
    ep[2 * t] = exp2v(fma2(lo2(p[t]), L, cx)); ep[2 * t + 1] = exp2v(fma2(hi2(p[t]), L, cx));
    en[2 * t] = exp2v(fma2(lo2(p[t]), nL, cn)); en[2 * t + 1] = exp2v(fma2(hi2(p[t]), nL, cn));
  }
  f32x2 sp = ep[0], sn = en[0];
#pragma unroll
  for (int i = 1; i < 10; ++i) { sp += ep[i]; sn += en[i]; }
  const f32x2 ip = splat2(__builtin_amdgcn_rcpf(sp[0] + sp[1])), in_ = splat2(__builtin_amdgcn_rcpf(sn[0] + sn[1]));
#pragma unroll
  for (int t = 0; t < 5; ++t) {
    pos[t] = u32x2{pack2v(ep[2 * t] * ip), pack2v(ep[2 * t + 1] * ip)};
    neg[t] = u32x2{pack2v(en[2 * t] * in_), pack2v(en[2 * t + 1] * in_)};
  }
}
DI f32x2 mish2(f32x2 hl) {
  f32x2 e = exp2v(hl);
  f32x2 d = fma2(e, e + splat2(2.0f), splat2(2.0f));
  f32x2 t = fma2(rcpv(d), splat2(-2.0f * LN2), splat2(LN2));
  return hl * t;
}
DI bf16x8 mk8(u32x2 a, u32x2 b) {
  u32x4 v = {a[0], a[1], b[0], b[1]};
  return __builtin_bit_cast(bf16x8, v);
}
DI bf16x8 tr2(const char* a, const char* b) {
  bf16x4 lo = __builtin_amdgcn_ds_read_tr16_b64_v4bf16((LDS3 bf16x4*)(a));
  bf16x4 hi = __builtin_amdgcn_ds_read_tr16_b64_v4bf16((LDS3 bf16x4*)(b));
  return __builtin_shufflevector(lo, hi, 0, 1, 2, 3, 4, 5, 6, 7);
}

struct P1 {
  const u16* x; const u16* wqkv; const u16* wphi; const u16* wo; const u16* w1; const u16* b1; const u16* w2;
  char* ws; u16* out;
};
typedef __amdgpu_buffer_rsrc_t rsrc_t;

DI unsigned lds_addr(const void* p) { return (unsigned)(size_t)(LDS3 const char*)p; }
DI void glds1(const void* g, unsigned lds) {
  unsigned keep;
  asm volatile("s_mov_b32 %0, m0\n\ts_mov_b32 m0, %2\n\ts_nop 0\n\tglobal_load_lds_dwordx4 %1, off\n\ts_mov_b32 m0, %0"
               : "=&s"(keep) : "v"(g), "s"(lds) : "memory");
}
DI void glds5_sc1(const void* g, unsigned lds) {
  unsigned keep;
  asm volatile("s_mov_b32 %0, m0\n\t"
               "s_mov_b32 m0, %2\n\ts_nop 0\n\tglobal_load_lds_dwordx4 %1, off sc1\n\t"
               "global_load_lds_dwordx4 %1, off offset:1024 sc1\n\t"
               "global_load_lds_dwordx4 %1, off offset:2048 sc1\n\t"
               "global_load_lds_dwordx4 %1, off offset:3072 sc1\n\t"
               "s_add_u32 m0, %2, 0x1000\n\ts_nop 0\n\tglobal_load_lds_dwordx4 %3, off sc1\n\t"
               "s_mov_b32 m0, %0"
               : "=&s"(keep) : "v"(g), "s"(lds), "v"((const char*)g + 4096) : "memory", "scc");
}
DI void touch(const void* g, unsigned lds) {
  unsigned keep;
  asm volatile("s_mov_b32 %0, m0\n\ts_mov_b32 m0, %2\n\ts_nop 0\n\tglobal_load_lds_dword %1, off\n\ts_mov_b32 m0, %0"
               : "=&s"(keep) : "v"(g), "s"(lds) : "memory");
}
DI u64 next_tag(u64 e) { return e * 0x9E3779B97F4A7C15ull + 0x632BE59BD9B4E019ull; }
DI void st_flag(char* p, u64 v) { __hip_atomic_store((u64*)p, v, __ATOMIC_RELAXED, __HIP_MEMORY_SCOPE_AGENT); }
DI void st16(rsrc_t rs, unsigned off, u32x4 v) { __builtin_amdgcn_raw_buffer_store_b128(v, rs, off, 0, 16); }
DI void st8(rsrc_t rs, unsigned off, u32x2 v) { __builtin_amdgcn_raw_buffer_store_b64(v, rs, off, 0, 16); }
DI void st4(rsrc_t rs, unsigned off, float v) { __builtin_amdgcn_raw_buffer_store_b32(__float_as_uint(v), rs, off, 0, 16); }
DI u32x4 ld16(rsrc_t rs, unsigned off) { return __builtin_amdgcn_raw_buffer_load_b128(rs, off, 0, 16); }
DI bool ready(u32x4 v) { return v[0] != 0xffffffffu && v[1] != 0xffffffffu && v[2] != 0xffffffffu && v[3] != 0xffffffffu; }
DI void drain() { asm volatile("s_waitcnt vmcnt(0)" ::: "memory"); }
constexpr int SPIN_MAX = 1 << 18;
DI bool flags_ok(u32x4 a, u32x4 b, u64 tag) {
  const unsigned tl = (unsigned)tag, th = (unsigned)(tag >> 32);
  return a[0] == tl && a[1] == th && a[2] == tl && a[3] == th && b[0] == tl && b[1] == th && b[2] == tl && b[3] == th;
}
DI void poll256(const char* flags, u64 tag, int lane) {
  const char* q = flags + lane * 32;
#pragma unroll 1
  for (int spin = 0; spin < SPIN_MAX; ++spin) {
    u32x4 a, b;
    asm volatile("global_load_dwordx4 %0, %2, off sc1\n\tglobal_load_dwordx4 %1, %2, off offset:16 sc1\n\ts_waitcnt vmcnt(0)"
                 : "=&v"(a), "=&v"(b) : "v"(q) : "memory");
    if (__all(flags_ok(a, b, tag))) break;
    __builtin_amdgcn_s_sleep(8);
  }
}
DI void pollq(const char* q, u64 tag) {
#pragma unroll 1
  for (int spin = 0; spin < SPIN_MAX; ++spin) {
    u32x2 a;
    asm volatile("global_load_dwordx2 %0, %1, off sc1\n\ts_waitcnt vmcnt(0)" : "=&v"(a) : "v"(q) : "memory");
    if (__all(a[0] == (unsigned)tag && a[1] == (unsigned)(tag >> 32))) break;
    __builtin_amdgcn_s_sleep(3);
  }
}
DI void polln(const char* flags, int n, u64 tag, int lane) { pollq(flags + (lane < n ? lane : 0) * 8, tag); }
DI bool arrive(char* smem, int idx, int n, int lane) {
  unsigned old = 0;
  if (lane == 0) old = __hip_atomic_fetch_add((LDS3 unsigned*)(smem + L_CNT + idx * 4), 1u, __ATOMIC_RELAXED, __HIP_MEMORY_SCOPE_WORKGROUP);
  return __builtin_amdgcn_readfirstlane(old) == (unsigned)(n - 1);
}
DI void set_cnt(char* smem, int idx, int lane) {
  if (lane == 0) __hip_atomic_store((LDS3 unsigned*)(smem + L_CNT + idx * 4), 1u, __ATOMIC_RELAXED, __HIP_MEMORY_SCOPE_WORKGROUP);
}
DI void wait_cnt(char* smem, int idx, unsigned n) {
#pragma unroll 1
  for (int spin = 0; spin < (SPIN_MAX << 4); ++spin) {
    unsigned v = __hip_atomic_load((LDS3 unsigned*)(smem + L_CNT + idx * 4), __ATOMIC_RELAXED, __HIP_MEMORY_SCOPE_WORKGROUP);
    if (__builtin_amdgcn_readfirstlane(v) >= n) break;
    __builtin_amdgcn_s_sleep(1);
  }
  asm volatile("" ::: "memory");
}

template <int NT> struct Frags { bf16x8 x[2]; bf16x8 w[NT]; };

#define STAMP_ARG
#define STAMP_PASS
template <int N> DI void waitv() { asm volatile("s_waitcnt vmcnt(%0)" :: "i"(N) : "memory"); }
DI void waitl() { __builtin_amdgcn_s_waitcnt(0xC07F); }

template <int TYPE, int C>
struct Gemm {
  static constexpr int NT = TYPE == 0 ? 5 : (C == 0 ? 9 : 10);
  static constexpr int NU = TYPE == 0 ? 11 : 19;
  static constexpr int WROW = NU * 32, NC = NU * 2, HB = 32 * WROW;
  static constexpr int NWH = TYPE == 0 ? 6 : 5;
  static constexpr int NWS = (NU + 7) / 8;
  static constexpr int NFULL = NU - (NWS - 1) * 8;
  static constexpr int MAGIC = (65536 + NC - 1) / NC;
  static constexpr bool VW = (TYPE == 0 && C == 1);
  static constexpr int unit_of(int t) { return TYPE == 0 ? t + 5 * C : t + 9 * C; }

  const char* xsrc[2];
  const char* wsrc[NWS];
  unsigned winc[NWS];
  unsigned xa0, wa, lds0;
  int cx, hw, hslot, w;
  bool full;
  char* smem;
  f32x4 acc[NT][2];

  DI void init(const P1& p, char* smem_, int rb, int g8, int t0w, int rot, int w_, int lane) {
    smem = smem_; w = w_; cx = rot; hw = 2 * rot; hslot = 0; lds0 = lds_addr(smem_); full = w < NFULL;
    const int r = w & 3, lg = lane >> 4, l15 = lane & 15;
#pragma unroll
    for (int i = 0; i < 2; ++i) {
      int row = (i * 8 + w) * 8 + (lane >> 3);
      int ch = (lane & 7) ^ ((row >> 1) & 7);
      xsrc[i] = (const char*)p.x + (unsigned)((rb * 128 + row) * 1280 + ch * 16 + rot * 128);
    }
#pragma unroll
    for (int i = 0; i < NWS; ++i) {
      int ci = (i * 8 + w) * 64 + lane;
      ci = ci < 32 * NC ? ci : lane;
      int pos = (ci * MAGIC) >> 16, c16 = ci - pos * NC;
      int kk = pos & 15;
      int k = (kk >= 4 && kk < 12) ? (pos ^ 12) : pos;
      bool isq = TYPE == 0 || c16 < 10;
      int colb;
      if (TYPE == 0) { int c = min(c16, 19); colb = 1280 + 160 * g8 + c * 16 + (c >= 10 ? 1120 : 0); }
      else colb = isq ? (160 * g8 + c16 * 16) : (32 * t0w + (c16 - 10) * 16);
      unsigned stride = isq ? 3840u : 3456u;
      wsrc[i] = (isq ? (const char*)p.wqkv : (const char*)p.w1) + (unsigned)((k + rot * 64) * stride + colb);
      winc[i] = stride * 32;
    }
    xa0 = (32 * r + l15) * 128 + ((lg ^ ((l15 >> 1) & 7)) * 16);
    wa = WB0 + (16 * (lg >> 1) + 4 * (lg & 1) + (l15 >> 2)) * WROW + (VW ? 160 + (l15 & 3) * 40 : (l15 & 3) * 8);
#pragma unroll
    for (int t = 0; t < NT; ++t) { acc[t][0] = f32x4{0, 0, 0, 0}; acc[t][1] = f32x4{0, 0, 0, 0}; }
  }
  DI void issue_x1(int s, int xslot) {
    glds1(xsrc[s], lds0 + xslot * XB + w * 1024 + s * 0x2000);
    xsrc[s] += 128;
    if (s == 1 && ++cx == 10) { cx = 0; xsrc[0] -= 1280; xsrc[1] -= 1280; }
  }
  DI void issue_w1(int s) {
    if (s < NWS - 1 || full) { glds1(wsrc[s], lds0 + WB0 + hslot * HB + w * 1024 + s * 0x2000); wsrc[s] += winc[s]; }
    if (s == NWS - 1) {
      hslot = hslot == NWH - 1 ? 0 : hslot + 1;
      if (++hw == 20) {
        hw = 0;
#pragma unroll
        for (int i = 0; i < NWS; ++i) wsrc[i] -= 20 * winc[i];
      }
    }
  }
  DI void issue_x(int xslot) { issue_x1(0, xslot); issue_x1(1, xslot); }
  DI void issue_w() {
#pragma unroll
    for (int s = 0; s < NWS; ++s) issue_w1(s);
  }
  template <int NX, int NH, int E> DI void wait() {
    if (full) waitv<2 * NX + NH * NWS + E>(); else waitv<2 * NX + NH * (NWS - 1) + E>();
  }
  DI void rd_x(Frags<NT>& f, int xslot, int ks, int tt) { f.x[tt] = *(const bf16x8*)(smem + xslot * XB + (xa0 ^ (ks * 64)) + tt * 2048); }
  DI void rd_w(Frags<NT>& f, int hs, int t) {
    const char* q = smem + hs * HB + wa + (VW ? t * 8 : unit_of(t) * 32);
    f.w[t] = tr2(q, q + 8 * WROW);
  }
  DI void kstep(const Frags<NT>& f, Frags<NT>& g, int nxs, int nks, int nhs, bool do_rd, int nreq, int ixs) {
    constexpr int NS = 2 + 2 * NWS;
#pragma unroll
    for (int t = 0; t < NT; ++t) {
      if (do_rd) {
        if (t < 2) rd_x(g, nxs, nks, t);
        rd_w(g, nhs, t);
      }
#pragma unroll
      for (int tt = 0; tt < 2; ++tt)
        acc[t][tt] = VW ? MFMA16(f.x[tt], f.w[t], acc[t][tt]) : MFMA16(f.w[t], f.x[tt], acc[t][tt]);
#pragma unroll
      for (int s = (t * NS) / NT; s < ((t + 1) * NS) / NT; ++s) {
        if (s < NWS) { if (nreq > 0) issue_w1(s); }
        else if (s < NWS + 2) { if (nreq > 1) issue_x1(s - NWS, ixs); }
        else { if (nreq > 1) issue_w1(s - NWS - 2); }
      }
      __builtin_amdgcn_sched_barrier(0);
    }
  }
};

DI void dots2(float& c0, float& c1, const bf16x8& wa_, const bf16x8& wb_) {
  u32x4 a = __builtin_bit_cast(u32x4, wa_), b = __builtin_bit_cast(u32x4, wb_);
  unsigned a0 = a[0], a1 = a[1], a2 = a[2], a3 = a[3], b0 = b[0], b1 = b[1], b2 = b[2], b3 = b[3];
  asm volatile("s_nop 1\n\t"
               "v_dot2c_f32_bf16 %0, 0x3f803f80, %2\n\tv_dot2c_f32_bf16 %1, 0x3f803f80, %6\n\t"
               "v_dot2c_f32_bf16 %0, 0x3f803f80, %3\n\tv_dot2c_f32_bf16 %1, 0x3f803f80, %7\n\t"
               "v_dot2c_f32_bf16 %0, 0x3f803f80, %4\n\tv_dot2c_f32_bf16 %1, 0x3f803f80, %8\n\t"
               "v_dot2c_f32_bf16 %0, 0x3f803f80, %5\n\tv_dot2c_f32_bf16 %1, 0x3f803f80, %9\n\ts_nop 3"
               : "+v"(c0), "+v"(c1) : "v"(a0), "v"(a1), "v"(a2), "v"(a3), "v"(b0), "v"(b1), "v"(b2), "v"(b3));
}
DI void dots_x(float& s1, float& s2, const bf16x8& x) {
  u32x4 a = __builtin_bit_cast(u32x4, x);
  unsigned a0 = a[0], a1 = a[1], a2 = a[2], a3 = a[3];
  asm volatile("s_nop 1\n\t"
               "v_dot2c_f32_bf16 %0, 0x3f803f80, %2\n\tv_dot2c_f32_bf16 %1, %2, %2\n\t"
               "v_dot2c_f32_bf16 %0, 0x3f803f80, %3\n\tv_dot2c_f32_bf16 %1, %3, %3\n\t"
               "v_dot2c_f32_bf16 %0, 0x3f803f80, %4\n\tv_dot2c_f32_bf16 %1, %4, %4\n\t"
               "v_dot2c_f32_bf16 %0, 0x3f803f80, %5\n\tv_dot2c_f32_bf16 %1, %5, %5\n\ts_nop 3"
               : "+v"(s1), "+v"(s2) : "v"(a0), "v"(a1), "v"(a2), "v"(a3));
}
DI f32x2 stat_of(const char* smem, int tok) { return *(const f32x2*)(smem + L_SUM + tok * 8); }
DI void mish_pair(rsrc_t rs, const char* smem, const f32x4 (&a0)[2], const f32x4 (&a1)[2], int ucs, int wl, int wt, bool skip,
                  const f32x2 (&st)[2], int tt0, int lane) {
  const int lg = lane >> 4;
  f32x4 bb[2], c4[2];
#pragma unroll
  for (int h = 0; h < 2; ++h) {
    bb[h] = *(const f32x4*)(smem + L_BIASL + ((wl + h) * 16 + 4 * lg) * 4);
    c4[h] = *(const f32x4*)(smem + L_CS + ((ucs + h) * 16 + 4 * lg) * 4);
  }
#pragma unroll
  for (int tt = 0; tt < 2; ++tt) {
    const f32x2 rsL = splat2(st[tt][0] * LOG2E), nbL = splat2(st[tt][1] * LOG2E);
    u32x4 o;
    o[0] = pack2v(mish2(fma2(lo2(a0[tt]), rsL, fma2(nbL, lo2(c4[0]), lo2(bb[0])))));
    o[1] = pack2v(mish2(fma2(hi2(a0[tt]), rsL, fma2(nbL, hi2(c4[0]), hi2(bb[0])))));
    o[2] = pack2v(mish2(fma2(lo2(a1[tt]), rsL, fma2(nbL, lo2(c4[1]), lo2(bb[1])))));
    o[3] = pack2v(mish2(fma2(hi2(a1[tt]), rsL, fma2(nbL, hi2(c4[1]), hi2(bb[1])))));
    st16(rs, skip ? OOB : OFF_ACT + (tt0 + tt) * AST + (wt >> 1) * 1024 + lane * 16, o);
  }
}
DI void load_aphi(const char* smem, int lane, bf16x8 (&ap)[5][3]) {
  const int lg = lane >> 4, l15 = lane & 15;
  const char* pa[6];
#pragma unroll
  for (int i = 0; i < 6; ++i) {
    int d0 = 16 * i + 4 * lg;
    int h = (d0 * 13) >> 8;
    bool ok = (h == (l15 & 3)) && i < 5;
    pa[i] = smem + (ok ? L_RAW + h * 800 + (d0 - 20 * h + (l15 >> 2)) * 40 : L_ZERO);
  }
#pragma unroll
  for (int t = 0; t < 5; ++t)
#pragma unroll
    for (int ks = 0; ks < 3; ++ks) ap[t][ks] = tr2(pa[2 * ks] + t * 8, pa[2 * ks + 1] + t * 8);
}
DI void pack_k(const char* smem, const f32x4& a, int t, f32x2 st, int lg, u32x2& kb) {
  f32x4 c4 = *(const f32x4*)(smem + L_CS + (t * 16 + 4 * lg) * 4);
  const f32x2 rsv = splat2(st[0]), nb = splat2(st[1]);
  kb = u32x2{pack2v(fma2(lo2(a), rsv, nb * lo2(c4))), pack2v(fma2(hi2(a), rsv, nb * hi2(c4)))};
}
DI void features(const u32x2 (&kb)[5], const bf16x8 (&ap)[5][3], u32x2 (&pos)[5], u32x2 (&neg)[5]) {
  bf16x8 b0 = mk8(kb[0], kb[1]), b1 = mk8(kb[2], kb[3]), b2 = mk8(kb[4], u32x2{0u, 0u});
  f32x4 pk[5];
#pragma unroll
  for (int t = 0; t < 5; ++t) pk[t] = MFMA16(ap[t][2], b2, MFMA16(ap[t][1], b1, MFMA16(ap[t][0], b0, (f32x4{0, 0, 0, 0}))));
  dual_softmax(pk, pos, neg);
}
DI void barrier() {
  waitl();
  __builtin_amdgcn_s_barrier();
  __builtin_amdgcn_sched_barrier(0);
}

template <int TYPE, int C, class G>
DI u64 first_pass(G& g, const P1& p, rsrc_t rs, char* smem, int rb, int g8, int t0w, int w, int lane STAMP_ARG) {
  constexpr int NT = G::NT, NWH = G::NWH;
  const int r = w & 3, lg = lane >> 4, l15 = lane & 15;
  const unsigned lds0 = lds_addr(smem);
  u32x2 ev;
  asm volatile("global_load_dwordx2 %0, %1, off sc1" : "=v"(ev) : "v"(p.ws + OFF_FLG) : "memory");
  const int job = TYPE * 8 + g8;
  const int myc = (rb + job) % 10;
  const int rot = (myc / ROTP) * ROTP;
  const int my_i = myc - rot;
  g.init(p, smem, rb, g8, t0w, rot, w, lane);
  if (C == 1) {
    int ci = r * 64 + lane;
    if (ci < 200) glds1((const char*)p.wphi + g8 * 3200 + ci * 16, lds0 + L_RAW + r * 1024);
  } else if (w == 3) {
    if (TYPE == 1 && lane < 28) glds1((const char*)p.b1 + t0w * 32 + lane * 16, lds0 + L_BIAS);
    if (lane < 32) *(unsigned*)(smem + L_ZERO + lane * 4) = 0u;
  }
  g.issue_w(); g.issue_x(0); g.issue_w();
  __builtin_amdgcn_s_barrier();
  g.issue_w(); g.issue_x(1); g.issue_w();
  g.issue_w(); g.issue_x(2);
  if (NWH == 6) g.issue_w();
  constexpr int AH = NWH - 4;
  const int ptj = w * 128 + rb * 8 + g8;
  const bool has_pt = TYPE == 0 && w < 6 && ptj < 740;
  const int ptu = ptj / 10, ptL = ptj - ptu * 10;
  if (has_pt) {
#pragma unroll
    for (int i = 0; i < 4; ++i) {
      int rho = i * 8 + (lane >> 3);
      const u16* src;
      if (ptu < 54) src = p.w2 + (unsigned)((32 * ptu + rho) * 640);
      else {
        int T = 2 * (ptu - 54) + (rho >> 4), Gq = (T * 13) >> 6;
        src = p.wo + (unsigned)(((4 * Gq + ((rho >> 2) & 3)) * 20 + 4 * (T - 5 * Gq) + (rho & 3)) * 640);
      }
      glds1((const char*)src + ptL * 128 + (lane & 7) * 16, lds0 + L_PT + w * 4096 + i * 1024);
    }
  }
  float cs[3] = {0.f, 0.f, 0.f}, sx[2] = {0.f, 0.f}, cd = 0.f;
  auto do_dots = [&](const Frags<NT>& f) {
    dots_x(sx[0], sx[1], f.x[C]);
    if (r == 0) { dots2(cs[0], cs[1], f.w[0], f.w[4]); if (NT > 8) dots2(cs[2], cd, f.w[NT > 8 ? 8 : 0], f.w[0]); }
    else if (r == 1) { dots2(cs[0], cs[1], f.w[1], f.w[NT > 5 ? 5 : 0]); if (NT > 9) dots2(cs[2], cd, f.w[NT > 9 ? 9 : 0], f.w[0]); }
    else if (r == 2) dots2(cs[0], cs[1], f.w[2], f.w[NT > 6 ? 6 : 0]);
    else dots2(cs[0], cs[1], f.w[3], f.w[NT > 7 ? 7 : 0]);
  };
  Frags<NT> f0, f1;
  if (has_pt) g.template wait<2, NWH - 2, 4>(); else g.template wait<2, NWH - 2, 0>();
  barrier();
  STAMP(1);
  g.rd_x(f0, 0, 0, 0); g.rd_x(f0, 0, 0, 1);
#pragma unroll
  for (int t = 0; t < NT; ++t) g.rd_w(f0, 0, t);
  if (TYPE == 1) {
    int tid = w * 64 + lane;
    if (tid < 224) *(float*)(smem + L_BIASL + tid * 4) = bf_lo(*(const u16*)(smem + L_BIAS + tid * 2)) * LOG2E;
  }
  asm volatile("" : "+v"(ev));
  const u64 tag = next_tag(((u64)(unsigned)__builtin_amdgcn_readfirstlane(ev[1]) << 32) | (unsigned)__builtin_amdgcn_readfirstlane(ev[0]));
  int xs = 0, hs = 0;
  u32x4 fa, fb;
  u32x2 tf;
  unsigned pv[10], pw[10];
  const char* tfp = p.ws + (lane < 10 ? F_ST + (rb * 16 + lane) * 8 : F_CS + (job * 16 + (lane < 20 ? lane - 10 : 0)) * 8);
  auto inc = [](int s) { return s == NWH - 1 ? 0 : s + 1; };
#define LD10(pv, q) \
    asm volatile("global_load_dword %0, %10, off sc1\n\tglobal_load_dword %1, %10, off offset:1280 sc1\n\t" \
                 "global_load_dword %2, %10, off offset:2560 sc1\n\tglobal_load_dword %3, %10, off offset:3840 sc1\n\t" \
                 "global_load_dword %4, %11, off sc1\n\tglobal_load_dword %5, %11, off offset:1280 sc1\n\t" \
                 "global_load_dword %6, %11, off offset:2560 sc1\n\tglobal_load_dword %7, %11, off offset:3840 sc1\n\t" \
                 "global_load_dword %8, %12, off sc1\n\tglobal_load_dword %9, %12, off offset:1280 sc1" \
                 : "=&v"(pv[0]), "=&v"(pv[1]), "=&v"(pv[2]), "=&v"(pv[3]), "=&v"(pv[4]), "=&v"(pv[5]), "=&v"(pv[6]), "=&v"(pv[7]), "=&v"(pv[8]), "=&v"(pv[9]) \
                 : "v"(q), "v"((q) + 5120), "v"((q) + 10240) : "memory")
#pragma unroll 1
  for (int i = 0; i < 8; ++i) {
    waitl();
    __builtin_amdgcn_sched_barrier(0);
    const int h1 = inc(hs), h2 = inc(h1);
    g.kstep(f0, f1, xs, 1, h1, true, 0, 0);
    if (i == my_i) do_dots(f0);
    const int nxs = xs == 2 ? 0 : xs + 1;
    if (TYPE == 0) {
      if (i == 0 && has_pt) g.template wait<1, AH, 4>();
      else if (i == 1) g.template wait<1, AH, 2>();
      else if (i == 2 && has_pt) g.template wait<1, AH, 6>();
      else if (i == 2) g.template wait<1, AH, 2>();
      else if (i == 3 && has_pt) g.template wait<1, AH, 4>();
      else g.template wait<1, AH, 0>();
    } else {
      if (i == 1) g.template wait<1, AH, 3>();
      else g.template wait<1, AH, 0>();
    }
    barrier();
    if (i == FLAG_I && w == 0 && lane < 3)
      st_flag(p.ws + (lane == 0 ? F_EARLY + blockIdx.x * 8 : (lane == 1 ? F_ST + (rb * 16 + job) * 8 : F_CS + (job * 16 + rb) * 8)), tag);
    g.kstep(f1, f0, nxs, 0, h2, true, i < 7 ? 3 : (NWH == 5 ? 1 : 0), xs);
    if (i == 0) {
      const u32x4 sent = {~0u, ~0u, ~0u, ~0u};
      if (TYPE == 0) {
        unsigned kb = OFF_KVP + (g8 * 16 + rb) * 15360 + lane * 16;
        st16(rs, kb + w * 1024, sent);
        st16(rs, w < 7 ? kb + (8 + w) * 1024 : (lane < 30 ? OFF_KVH + g8 * 7680 + rb * 480 + lane * 16 : OOB), sent);
      } else {
        const unsigned ob = OFF_O + (rb * 8 + w) * OST + ((5 * g8) >> 1) * 1024 + lane * 16;
        st16(rs, ob + 1024, sent);
        st16(rs, ob + ((g8 & 1) ? 2048 : 0), sent);
        st8(rs, ob + ((g8 & 1) ? 8 : 2048), u32x2{~0u, ~0u});
      }
    }
    if (i == 1 && has_pt) {
      const char* q = smem + L_PT + w * 4096 + (4 * lg + (l15 >> 2)) * 128 + (l15 & 3) * 8;
#pragma unroll
      for (int n = 0; n < 4; ++n) {
        bf16x8 f = tr2(q + n * 32, q + 2048 + n * 32);
        int N = 4 * ptL + n, ct = (N * 13) >> 6;
        st16(rs, OFF_W2F + ((ct * 74 + ptu) * 5 + (N - 5 * ct)) * 1024 + lane * 16, __builtin_bit_cast(u32x4, f));
      }
    }
    if (i == my_i) {
      do_dots(f1);
      float v = rscatter_add(sx[0], sx[1], cs[0], cs[1]);
      unsigned a = lg < 2 ? OFF_STP + (rb * 10 + myc) * 1280 + lg * 512 + (32 * r + 16 * C + l15) * 4
                          : OFF_CSP + (job * 10 + myc) * 1280 + ((G::unit_of(0) + r + (lg == 2 ? 0 : 4)) * 16 + l15) * 4;
      bool on = lg < 2 ? job < 10 : (rb < 10 && (lg == 2 || r + 4 < NT));
      st4(rs, on ? a : OOB, v);
      if (NT > 8) {
        float v2 = rscatter_add(cs[2], 0.f, 0.f, 0.f);
        st4(rs, (lg == 0 && rb < 10 && r + 8 < NT) ? OFF_CSP + (job * 10 + myc) * 1280 + ((G::unit_of(0) + r + 8) * 16 + l15) * 4 : OOB, v2);
      }
    }
    if (i == 6) {
      asm volatile("global_load_dwordx2 %0, %1, off sc1" : "=v"(tf) : "v"(tfp) : "memory");
      if (w == 5)
        asm volatile("global_load_dwordx4 %0, %2, off sc1\n\tglobal_load_dwordx4 %1, %2, off offset:16 sc1"
                     : "=&v"(fa), "=&v"(fb) : "v"(p.ws + F_EARLY + lane * 32) : "memory");
    }
    xs = nxs; hs = h2;
  }
  {
    waitl();
    __builtin_amdgcn_sched_barrier(0);
    const int h1 = inc(hs), h2 = inc(h1);
    g.kstep(f0, f1, xs, 1, h1, true, 0, 0);
    const int nxs = xs == 2 ? 0 : xs + 1;
    waitv<0>();
    barrier();
    asm volatile("" : "+v"(tf));
    if (!__all(tf[0] == (unsigned)tag && tf[1] == (unsigned)(tag >> 32))) pollq(tfp, tag);
    if (C == 1 && w == 5) {
      asm volatile("" : "+v"(fa), "+v"(fb));
      if (__all(flags_ok(fa, fb, tag))) {
        if (blockIdx.x == 0 && lane == 0) st_flag(p.ws + OFF_FLG, tag);
        set_cnt(smem, 3, lane);
        constexpr int NL = DMAP == 0 ? 370 : 740;
        const char* q2 = p.ws + OFF_W2F + (DMAP == 0 ? (blockIdx.x & 1) * 4 * 378880 : 0) + ((blockIdx.x >> 3) * NL + lane) * 128;
#pragma unroll
        for (int j = 0; j < (NL + 63) / 64; ++j) touch(q2 + (j * 64 + lane < NL ? j * 8192 : 0), lds0 + L_DUMMY);
      }
    }
    const int tid = w * 64 + lane;
    const char* q = p.ws + (tid < 128 ? OFF_STP + rb * 12800 + tid * 4 : OFF_CSP + job * 12800 + min(tid - 128, 303) * 4);
    LD10(pv, q);
    if constexpr (C == 0) { LD10(pw, q + 512); }
    g.kstep(f1, f0, nxs, 0, h2, true, 0, 0);
    xs = nxs; hs = h2;
  }
  waitl();
  __builtin_amdgcn_sched_barrier(0);
  g.kstep(f0, f1, xs, 1, inc(hs), true, 0, 0);
  waitl();
  __builtin_amdgcn_sched_barrier(0);
  g.kstep(f1, f0, 0, 0, 0, false, 0, 0);
  STAMP(2);
  {
    waitv<0>();
    asm volatile("" : "+v"(pv[0]), "+v"(pv[1]), "+v"(pv[2]), "+v"(pv[3]), "+v"(pv[4]), "+v"(pv[5]), "+v"(pv[6]), "+v"(pv[7]), "+v"(pv[8]), "+v"(pv[9]) :: "memory");
    float v = 0.f;
#pragma unroll
    for (int i = 0; i < 10; ++i) v += __uint_as_float(pv[i]);
    const int tid = w * 64 + lane;
    if constexpr (C == 0) {
      asm volatile("" : "+v"(pw[0]), "+v"(pw[1]), "+v"(pw[2]), "+v"(pw[3]), "+v"(pw[4]), "+v"(pw[5]), "+v"(pw[6]), "+v"(pw[7]), "+v"(pw[8]), "+v"(pw[9]) :: "memory");
      if (w < 2) {
        float v2 = 0.f;
#pragma unroll
        for (int i = 0; i < 10; ++i) v2 += __uint_as_float(pw[i]);
        float mean = v * (1.0f / 640.0f);
        float var = fmaxf(fmaf(-mean, mean, v2 * (1.0f / 640.0f)), 0.f);
        float rstd = __builtin_amdgcn_rsqf(var + 1e-5f);
        *(f32x2*)(smem + L_SUM + tid * 8) = f32x2{rstd, -rstd * mean};
      }
    }
    if (tid >= 128 && tid < 432) *(float*)(smem + L_CS + (tid - 128) * 4) = v;
  }
  barrier();
  STAMP(3);
  return tag;
}

constexpr int RING0 = 8192, RINGB = 35840;
DI int slot_of(int w) { return w >= 4 ? RING0 + (w - 4) * RINGB : RING0 + w * RINGB + 18432; }
DI int d_rt() { const int xcd = blockIdx.x & 7, idx = blockIdx.x >> 3; return DMAP == 0 ? (xcd >> 1) * 8 + (idx >> 2) : xcd * 4 + (idx >> 3); }
DI int d_ct() { const int xcd = blockIdx.x & 7, idx = blockIdx.x >> 3; return DMAP == 0 ? (xcd & 1) * 4 + (idx & 3) : (idx & 7); }
DI int d_u0(int w) { return (w - 4) * 14 - (w == 7 ? 1 : 0); }
DI void ring_fill(const P1& p, char* smem, int w, int lane, int s0, int s1) {
  const char* pb = p.ws + OFF_W2F + (d_ct() * 74 + d_u0(w)) * 5120 + lane * 16;
  const unsigned ringa = lds_addr(smem) + slot_of(w);
#pragma unroll
  for (int s = s0; s < s1; ++s) glds5_sc1(pb + s * 5120, ringa + s * 5120);
}
DI bool early_ok(char* smem) {
  unsigned v = __hip_atomic_load((LDS3 unsigned*)(smem + L_CNT + 12), __ATOMIC_RELAXED, __HIP_MEMORY_SCOPE_WORKGROUP);
  return __builtin_amdgcn_readfirstlane(v) != 0;
}

DI void need_early(const P1& p, char* smem, u64 tag, int lane) {
  if (early_ok(smem)) return;
  poll256(p.ws + F_EARLY, tag, lane);
  if (blockIdx.x == 0 && lane == 0) st_flag(p.ws + OFF_FLG, tag);
  set_cnt(smem, 3, lane);
}
DI void poll_word(const char* q) {
#pragma unroll 1
  for (int spin = 0; spin < (SPIN_MAX >> 3); ++spin) {
    unsigned a;
    asm volatile("global_load_dword %0, %1, off sc1\n\ts_waitcnt vmcnt(0)" : "=&v"(a) : "v"(q) : "memory");
    if (__all(a != ~0u)) break;
    __builtin_amdgcn_s_sleep(2);
  }
}

template <int C>
DI u64 body_a(const P1& p, rsrc_t rs, char* smem, int rb, int g8, int w, int lane, bool& filled STAMP_ARG) {
  const int r = w & 3, lg = lane >> 4, l15 = lane & 15;
  Gemm<0, C> g;
  const u64 tag = first_pass<0, C>(g, p, rs, smem, rb, g8, 0, w, lane STAMP_PASS);
  u32x2 kb[5];
  bf16x8 ap[5][3];
  load_aphi(smem, lane, ap);
  if constexpr (C == 0) {
    const f32x2 s0 = stat_of(smem, 32 * r + l15), s1 = stat_of(smem, 32 * r + 16 + l15);
#pragma unroll
    for (int t = 0; t < 5; ++t) {
      u32x2 k1;
      pack_k(smem, g.acc[t][1], t, s1, lg, k1);
      *(u32x2*)(smem + E_KB + (r * 5 + t) * 512 + lane * 8) = k1;
      pack_k(smem, g.acc[t][0], t, s0, lg, kb[t]);
    }
  } else {
    f32x4 sa[2], sb[2];
#pragma unroll
    for (int tt = 0; tt < 2; ++tt) {
      sa[tt] = *(const f32x4*)(smem + L_SUM + (32 * r + 16 * tt + 4 * lg) * 8);
      sb[tt] = *(const f32x4*)(smem + L_SUM + (32 * r + 16 * tt + 4 * lg + 2) * 8);
    }
#pragma unroll
    for (int t = 0; t < 5; ++t) {
      float c1 = *(const float*)(smem + L_CS + ((5 + t) * 16 + l15) * 4);
      unsigned o[4];
#pragma unroll
      for (int tt = 0; tt < 2; ++tt) {
        float y0 = fmaf(g.acc[t][tt][0], sa[tt][0], sa[tt][1] * c1);
        float y1 = fmaf(g.acc[t][tt][1], sa[tt][2], sa[tt][3] * c1);
        float y2 = fmaf(g.acc[t][tt][2], sb[tt][0], sb[tt][1] * c1);
        float y3 = fmaf(g.acc[t][tt][3], sb[tt][2], sb[tt][3] * c1);
        o[2 * tt] = pack2(y0, y1); o[2 * tt + 1] = pack2(y2, y3);
      }
      *(u32x4*)(smem + E_V + (r * 5 + t) * 1024 + lane * 16) = u32x4{o[0], o[1], o[2], o[3]};
    }
  }
  barrier();
  if constexpr (C == 1) {
#pragma unroll
    for (int t = 0; t < 5; ++t) kb[t] = *(const u32x2*)(smem + E_KB + (r * 5 + t) * 512 + lane * 8);
  }
  {
    const int tok = 32 * r + 16 * C + l15;
    u32x2 pos[5], neg[5];
    features(kb, ap, pos, neg);
#pragma unroll
    for (int t = 0; t < 5; ++t) {
      *(u32x2*)(smem + E_PHI + (t * 128 + tok) * 32 + lg * 8) = pos[t];
      *(u32x2*)(smem + E_PHI + ((5 + t) * 128 + tok) * 32 + lg * 8) = neg[t];
    }
  }
  barrier();
  STAMP(4);
  {
    const unsigned pa = E_PHI + (4 * lg + (l15 >> 2)) * 32 + (l15 & 3) * 8;
    const u32x4 one4 = {0x3f803f80u, 0x3f803f80u, 0x3f803f80u, 0x3f803f80u};
    const bool valid = (l15 >> 2) == lg;
    f32x4 a[3][3];
    const char* qa[3]; const char* vb[3]; bool hf[3];
#pragma unroll
    for (int k3 = 0; k3 < 3; ++k3) {
      const int un0 = w + 8 * k3, un = un0 < 20 ? un0 : 19;
      hf[k3] = un & 1;
      qa[k3] = smem + pa + (un >> 1) * 4096;
      vb[k3] = smem + E_V + (un & 1) * 3072 + lane * 16;
#pragma unroll
      for (int j = 0; j < 3; ++j) a[k3][j] = f32x4{0, 0, 0, 0};
    }
#pragma unroll
    for (int ks = 0; ks < 4; ++ks)
#pragma unroll
      for (int k3 = 0; k3 < 3; ++k3) {
        bf16x8 af = tr2(qa[k3] + ks * 1024, qa[k3] + ks * 1024 + 512);
        const char* v = vb[k3] + ks * 5120;
        a[k3][0] = MFMA16(af, *(const bf16x8*)(v), a[k3][0]);
        a[k3][1] = MFMA16(af, *(const bf16x8*)(v + 1024), a[k3][1]);
        u32x4 b2 = *(const u32x4*)(v + (hf[k3] ? 0 : 2048));
        b2 = hf[k3] ? one4 : b2;
        a[k3][2] = MFMA16(af, __builtin_bit_cast(bf16x8, b2), a[k3][2]);
      }
#pragma unroll
    for (int k3 = 0; k3 < 3; ++k3) {
      const int un0 = w + 8 * k3;
      unsigned dst = (valid && un0 < 20) ? OFF_KVP + ((g8 * 16 + rb) * 60 + un0 * 3) * 256 + (lg * 4 + (l15 & 3)) * 16 : OOB;
#pragma unroll
      for (int j = 0; j < 3; ++j) st16(rs, dst + j * 256, __builtin_bit_cast(u32x4, a[k3][j]));
    }
  }
  (void)arrive(smem, 1, 8, lane);
  STAMP(5);
  if (C == 0 && w == 1 && rb < 15) {
    need_early(p, smem, tag, lane);
    f32x4 a;
#pragma unroll 1
    for (int spin = 0; spin < 8; ++spin) {
      poll_word(p.ws + OFF_KVP + (g8 * 16 + l15) * 15360 + rb * 1024 + lg * 256 + 252);
      u32x4 kv[16];
#pragma unroll
      for (int i = 0; i < 16; ++i) kv[i] = ld16(rs, OFF_KVP + (g8 * 16 + i) * 15360 + rb * 1024 + lane * 16);
      bool ok = ready(kv[0]);
      a = __builtin_bit_cast(f32x4, kv[0]);
#pragma unroll
      for (int i = 1; i < 16; ++i) { ok = ok && ready(kv[i]); a += __builtin_bit_cast(f32x4, kv[i]); }
      if (__all(ok)) break;
    }
    int tau = rb * 4 + lg, m = (tau * 43) >> 8, tn = tau - 6 * m;
    int sg = m >= 5 ? 1 : 0, t = m - 5 * sg;
    int ks = t < 4 ? sg * 2 + (t >> 1) : 4, hf = t < 4 ? (t & 1) : sg;
    st8(rs, OFF_KVH + g8 * 7680 + (tn * 5 + ks) * 256 + l15 * 16 + hf * 8, u32x2{pack2(a[0], a[1]), pack2(a[2], a[3])});
    set_cnt(smem, 2, lane);
  } else if (rb < 15) wait_cnt(smem, 2, 1);
  wait_cnt(smem, 1, 8);
  STAMP(6);
  return tag;
}

template <int C>
DI u64 body_b(const P1& p, rsrc_t rs, char* smem, int rb, int g8, int w, int lane, bool& filled STAMP_ARG) {
  const int r = w & 3, lg = lane >> 4, l15 = lane & 15;
  const int t0w = min(14 * g8, 94);
  Gemm<1, C> g;
  const u64 tag = first_pass<1, C>(g, p, rs, smem, rb, g8, t0w, w, lane STAMP_PASS);
  f32x2 st[2] = {stat_of(smem, 32 * r + l15), stat_of(smem, 32 * r + 16 + l15)};
  if constexpr (C == 1) {
    filled = early_ok(smem);
    __builtin_amdgcn_s_setprio(2);
#pragma unroll
    for (int pp = 0; pp < 5; ++pp) {
      if (filled) ring_fill(p, smem, w, lane, pp < 2 ? 2 * pp : pp + 2, pp < 2 ? 2 * pp + 2 : pp + 3);
      __builtin_amdgcn_sched_barrier(0);
      mish_pair(rs, smem, g.acc[2 * pp], g.acc[2 * pp + 1], 9 + 2 * pp, 4 + 2 * pp, t0w + 4 + 2 * pp, false, st, rb * 8 + 2 * r, lane);
      __builtin_amdgcn_sched_barrier(0);
    }
    __builtin_amdgcn_s_setprio(0);
    STAMP(4);
    drain();
    if (arrive(smem, 0, 8, lane) && lane == 0) st_flag(p.ws + F_ACT + (rb * 16 + g8) * 8, tag);
    STAMP(5);
  } else {
    __builtin_amdgcn_s_setprio(2);
    mish_pair(rs, smem, g.acc[5], g.acc[6], 5, 0, t0w, g8 == 7, st, rb * 8 + 2 * r, lane);
    mish_pair(rs, smem, g.acc[7], g.acc[8], 7, 2, t0w + 2, g8 == 7, st, rb * 8 + 2 * r, lane);
    __builtin_amdgcn_s_setprio(0);
    STAMP(4);
    if (w == 0 && lane < 30) *(u32x4*)(smem + E_KV + lane * 272 + 256) = u32x4{0u, 0u, 0u, 0u};
    bf16x8 ap[5][3];
    load_aphi(smem, lane, ap);
    bf16x8 ph[2][5];
#pragma unroll
    for (int tt = 0; tt < 2; ++tt) {
      u32x2 kb[5], pos[5], neg[5];
#pragma unroll
      for (int t = 0; t < 5; ++t) pack_k(smem, g.acc[t][tt], t, st[tt], lg, kb[t]);
      if (tt == 0) {
        drain();
        if (arrive(smem, 0, 8, lane) && lane == 0) st_flag(p.ws + F_ACT + (rb * 16 + g8) * 8, tag);
      }
      features(kb, ap, pos, neg);
      ph[tt][0] = mk8(pos[0], pos[1]); ph[tt][1] = mk8(pos[2], pos[3]);
      ph[tt][2] = mk8(neg[0], neg[1]); ph[tt][3] = mk8(neg[2], neg[3]); ph[tt][4] = mk8(pos[4], neg[4]);
#pragma unroll
      for (int k = 0; k < 5; ++k) asm volatile("" : "+v"(ph[tt][k]));
    }
    STAMP(5);
    if (w == 0) {
      need_early(p, smem, tag, lane);
      poll_word(p.ws + OFF_KVH + g8 * 7680 + (lane < 60 ? (lane >> 1) * 256 + (lane & 1) * 8 : 0));
      set_cnt(smem, 4, lane);
    } else wait_cnt(smem, 4, 1);
    {
      const int e0 = w * 120 + lane, e1 = e0 + 64;
      u32x4 a, b;
#pragma unroll 1
      for (int spin = 0; spin < SPIN_MAX; ++spin) {
        a = ld16(rs, OFF_KVH + g8 * 7680 + e0 * 16);
        b = ld16(rs, lane < 56 ? OFF_KVH + g8 * 7680 + e1 * 16 : OOB);
        if (__all(ready(a) && ready(b))) break;
        __builtin_amdgcn_s_sleep(2);
      }
      *(u32x4*)(smem + E_KV + (e0 >> 4) * 272 + (e0 & 15) * 16) = a;
      if (lane < 56) *(u32x4*)(smem + E_KV + (e1 >> 4) * 272 + (e1 & 15) * 16) = b;
    }
    waitl();
    asm volatile("" ::: "memory");
    (void)arrive(smem, 1, 4, lane);
    wait_cnt(smem, 1, 4);
    STAMP(6);
    const char* ks_ = smem + E_KV + (((l15 >> 2) == lg) ? (lg * 4 + (l15 & 3)) * 16 : 256);
    f32x4 n[2][6];
#pragma unroll
    for (int tn = 0; tn < 6; ++tn) {
      n[0][tn] = f32x4{0, 0, 0, 0}; n[1][tn] = f32x4{0, 0, 0, 0};
#pragma unroll
      for (int ks = 0; ks < 5; ++ks) {
        bf16x8 ka = *(const bf16x8*)(ks_ + (tn * 5 + ks) * 272);
        n[0][tn] = MFMA16(ka, ph[0][ks], n[0][tn]);
        n[1][tn] = MFMA16(ka, ph[1][ks], n[1][tn]);
      }
    }
#pragma unroll
    for (int tt = 0; tt < 2; ++tt) {
      const f32x2 rc = splat2(__builtin_amdgcn_rcpf(n[tt][5][0] + 1e-6f));
      u32x2 o[5];
#pragma unroll
      for (int t = 0; t < 5; ++t) o[t] = u32x2{pack2v(lo2(n[tt][t]) * rc), pack2v(hi2(n[tt][t]) * rc)};
      const unsigned ob = OFF_O + (rb * 8 + 2 * r + tt) * OST + ((5 * g8) >> 1) * 1024 + lane * 16;
      if ((g8 & 1) == 0) {
        st16(rs, ob, u32x4{o[0][0], o[0][1], o[1][0], o[1][1]});
        st16(rs, ob + 1024, u32x4{o[2][0], o[2][1], o[3][0], o[3][1]});
        st8(rs, ob + 2048, o[4]);
      } else {
        st8(rs, ob + 8, o[0]);
        st16(rs, ob + 1024, u32x4{o[1][0], o[1][1], o[2][0], o[2][1]});
        st16(rs, ob + 2048, u32x4{o[3][0], o[3][1], o[4][0], o[4][1]});
      }
    }
  }
  return tag;
}

#define GLD(dst, ptr, off) asm volatile("global_load_dwordx4 %0, %1, off offset:" #off " sc1" : "=v"(dst) : "v"(ptr) : "memory")
#define WAITV(n) do { asm volatile("s_waitcnt vmcnt(%0)" :: "i"(n) : "memory"); __builtin_amdgcn_sched_barrier(0); } while (0)

#define ISSUE_A(A, s) do { GLD(A[s][0], pa, 0); GLD(A[s][1], pa1, 0); GLD(A[s][2], pa2, 0); GLD(A[s][3], pa3, 0); \
    pa += 1024; pa1 += 1024; pa2 += 1024; pa3 += 1024; } while (0)
#define ISSUE_B(B, s) do { GLD(B[s][0], pb, -2048); GLD(B[s][1], pb, -1024); GLD(B[s][2], pb, 0); GLD(B[s][3], pb, 1024); GLD(B[s][4], pb, 2048); \
    pb += 5120; } while (0)
DI void wait_flags(char* smem, int cnt, bool poller, const char* flags, int n, u64 tag, int lane) {
  if (poller) { polln(flags, n, tag, lane); set_cnt(smem, cnt, lane); }
  else { wait_cnt(smem, cnt, 1); drain(); }
}
DI void stream_o(const char* pa, const char* pb, const char* pw, char* smem, bool poller, int lane, int rot, f32x4 (&acc)[5][4] STAMP_ARG) {
  u32x4 A[5][4], B[4][5];
#pragma unroll
  for (int s = 0; s < 4; ++s) ISSUE_B(B, s);
  if (poller) { poll_word(pw); set_cnt(smem, 7, lane); }
  else wait_cnt(smem, 7, 1);
#pragma unroll 1
  for (int spin = 0; spin < (SPIN_MAX >> 2); ++spin) {
    const char* q0 = pa + 2048 + rot * OST; const char* q1 = pa + 2048 + ((rot + 1) & 3) * OST;
    const char* q2 = pa + 2048 + ((rot + 2) & 3) * OST; const char* q3 = pa + 2048 + ((rot + 3) & 3) * OST;
#define LD5(t, q) GLD(A[0][t], q, -2048); GLD(A[1][t], q, -1024); GLD(A[2][t], q, 0); GLD(A[3][t], q, 1024); GLD(A[4][t], q, 2048);
    LD5(0, q0) LD5(1, q1) LD5(2, q2) LD5(3, q3)
    WAITV(0);
    unsigned mx = 0;
#pragma unroll
    for (int s = 0; s < 5; ++s)
#pragma unroll
      for (int t = 0; t < 4; ++t) {
        asm volatile("" : "+v"(A[s][t]));
        mx = max(max(mx, A[s][t][0]), max(A[s][t][1], max(A[s][t][2], A[s][t][3])));
      }
    if (__all(mx != ~0u)) break;
    __builtin_amdgcn_s_sleep(4);
  }
  STAMP(8);
#define OSTEP(sa, sb) \
  _Pragma("unroll") for (int i = 0; i < 5; ++i) asm volatile("" : "+v"(B[sb][i])); \
  _Pragma("unroll") for (int n = 0; n < 5; ++n) _Pragma("unroll") for (int t = 0; t < 4; ++t) \
    acc[n][t] = MFMA16(__builtin_bit_cast(bf16x8, B[sb][n]), __builtin_bit_cast(bf16x8, A[sa][t]), acc[n][t]); \
  __builtin_amdgcn_sched_barrier(0);
  OSTEP(0, 0)
  ISSUE_B(B, 0);
  OSTEP(1, 1) OSTEP(2, 2) OSTEP(3, 3)
  WAITV(0);
  OSTEP(4, 0)
}

template <int NST, int NA, int NB, int S>
DI void lds_steps(const char*& pa, const char*& pa1, const char*& pa2, const char*& pa3, const char*& pb, const char* ring, unsigned ringa,
                  u32x4 (&A)[NA][4], f32x4 (&acc)[5][4]) {
  if constexpr (S < NST) {
    constexpr int ja = S - NA, jb = S - NB;
    auto cnt = [](int j0, bool skipA) constexpr {
      int n = 0;
      for (int j = j0; j < S; ++j) {
        if (j + NA < NST && !(skipA && j == j0)) n += 4;
        if (j + NB < NST) n += 5;
      }
      return n;
    };
    constexpr int ya = ja < 0 ? 4 * ((NA < NST ? NA : NST) - 1 - S) + cnt(0, false) : cnt(ja, true);
    constexpr int yb = jb < 0 ? 1000 : cnt(jb + 1, false);
    constexpr int y = ya < yb ? ya : yb;
    WAITV(y);
    bf16x8 bf[5];
#pragma unroll
    for (int n = 0; n < 5; ++n) bf[n] = *(const bf16x8*)(ring + (S % NB) * 5120 + n * 1024);
#pragma unroll
    for (int i = 0; i < 4; ++i) asm volatile("" : "+v"(A[S % NA][i]));
#define LSTEP(t, ptr) \
    _Pragma("unroll") for (int n = 0; n < 5; ++n) acc[n][t] = MFMA16(bf[n], __builtin_bit_cast(bf16x8, A[S % NA][t]), acc[n][t]); \
    __builtin_amdgcn_sched_barrier(0); \
    if constexpr (S + NA < NST) { GLD(A[S % NA][t], ptr, 0); ptr += 1024; } \
    __builtin_amdgcn_sched_barrier(0);
    LSTEP(0, pa) LSTEP(1, pa1) LSTEP(2, pa2) LSTEP(3, pa3)
    if constexpr (S + NB < NST) { glds5_sc1(pb, ringa + (S % NB) * 5120); pb += 5120; }
    lds_steps<NST, NA, NB, S + 1>(pa, pa1, pa2, pa3, pb, ring, ringa, A, acc);
  }
}
template <int NST, int NA, int NB>
DI void stream_lds(const char* pa, unsigned astr, const char* pb, char* smem, int roff, int cnt, bool poller, const char* flags, int nflags,
                   u64 tag, int lane, int rot, f32x4 (&acc)[5][4] STAMP_ARG) {
  const char* pa1 = pa + ((rot + 1) & 3) * astr; const char* pa2 = pa + ((rot + 2) & 3) * astr; const char* pa3 = pa + ((rot + 3) & 3) * astr;
  pa += rot * astr;
  const unsigned ringa = lds_addr(smem) + roff;
  u32x4 A[NA][4];
  pb += NB * 5120;
  if (poller) { polln(flags, nflags, tag, lane); set_cnt(smem, cnt, lane); }
  else wait_cnt(smem, cnt, 1);
  STAMP(8);
#pragma unroll
  for (int s = 0; s < NA; ++s) ISSUE_A(A, s);
  lds_steps<NST, NA, NB, 0>(pa, pa1, pa2, pa3, pb, smem + roff + lane * 16, ringa, A, acc);
}

template <int C>
DI void finish(const P1& p, char* smem, int rt, int ct, int w, int lane, const f32x4 (&acc)[5][4] STAMP_ARG) {
  constexpr int N0 = C == 0 ? 0 : 3, NN = C == 0 ? 3 : 2;
  const int lg = lane >> 4, l15 = lane & 15;
  const unsigned o0 = (unsigned)((rt * 64 + (w & 3) * 16 + l15) * 640 + ct * 80 + N0 * 16 + 4 * lg);
  u32x2 xv[NN];
#pragma unroll
  for (int m = 0; m < NN; ++m) xv[m] = *(const u32x2*)(p.x + o0 + 16 * m);
  char* dst = smem + slot_of(w) + lane * 16;
#pragma unroll
  for (int i = 1; i < 4; ++i)
#pragma unroll
    for (int n = 0; n < 5; ++n) *(f32x4*)(dst + ((i - 1) * 5 + n) * 1024) = acc[n][i];
#pragma unroll
  for (int m = 0; m < 5 - NN; ++m) *(f32x4*)(dst + (15 + m) * 1024) = acc[(C == 0 ? 3 : 0) + m][0];
  __syncthreads();
  STAMP(10);
  const char* same[3]; const char* other[3];
#pragma unroll
  for (int j = 1; j < 4; ++j) {
    const int v = (w - j) & 3;
    same[j - 1] = smem + slot_of(4 * C + v) + ((j - 1) * 5 + N0) * 1024 + lane * 16;
    other[j - 1] = smem + slot_of(4 * (1 - C) + v) + ((j - 1) * 5 + N0) * 1024 + lane * 16;
  }
  const char* partner = smem + slot_of(w ^ 4) + 15 * 1024 + lane * 16;
#pragma unroll
  for (int m = 0; m < NN; ++m) {
    f32x4 mine = (acc[N0 + m][0] + *(const f32x4*)(same[0] + m * 1024)) + (*(const f32x4*)(same[1] + m * 1024) + *(const f32x4*)(same[2] + m * 1024));
    f32x4 theirs = (*(const f32x4*)(partner + m * 1024) + *(const f32x4*)(other[0] + m * 1024)) +
                   (*(const f32x4*)(other[1] + m * 1024) + *(const f32x4*)(other[2] + m * 1024));
    const f32x4 ssum = C == 0 ? mine : theirs, bsum = C == 0 ? theirs : mine;
    float xs[4] = {bf_lo(xv[m][0]), bf_hi(xv[m][0]), bf_lo(xv[m][1]), bf_hi(xv[m][1])};
    float r[4];
#pragma unroll
    for (int j = 0; j < 4; ++j) {
      float bb = bsum[j];
      float g = bb * __builtin_amdgcn_rcpf(1.0f + __builtin_amdgcn_exp2f(-bb * LOG2E));
      r[j] = fmaf(ssum[j], g, xs[j]);
    }
    *(u32x2*)(p.out + o0 + 16 * m) = u32x2{pack2(r[0], r[1]), pack2(r[2], r[3])};
  }
}

__global__ __launch_bounds__(512, 2) void k_fused(P1 p) {
  extern __shared__ __attribute__((aligned(16))) char smem[];
  const int b = blockIdx.x, xcd = b & 7, idx = b >> 3;
  const int w = __builtin_amdgcn_readfirstlane(threadIdx.x >> 6), lane = threadIdx.x & 63;
  const int rb = (xcd >> 2) * 8 + (idx >> 2);
  const int jl = idx & 3, xc = xcd & 3;
  const int type = jl >> 1, g8 = 2 * xc + (jl & 1);
  const int c = w >> 2;
  STAMP_DECL; STAMP(0);
  const rsrc_t rs = __builtin_amdgcn_make_buffer_rsrc(p.ws, 0, WS_USED, 0x00020000);
  {
    u64 pc;
    asm volatile("s_getpc_b64 %0" : "=s"(pc));
    const char* q = (const char*)(pc & ~127ull) + (w * 64 + lane) * 128;
    touch(q, lds_addr(smem) + L_DUMMY);
    if (w < 1) touch(q + 65536, lds_addr(smem) + L_DUMMY);
  }
  u64 tag;
  bool filled = false;
  if (type == 0) {
    if (c == 0) tag = body_a<0>(p, rs, smem, rb, g8, w, lane, filled STAMP_PASS);
    else tag = body_a<1>(p, rs, smem, rb, g8, w, lane, filled STAMP_PASS);
  } else {
    if (c == 0) tag = body_b<0>(p, rs, smem, rb, g8, w, lane, filled STAMP_PASS);
    else tag = body_b<1>(p, rs, smem, rb, g8, w, lane, filled STAMP_PASS);
  }
  STAMP(7);
  if (w == 5) {
    unsigned v = __hip_atomic_load((LDS3 unsigned*)(smem + L_CNT + 12), __ATOMIC_RELAXED, __HIP_MEMORY_SCOPE_WORKGROUP);
    if (__builtin_amdgcn_readfirstlane(v) == 0) {
      poll256(p.ws + F_EARLY, tag, lane);
      if (blockIdx.x == 0 && lane == 0) st_flag(p.ws + OFF_FLG, tag);
      set_cnt(smem, 3, lane);
    }
  } else wait_cnt(smem, 3, 1);
  const int rt = DMAP == 0 ? (xcd >> 1) * 8 + (idx >> 2) : xcd * 4 + (idx >> 3);
  const int ct = DMAP == 0 ? (xcd & 1) * 4 + (idx & 3) : (idx & 7);
  f32x4 acc[5][4];
#pragma unroll
  for (int n = 0; n < 5; ++n)
#pragma unroll
    for (int t = 0; t < 4; ++t) acc[n][t] = f32x4{0, 0, 0, 0};
  if (w >= 4) {
    const int u0 = (w - 4) * 14 - (w == 7 ? 1 : 0);
    const char* pa = p.ws + OFF_ACT + (rt * 4) * AST + u0 * 1024 + lane * 16;
    const char* pb = p.ws + OFF_W2F + (ct * 74 + u0) * 5120 + lane * 16;
    const char* fact = p.ws + F_ACT + (rt >> 1) * 128;
    if (!filled) ring_fill(p, smem, w, lane, 0, 7);
    if (w < 6) stream_lds<14, NA_ACT, 7>(pa, AST, pb, smem, slot_of(w), 5, w == 4, fact, 8, tag, lane, w & 3, acc STAMP_PASS);
    else stream_lds<13, NA_ACT, 7>(pa, AST, pb, smem, slot_of(w), 5, false, fact, 8, tag, lane, w & 3, acc STAMP_PASS);
    (void)arrive(smem, 8, 4, lane);
  } else {
    stream_o(p.ws + OFF_O + (rt * 4) * OST + 5 * w * 1024 + lane * 16,
             p.ws + OFF_W2F + (ct * 74 + 54 + 5 * w) * 5120 + lane * 16 + 2048,
             p.ws + OFF_O + (rt * 4 + 1 + 2 * ((lane >> 3) & 1)) * OST + (((5 * (lane & 7)) >> 1) + 2) * 1024 + 1008 - ((lane & 1) ? 0 : 1024) , smem, w == 0, lane, w & 3, acc STAMP_PASS);
    wait_cnt(smem, 8, 4);
  }
  STAMP(9);
  if (w >= 4) finish<1>(p, smem, rt, ct, w, lane, acc STAMP_PASS);
  else finish<0>(p, smem, rt, ct, w, lane, acc STAMP_PASS);
  STAMP(11);
  STAMP_FLUSH(p.ws, blockIdx.x * 8 + w);
}
}

extern "C" void kernel_launch(void* const* d_in, const int* in_sizes, int n_in, void* d_out, int out_size, void* d_ws,
                              size_t ws_size, hipStream_t stream) {
  using namespace hk;
  P1 p;
  p.x = (const u16*)d_in[0]; p.wqkv = (const u16*)d_in[1]; p.wphi = (const u16*)d_in[2]; p.wo = (const u16*)d_in[3];
  p.w1 = (const u16*)d_in[5]; p.b1 = (const u16*)d_in[6]; p.w2 = (const u16*)d_in[7]; p.ws = (char*)d_ws; p.out = (u16*)d_out;
  (void)hipFuncSetAttribute((const void*)k_fused, hipFuncAttributeMaxDynamicSharedMemorySize, L_END);
  k_fused<<<256, 512, L_END, stream>>>(p);
}
```

```cpp
#include <hip/hip_runtime.h>
#include <hip/hip_bf16.h>

typedef unsigned short u16;
typedef __bf16 bf16x2 __attribute__((ext_vector_type(2)));
typedef __bf16 bf16x4 __attribute__((ext_vector_type(4)));
typedef __bf16 bf16x8 __attribute__((ext_vector_type(8)));
typedef float f32x2 __attribute__((ext_vector_type(2)));
typedef float f32x4 __attribute__((ext_vector_type(4)));
typedef unsigned u32x2 __attribute__((ext_vector_type(2)));
typedef unsigned u32x4 __attribute__((ext_vector_type(4)));
#define LDS3 __attribute__((address_space(3)))
#define DI __device__ __forceinline__
#define MFMA16(a, b, c) __builtin_amdgcn_mfma_f32_16x16x32_bf16((a), (b), (c), 0, 0, 0)

#define STAMP(i)
#define STAMP_DECL
#define STAMP_FLUSH(ws, slot)
namespace hk {
typedef unsigned long long u64;
constexpr float LOG2E = 1.4426950408889634f;
constexpr float LN2 = 0.6931471805599453f;
#define ROTP 1
#define NA_ACT 3
#define DMAP 0
constexpr int FLAG_I = 4;
#define APAD 256
constexpr unsigned AST = 54 * 1024 + APAD, OST = 20 * 1024 + APAD;
constexpr unsigned OFF_ACT = 0;
constexpr unsigned OFF_O = OFF_ACT + 128 * AST;
constexpr unsigned OFF_W2F = OFF_O + 128 * OST;
constexpr unsigned OFF_KVP = OFF_W2F + 8 * 74 * 5 * 1024;
constexpr unsigned OFF_KVH = OFF_KVP + 8 * 16 * 60 * 256;
constexpr unsigned OFF_STP = OFF_KVH + 8 * 30 * 256;
constexpr unsigned OFF_CSP = OFF_STP + 16 * 12800;
constexpr unsigned OFF_FLG = OFF_CSP + 16 * 12800;
constexpr unsigned F_EARLY = OFF_FLG + 128;
constexpr unsigned F_ACT = F_EARLY + 2048;
constexpr unsigned F_KVP = F_ACT + 2048;
constexpr unsigned F_KVH = F_KVP + 1024;
constexpr unsigned F_O = F_KVH + 1024;
constexpr unsigned F_ST = F_O + 2048;
constexpr unsigned F_CS = F_ST + 2048;
constexpr unsigned WS_USED = F_CS + 2048;
constexpr unsigned OOB = 0x7ffff000u;
constexpr int XB = 16384, WB0 = 3 * XB;
constexpr int L_PT = 118784;
constexpr int L_DUMMY = 151552;
constexpr int L_RAW = 155648;
constexpr int L_BIAS = L_RAW + 3200;
constexpr int L_ZERO = L_BIAS + 448;
constexpr int L_CNT = L_ZERO + 64;
constexpr int L_BIASL = L_CNT + 64;
constexpr int L_CS = L_BIASL + 896;
constexpr int L_SUM = L_CS + 1216;
constexpr int L_END = 163840;
static_assert(L_SUM + 1024 <= L_END - 256, "lds");
constexpr int E_PHI = 0;
constexpr int E_V = 40960;
constexpr int E_KB = 61440;
constexpr int E_KV = 0;

DI unsigned pack2(float a, float b) {
  f32x2 v = {a, b};
  return __builtin_bit_cast(unsigned, __builtin_convertvector(v, bf16x2));
}
DI unsigned pack2v(f32x2 v) { return __builtin_bit_cast(unsigned, __builtin_convertvector(v, bf16x2)); }
DI bf16x2 as_bf2(unsigned u) { return __builtin_bit_cast(bf16x2, u); }
DI float bf_lo(unsigned u) { return __uint_as_float(u << 16); }
DI float bf_hi(unsigned u) { return __uint_as_float(u & 0xffff0000u); }
DI float rscatter_add(float m0, float m1, float m2, float m3) {
  auto a = __builtin_amdgcn_permlane16_swap(__float_as_uint(m0), __float_as_uint(m1), false, false);
  auto b = __builtin_amdgcn_permlane16_swap(__float_as_uint(m2), __float_as_uint(m3), false, false);
  float A = __uint_as_float(a[0]) + __uint_as_float(a[1]);
  float B = __uint_as_float(b[0]) + __uint_as_float(b[1]);
  auto c = __builtin_amdgcn_permlane32_swap(__float_as_uint(A), __float_as_uint(B), false, false);
  return __uint_as_float(c[0]) + __uint_as_float(c[1]);
}
DI f32x2 lo2(f32x4 v) { return __builtin_shufflevector(v, v, 0, 1); }
DI f32x2 hi2(f32x4 v) { return __builtin_shufflevector(v, v, 2, 3); }
DI f32x2 fma2(f32x2 a, f32x2 b, f32x2 c) { return __builtin_elementwise_fma(a, b, c); }
DI f32x2 splat2(float a) { return f32x2{a, a}; }
DI f32x2 exp2v(f32x2 a) { return f32x2{__builtin_amdgcn_exp2f(a[0]), __builtin_amdgcn_exp2f(a[1])}; }
DI f32x2 rcpv(f32x2 a) { return f32x2{__builtin_amdgcn_rcpf(a[0]), __builtin_amdgcn_rcpf(a[1])}; }
DI void dual_softmax(const f32x4 (&p)[5], u32x2 (&pos)[5], u32x2 (&neg)[5]) {
  float mx = fmaxf(p[0][0], p[0][1]), mn = fminf(p[0][0], p[0][1]);
  mx = fmaxf(fmaxf(mx, p[0][2]), p[0][3]); mn = fminf(fminf(mn, p[0][2]), p[0][3]);
#pragma unroll
  for (int t = 1; t < 5; ++t) {
    mx = fmaxf(fmaxf(mx, p[t][0]), p[t][1]); mx = fmaxf(fmaxf(mx, p[t][2]), p[t][3]);
    mn = fminf(fminf(mn, p[t][0]), p[t][1]); mn = fminf(fminf(mn, p[t][2]), p[t][3]);
  }
  const f32x2 L = splat2(LOG2E), nL = splat2(-LOG2E), cx = splat2(-mx * LOG2E), cn = splat2(mn * LOG2E);
  f32x2 ep[10], en[10];
#pragma unroll
  for (int t = 0; t < 5; ++t) {
    ep[2 * t] = exp2v(fma2(lo2(p[t]), L, cx)); ep[2 * t + 1] = exp2v(fma2(hi2(p[t]), L, cx));
    en[2 * t] = exp2v(fma2(lo2(p[t]), nL, cn)); en[2 * t + 1] = exp2v(fma2(hi2(p[t]), nL, cn));
  }
  f32x2 sp = ep[0], sn = en[0];
#pragma unroll
  for (int i = 1; i < 10; ++i) { sp += ep[i]; sn += en[i]; }
  const f32x2 ip = splat2(__builtin_amdgcn_rcpf(sp[0] + sp[1])), in_ = splat2(__builtin_amdgcn_rcpf(sn[0] + sn[1]));
#pragma unroll
  for (int t = 0; t < 5; ++t) {
    pos[t] = u32x2{pack2v(ep[2 * t] * ip), pack2v(ep[2 * t + 1] * ip)};
    neg[t] = u32x2{pack2v(en[2 * t] * in_), pack2v(en[2 * t + 1] * in_)};
  }
}
DI f32x2 mish2(f32x2 hl) {
  f32x2 e = exp2v(hl);
  f32x2 d = fma2(e, e + splat2(2.0f), splat2(2.0f));
  f32x2 t = fma2(rcpv(d), splat2(-2.0f * LN2), splat2(LN2));
  return hl * t;
}
DI bf16x8 mk8(u32x2 a, u32x2 b) {
  u32x4 v = {a[0], a[1], b[0], b[1]};
  return __builtin_bit_cast(bf16x8, v);
}
DI bf16x8 tr2(const char* a, const char* b) {
  bf16x4 lo = __builtin_amdgcn_ds_read_tr16_b64_v4bf16((LDS3 bf16x4*)(a));
  bf16x4 hi = __builtin_amdgcn_ds_read_tr16_b64_v4bf16((LDS3 bf16x4*)(b));
  return __builtin_shufflevector(lo, hi, 0, 1, 2, 3, 4, 5, 6, 7);
}

struct P1 {
  const u16* x; const u16* wqkv; const u16* wphi; const u16* wo; const u16* w1; const u16* b1; const u16* w2;
  char* ws; u16* out;
};
typedef __amdgpu_buffer_rsrc_t rsrc_t;

DI unsigned lds_addr(const void* p) { return (unsigned)(size_t)(LDS3 const char*)p; }
DI void glds1(const void* g, unsigned lds) {
  unsigned keep;
  asm volatile("s_mov_b32 %0, m0\n\ts_mov_b32 m0, %2\n\ts_nop 0\n\tglobal_load_lds_dwordx4 %1, off\n\ts_mov_b32 m0, %0"
               : "=&s"(keep) : "v"(g), "s"(lds) : "memory");
}
DI void glds5_sc1(const void* g, unsigned lds) {
  unsigned keep;
  asm volatile("s_mov_b32 %0, m0\n\t"
               "s_mov_b32 m0, %2\n\ts_nop 0\n\tglobal_load_lds_dwordx4 %1, off sc1\n\t"
               "global_load_lds_dwordx4 %1, off offset:1024 sc1\n\t"
               "global_load_lds_dwordx4 %1, off offset:2048 sc1\n\t"
               "global_load_lds_dwordx4 %1, off offset:3072 sc1\n\t"
               "s_add_u32 m0, %2, 0x1000\n\ts_nop 0\n\tglobal_load_lds_dwordx4 %3, off sc1\n\t"
               "s_mov_b32 m0, %0"
               : "=&s"(keep) : "v"(g), "s"(lds), "v"((const char*)g + 4096) : "memory", "scc");
}
DI void touch(const void* g, unsigned lds) {
  unsigned keep;
  asm volatile("s_mov_b32 %0, m0\n\ts_mov_b32 m0, %2\n\ts_nop 0\n\tglobal_load_lds_dword %1, off\n\ts_mov_b32 m0, %0"
               : "=&s"(keep) : "v"(g), "s"(lds) : "memory");
}
DI u64 next_tag(u64 e) { return e * 0x9E3779B97F4A7C15ull + 0x632BE59BD9B4E019ull; }
DI void st_flag(char* p, u64 v) { __hip_atomic_store((u64*)p, v, __ATOMIC_RELAXED, __HIP_MEMORY_SCOPE_AGENT); }
DI void st16(rsrc_t rs, unsigned off, u32x4 v) { __builtin_amdgcn_raw_buffer_store_b128(v, rs, off, 0, 16); }
DI void st8(rsrc_t rs, unsigned off, u32x2 v) { __builtin_amdgcn_raw_buffer_store_b64(v, rs, off, 0, 16); }
DI void st4(rsrc_t rs, unsigned off, float v) { __builtin_amdgcn_raw_buffer_store_b32(__float_as_uint(v), rs, off, 0, 16); }
DI u32x4 ld16(rsrc_t rs, unsigned off) { return __builtin_amdgcn_raw_buffer_load_b128(rs, off, 0, 16); }
DI bool ready(u32x4 v) { return v[0] != 0xffffffffu && v[1] != 0xffffffffu && v[2] != 0xffffffffu && v[3] != 0xffffffffu; }
DI void drain() { asm volatile("s_waitcnt vmcnt(0)" ::: "memory"); }
constexpr int SPIN_MAX = 1 << 18;
DI bool flags_ok(u32x4 a, u32x4 b, u64 tag) {
  const unsigned tl = (unsigned)tag, th = (unsigned)(tag >> 32);
  return a[0] == tl && a[1] == th && a[2] == tl && a[3] == th && b[0] == tl && b[1] == th && b[2] == tl && b[3] == th;
}
DI void poll256(const char* flags, u64 tag, int lane) {
  const char* q = flags + lane * 32;
#pragma unroll 1
  for (int spin = 0; spin < SPIN_MAX; ++spin) {
    u32x4 a, b;
    asm volatile("global_load_dwordx4 %0, %2, off sc1\n\tglobal_load_dwordx4 %1, %2, off offset:16 sc1\n\ts_waitcnt vmcnt(0)"
                 : "=&v"(a), "=&v"(b) : "v"(q) : "memory");
    if (__all(flags_ok(a, b, tag))) break;
    __builtin_amdgcn_s_sleep(8);
  }
}
DI void pollq(const char* q, u64 tag) {
#pragma unroll 1
  for (int spin = 0; spin < SPIN_MAX; ++spin) {
    u32x2 a;
    asm volatile("global_load_dwordx2 %0, %1, off sc1\n\ts_waitcnt vmcnt(0)" : "=&v"(a) : "v"(q) : "memory");
    if (__all(a[0] == (unsigned)tag && a[1] == (unsigned)(tag >> 32))) break;
    __builtin_amdgcn_s_sleep(3);
  }
}
DI void polln(const char* flags, int n, u64 tag, int lane) { pollq(flags + (lane < n ? lane : 0) * 8, tag); }
DI bool arrive(char* smem, int idx, int n, int lane) {
  unsigned old = 0;
  if (lane == 0) old = __hip_atomic_fetch_add((LDS3 unsigned*)(smem + L_CNT + idx * 4), 1u, __ATOMIC_RELAXED, __HIP_MEMORY_SCOPE_WORKGROUP);
  return __builtin_amdgcn_readfirstlane(old) == (unsigned)(n - 1);
}
DI void set_cnt(char* smem, int idx, int lane) {
  if (lane == 0) __hip_atomic_store((LDS3 unsigned*)(smem + L_CNT + idx * 4), 1u, __ATOMIC_RELAXED, __HIP_MEMORY_SCOPE_WORKGROUP);
}
DI void wait_cnt(char* smem, int idx, unsigned n) {
#pragma unroll 1
  for (int spin = 0; spin < (SPIN_MAX << 4); ++spin) {
    unsigned v = __hip_atomic_load((LDS3 unsigned*)(smem + L_CNT + idx * 4), __ATOMIC_RELAXED, __HIP_MEMORY_SCOPE_WORKGROUP);
    if (__builtin_amdgcn_readfirstlane(v) >= n) break;
    __builtin_amdgcn_s_sleep(1);
  }
  asm volatile("" ::: "memory");
}

template <int NT> struct Frags { bf16x8 x[2]; bf16x8 w[NT]; };

#define STAMP_ARG
#define STAMP_PASS
template <int N> DI void waitv() { asm volatile("s_waitcnt vmcnt(%0)" :: "i"(N) : "memory"); }
DI void waitl() { __builtin_amdgcn_s_waitcnt(0xC07F); }

template <int TYPE, int C>
struct Gemm {
  static constexpr int NT = TYPE == 0 ? 5 : (C == 0 ? 9 : 10);
  static constexpr int NU = TYPE == 0 ? 11 : 19;
  static constexpr int WROW = NU * 32, NC = NU * 2, HB = 32 * WROW;
  static constexpr int NWH = TYPE == 0 ? 6 : 5;
  static constexpr int NWS = (NU + 7) / 8;
  static constexpr int NFULL = NU - (NWS - 1) * 8;
  static constexpr int MAGIC = (65536 + NC - 1) / NC;
  static constexpr bool VW = (TYPE == 0 && C == 1);
  static constexpr int unit_of(int t) { return TYPE == 0 ? t + 5 * C : t + 9 * C; }

  const char* xsrc[2];
  const char* wsrc[NWS];
  unsigned winc[NWS];
  unsigned xa0, wa, lds0;
  int cx, hw, hslot, w;
  bool full;
  char* smem;
  f32x4 acc[NT][2];

  DI void init(const P1& p, char* smem_, int rb, int g8, int t0w, int rot, int w_, int lane) {
    smem = smem_; w = w_; cx = rot; hw = 2 * rot; hslot = 0; lds0 = lds_addr(smem_); full = w < NFULL;
    const int r = w & 3, lg = lane >> 4, l15 = lane & 15;
#pragma unroll
    for (int i = 0; i < 2; ++i) {
      int row = (i * 8 + w) * 8 + (lane >> 3);
      int ch = (lane & 7) ^ ((row >> 1) & 7);
      xsrc[i] = (const char*)p.x + (unsigned)((rb * 128 + row) * 1280 + ch * 16 + rot * 128);
    }
#pragma unroll
    for (int i = 0; i < NWS; ++i) {
      int ci = (i * 8 + w) * 64 + lane;
      ci = ci < 32 * NC ? ci : lane;
      int pos = (ci * MAGIC) >> 16, c16 = ci - pos * NC;
      int kk = pos & 15;
      int k = (kk >= 4 && kk < 12) ? (pos ^ 12) : pos;
      bool isq = TYPE == 0 || c16 < 10;
      int colb;
      if (TYPE == 0) { int c = min(c16, 19); colb = 1280 + 160 * g8 + c * 16 + (c >= 10 ? 1120 : 0); }
      else colb = isq ? (160 * g8 + c16 * 16) : (32 * t0w + (c16 - 10) * 16);
      unsigned stride = isq ? 3840u : 3456u;
      wsrc[i] = (isq ? (const char*)p.wqkv : (const char*)p.w1) + (unsigned)((k + rot * 64) * stride + colb);
      winc[i] = stride * 32;
    }
    xa0 = (32 * r + l15) * 128 + ((lg ^ ((l15 >> 1) & 7)) * 16);
    wa = WB0 + (16 * (lg >> 1) + 4 * (lg & 1) + (l15 >> 2)) * WROW + (VW ? 160 + (l15 & 3) * 40 : (l15 & 3) * 8);
#pragma unroll
    for (int t = 0; t < NT; ++t) { acc[t][0] = f32x4{0, 0, 0, 0}; acc[t][1] = f32x4{0, 0, 0, 0}; }
  }
  DI void issue_x1(int s, int xslot) {
    glds1(xsrc[s], lds0 + xslot * XB + w * 1024 + s * 0x2000);
    xsrc[s] += 128;
    if (s == 1 && ++cx == 10) { cx = 0; xsrc[0] -= 1280; xsrc[1] -= 1280; }
  }
  DI void issue_w1(int s) {
    if (s < NWS - 1 || full) { glds1(wsrc[s], lds0 + WB0 + hslot * HB + w * 1024 + s * 0x2000); wsrc[s] += winc[s]; }
    if (s == NWS - 1) {
      hslot = hslot == NWH - 1 ? 0 : hslot + 1;
      if (++hw == 20) {
        hw = 0;
#pragma unroll
        for (int i = 0; i < NWS; ++i) wsrc[i] -= 20 * winc[i];
      }
    }
  }
  DI void issue_x(int xslot) { issue_x1(0, xslot); issue_x1(1, xslot); }
  DI void issue_w() {
#pragma unroll
    for (int s = 0; s < NWS; ++s) issue_w1(s);
  }
  template <int NX, int NH, int E> DI void wait() {
    if (full) waitv<2 * NX + NH * NWS + E>(); else waitv<2 * NX + NH * (NWS - 1) + E>();
  }
  DI void rd_x(Frags<NT>& f, int xslot, int ks, int tt) { f.x[tt] = *(const bf16x8*)(smem + xslot * XB + (xa0 ^ (ks * 64)) + tt * 2048); }
  DI void rd_w(Frags<NT>& f, int hs, int t) {
    const char* q = smem + hs * HB + wa + (VW ? t * 8 : unit_of(t) * 32);
    f.w[t] = tr2(q, q + 8 * WROW);
  }
  DI void kstep(const Frags<NT>& f, Frags<NT>& g, int nxs, int nks, int nhs, bool do_rd, int nreq, int ixs) {
    constexpr int NS = 2 + 2 * NWS;
#pragma unroll
    for (int t = 0; t < NT; ++t) {
      if (do_rd) {
        if (t < 2) rd_x(g, nxs, nks, t);
        rd_w(g, nhs, t);
      }
#pragma unroll
      for (int tt = 0; tt < 2; ++tt)
        acc[t][tt] = VW ? MFMA16(f.x[tt], f.w[t], acc[t][tt]) : MFMA16(f.w[t], f.x[tt], acc[t][tt]);
#pragma unroll
      for (int s = (t * NS) / NT; s < ((t + 1) * NS) / NT; ++s) {
        if (s < NWS) { if (nreq > 0) issue_w1(s); }
        else if (s < NWS + 2) { if (nreq > 1) issue_x1(s - NWS, ixs); }
        else { if (nreq > 1) issue_w1(s - NWS - 2); }
      }
      __builtin_amdgcn_sched_barrier(0);
    }
  }
};

DI void dots2(float& c0, float& c1, const bf16x8& wa_, const bf16x8& wb_) {
  u32x4 a = __builtin_bit_cast(u32x4, wa_), b = __builtin_bit_cast(u32x4, wb_);
  unsigned a0 = a[0], a1 = a[1], a2 = a[2], a3 = a[3], b0 = b[0], b1 = b[1], b2 = b[2], b3 = b[3];
  asm volatile("s_nop 1\n\t"
               "v_dot2c_f32_bf16 %0, 0x3f803f80, %2\n\tv_dot2c_f32_bf16 %1, 0x3f803f80, %6\n\t"
               "v_dot2c_f32_bf16 %0, 0x3f803f80, %3\n\tv_dot2c_f32_bf16 %1, 0x3f803f80, %7\n\t"
               "v_dot2c_f32_bf16 %0, 0x3f803f80, %4\n\tv_dot2c_f32_bf16 %1, 0x3f803f80, %8\n\t"
               "v_dot2c_f32_bf16 %0, 0x3f803f80, %5\n\tv_dot2c_f32_bf16 %1, 0x3f803f80, %9\n\ts_nop 3"
               : "+v"(c0), "+v"(c1) : "v"(a0), "v"(a1), "v"(a2), "v"(a3), "v"(b0), "v"(b1), "v"(b2), "v"(b3));
}
DI void dots_x(float& s1, float& s2, const bf16x8& x) {
  u32x4 a = __builtin_bit_cast(u32x4, x);
  unsigned a0 = a[0], a1 = a[1], a2 = a[2], a3 = a[3];
  asm volatile("s_nop 1\n\t"
               "v_dot2c_f32_bf16 %0, 0x3f803f80, %2\n\tv_dot2c_f32_bf16 %1, %2, %2\n\t"
               "v_dot2c_f32_bf16 %0, 0x3f803f80, %3\n\tv_dot2c_f32_bf16 %1, %3, %3\n\t"
               "v_dot2c_f32_bf16 %0, 0x3f803f80, %4\n\tv_dot2c_f32_bf16 %1, %4, %4\n\t"
               "v_dot2c_f32_bf16 %0, 0x3f803f80, %5\n\tv_dot2c_f32_bf16 %1, %5, %5\n\ts_nop 3"
               : "+v"(s1), "+v"(s2) : "v"(a0), "v"(a1), "v"(a2), "v"(a3));
}
DI f32x2 stat_of(const char* smem, int tok) { return *(const f32x2*)(smem + L_SUM + tok * 8); }
DI void mish_pair(rsrc_t rs, const char* smem, const f32x4 (&a0)[2], const f32x4 (&a1)[2], int ucs, int wl, int wt, bool skip,
                  const f32x2 (&st)[2], int tt0, int lane) {
  const int lg = lane >> 4;
  f32x4 bb[2], c4[2];
#pragma unroll
  for (int h = 0; h < 2; ++h) {
    bb[h] = *(const f32x4*)(smem + L_BIASL + ((wl + h) * 16 + 4 * lg) * 4);
    c4[h] = *(const f32x4*)(smem + L_CS + ((ucs + h) * 16 + 4 * lg) * 4);
  }
#pragma unroll
  for (int tt = 0; tt < 2; ++tt) {
    const f32x2 rsL = splat2(st[tt][0] * LOG2E), nbL = splat2(st[tt][1] * LOG2E);
    u32x4 o;
    o[0] = pack2v(mish2(fma2(lo2(a0[tt]), rsL, fma2(nbL, lo2(c4[0]), lo2(bb[0])))));
    o[1] = pack2v(mish2(fma2(hi2(a0[tt]), rsL, fma2(nbL, hi2(c4[0]), hi2(bb[0])))));
    o[2] = pack2v(mish2(fma2(lo2(a1[tt]), rsL, fma2(nbL, lo2(c4[1]), lo2(bb[1])))));
    o[3] = pack2v(mish2(fma2(hi2(a1[tt]), rsL, fma2(nbL, hi2(c4[1]), hi2(bb[1])))));
    st16(rs, skip ? OOB : OFF_ACT + (tt0 + tt) * AST + (wt >> 1) * 1024 + lane * 16, o);
  }
}
DI void load_aphi(const char* smem, int lane, bf16x8 (&ap)[5][3]) {
  const int lg = lane >> 4, l15 = lane & 15;
  const char* pa[6];
#pragma unroll
  for (int i = 0; i < 6; ++i) {
    int d0 = 16 * i + 4 * lg;
    int h = (d0 * 13) >> 8;
    bool ok = (h == (l15 & 3)) && i < 5;
    pa[i] = smem + (ok ? L_RAW + h * 800 + (d0 - 20 * h + (l15 >> 2)) * 40 : L_ZERO);
  }
#pragma unroll
  for (int t = 0; t < 5; ++t)
#pragma unroll
    for (int ks = 0; ks < 3; ++ks) ap[t][ks] = tr2(pa[2 * ks] + t * 8, pa[2 * ks + 1] + t * 8);
}
DI void pack_k(const char* smem, const f32x4& a, int t, f32x2 st, int lg, u32x2& kb) {
  f32x4 c4 = *(const f32x4*)(smem + L_CS + (t * 16 + 4 * lg) * 4);
  const f32x2 rsv = splat2(st[0]), nb = splat2(st[1]);
  kb = u32x2{pack2v(fma2(lo2(a), rsv, nb * lo2(c4))), pack2v(fma2(hi2(a), rsv, nb * hi2(c4)))};
}
DI void features(const u32x2 (&kb)[5], const bf16x8 (&ap)[5][3], u32x2 (&pos)[5], u32x2 (&neg)[5]) {
  bf16x8 b0 = mk8(kb[0], kb[1]), b1 = mk8(kb[2], kb[3]), b2 = mk8(kb[4], u32x2{0u, 0u});
  f32x4 pk[5];
#pragma unroll
  for (int t = 0; t < 5; ++t) pk[t] = MFMA16(ap[t][2], b2, MFMA16(ap[t][1], b1, MFMA16(ap[t][0], b0, (f32x4{0, 0, 0, 0}))));
  dual_softmax(pk, pos, neg);
}
DI void barrier() {
  waitl();
  __builtin_amdgcn_s_barrier();
  __builtin_amdgcn_sched_barrier(0);
}

template <int TYPE, int C, class G>
DI u64 first_pass(G& g, const P1& p, rsrc_t rs, char* smem, int rb, int g8, int t0w, int w, int lane STAMP_ARG) {
  constexpr int NT = G::NT, NWH = G::NWH;
  const int r = w & 3, lg = lane >> 4, l15 = lane & 15;
  const unsigned lds0 = lds_addr(smem);
  u32x2 ev;
  asm volatile("global_load_dwordx2 %0, %1, off sc1" : "=v"(ev) : "v"(p.ws + OFF_FLG) : "memory");
  const int job = TYPE * 8 + g8;
  const int myc = (rb + job) % 10;
  const int rot = (myc / ROTP) * ROTP;
  const int my_i = myc - rot;
  g.init(p, smem, rb, g8, t0w, rot, w, lane);
  if (C == 1) {
    int ci = r * 64 + lane;
    if (ci < 200) glds1((const char*)p.wphi + g8 * 3200 + ci * 16, lds0 + L_RAW + r * 1024);
  } else if (w == 3) {
    if (TYPE == 1 && lane < 28) glds1((const char*)p.b1 + t0w * 32 + lane * 16, lds0 + L_BIAS);
    if (lane < 32) *(unsigned*)(smem + L_ZERO + lane * 4) = 0u;
  }
  g.issue_w(); g.issue_x(0); g.issue_w();
  __builtin_amdgcn_s_barrier();
  g.issue_w(); g.issue_x(1); g.issue_w();
  g.issue_w(); g.issue_x(2);
  if (NWH == 6) g.issue_w();
  constexpr int AH = NWH - 4;
  const int ptj = w * 128 + rb * 8 + g8;
  const bool has_pt = TYPE == 0 && w < 6 && ptj < 740;
  const int ptu = ptj / 10, ptL = ptj - ptu * 10;
  if (has_pt) {
#pragma unroll
    for (int i = 0; i < 4; ++i) {
      int rho = i * 8 + (lane >> 3);
      const u16* src;
      if (ptu < 54) src = p.w2 + (unsigned)((32 * ptu + rho) * 640);
      else {
        int T = 2 * (ptu - 54) + (rho >> 4), Gq = (T * 13) >> 6;
        src = p.wo + (unsigned)(((4 * Gq + ((rho >> 2) & 3)) * 20 + 4 * (T - 5 * Gq) + (rho & 3)) * 640);
      }
      glds1((const char*)src + ptL * 128 + (lane & 7) * 16, lds0 + L_PT + w * 4096 + i * 1024);
    }
  }
  float cs[3] = {0.f, 0.f, 0.f}, sx[2] = {0.f, 0.f}, cd = 0.f;
  auto do_dots = [&](const Frags<NT>& f) {
    dots_x(sx[0], sx[1], f.x[C]);
    if (r == 0) { dots2(cs[0], cs[1], f.w[0], f.w[4]); if (NT > 8) dots2(cs[2], cd, f.w[NT > 8 ? 8 : 0], f.w[0]); }
    else if (r == 1) { dots2(cs[0], cs[1], f.w[1], f.w[NT > 5 ? 5 : 0]); if (NT > 9) dots2(cs[2], cd, f.w[NT > 9 ? 9 : 0], f.w[0]); }
    else if (r == 2) dots2(cs[0], cs[1], f.w[2], f.w[NT > 6 ? 6 : 0]);
    else dots2(cs[0], cs[1], f.w[3], f.w[NT > 7 ? 7 : 0]);
  };
  Frags<NT> f0, f1;
  if (has_pt) g.template wait<2, NWH - 2, 4>(); else g.template wait<2, NWH - 2, 0>();
  barrier();
  STAMP(1);
  g.rd_x(f0, 0, 0, 0); g.rd_x(f0, 0, 0, 1);
#pragma unroll
  for (int t = 0; t < NT; ++t) g.rd_w(f0, 0, t);
  if (TYPE == 1) {
    int tid = w * 64 + lane;
    if (tid < 224) *(float*)(smem + L_BIASL + tid * 4) = bf_lo(*(const u16*)(smem + L_BIAS + tid * 2)) * LOG2E;
  }
  asm volatile("" : "+v"(ev));
  const u64 tag = next_tag(((u64)(unsigned)__builtin_amdgcn_readfirstlane(ev[1]) << 32) | (unsigned)__builtin_amdgcn_readfirstlane(ev[0]));
  int xs = 0, hs = 0;
  u32x4 fa, fb;
  u32x2 tf;
  unsigned pv[10], pw[10];
  const char* tfp = p.ws + (lane < 10 ? F_ST + (rb * 16 + lane) * 8 : F_CS + (job * 16 + (lane < 20 ? lane - 10 : 0)) * 8);
  auto inc = [](int s) { return s == NWH - 1 ? 0 : s + 1; };
#define LD10(pv, q) \
    asm volatile("global_load_dword %0, %10, off sc1\n\tglobal_load_dword %1, %10, off offset:1280 sc1\n\t" \
                 "global_load_dword %2, %10, off offset:2560 sc1\n\tglobal_load_dword %3, %10, off offset:3840 sc1\n\t" \
                 "global_load_dword %4, %11, off sc1\n\tglobal_load_dword %5, %11, off offset:1280 sc1\n\t" \
                 "global_load_dword %6, %11, off offset:2560 sc1\n\tglobal_load_dword %7, %11, off offset:3840 sc1\n\t" \
                 "global_load_dword %8, %12, off sc1\n\tglobal_load_dword %9, %12, off offset:1280 sc1" \
                 : "=&v"(pv[0]), "=&v"(pv[1]), "=&v"(pv[2]), "=&v"(pv[3]), "=&v"(pv[4]), "=&v"(pv[5]), "=&v"(pv[6]), "=&v"(pv[7]), "=&v"(pv[8]), "=&v"(pv[9]) \
                 : "v"(q), "v"((q) + 5120), "v"((q) + 10240) : "memory")
#pragma unroll 1
  for (int i = 0; i < 8; ++i) {
    waitl();
    __builtin_amdgcn_sched_barrier(0);
    const int h1 = inc(hs), h2 = inc(h1);
    g.kstep(f0, f1, xs, 1, h1, true, 0, 0);
    if (i == my_i) do_dots(f0);
    const int nxs = xs == 2 ? 0 : xs + 1;
    if (TYPE == 0) {
      if (i == 0 && has_pt) g.template wait<1, AH, 4>();
      else if (i == 1) g.template wait<1, AH, 2>();
      else if (i == 2 && has_pt) g.template wait<1, AH, 6>();
      else if (i == 2) g.template wait<1, AH, 2>();
      else if (i == 3 && has_pt) g.template wait<1, AH, 4>();
      else g.template wait<1, AH, 0>();
    } else {
      if (i == 1) g.template wait<1, AH, 3>();
      else g.template wait<1, AH, 0>();
    }
    barrier();
    if (i == FLAG_I && w == 0 && lane < 3)
      st_flag(p.ws + (lane == 0 ? F_EARLY + blockIdx.x * 8 : (lane == 1 ? F_ST + (rb * 16 + job) * 8 : F_CS + (job * 16 + rb) * 8)), tag);
    g.kstep(f1, f0, nxs, 0, h2, true, i < 7 ? 3 : (NWH == 5 ? 1 : 0), xs);
    if (i == 0) {
      const u32x4 sent = {~0u, ~0u, ~0u, ~0u};
      if (TYPE == 0) {
        unsigned kb = OFF_KVP + (g8 * 16 + rb) * 15360 + lane * 16;
        st16(rs, kb + w * 1024, sent);
        st16(rs, w < 7 ? kb + (8 + w) * 1024 : (lane < 30 ? OFF_KVH + g8 * 7680 + rb * 480 + lane * 16 : OOB), sent);
      } else {
        const unsigned ob = OFF_O + (rb * 8 + w) * OST + ((5 * g8) >> 1) * 1024 + lane * 16;
        st16(rs, ob + 1024, sent);
        st16(rs, ob + ((g8 & 1) ? 2048 : 0), sent);
        st8(rs, ob + ((g8 & 1) ? 8 : 2048), u32x2{~0u, ~0u});
      }
    }
    if (i == 1 && has_pt) {
      const char* q = smem + L_PT + w * 4096 + (4 * lg + (l15 >> 2)) * 128 + (l15 & 3) * 8;
#pragma unroll
      for (int n = 0; n < 4; ++n) {
        bf16x8 f = tr2(q + n * 32, q + 2048 + n * 32);
        int N = 4 * ptL + n, ct = (N * 13) >> 6;
        st16(rs, OFF_W2F + ((ct * 74 + ptu) * 5 + (N - 5 * ct)) * 1024 + lane * 16, __builtin_bit_cast(u32x4, f));
      }
    }
    if (i == my_i) {
      do_dots(f1);
      float v = rscatter_add(sx[0], sx[1], cs[0], cs[1]);
      unsigned a = lg < 2 ? OFF_STP + (rb * 10 + myc) * 1280 + lg * 512 + (32 * r + 16 * C + l15) * 4
                          : OFF_CSP + (job * 10 + myc) * 1280 + ((G::unit_of(0) + r + (lg == 2 ? 0 : 4)) * 16 + l15) * 4;
      bool on = lg < 2 ? job < 10 : (rb < 10 && (lg == 2 || r + 4 < NT));
      st4(rs, on ? a : OOB, v);
      if (NT > 8) {
        float v2 = rscatter_add(cs[2], 0.f, 0.f, 0.f);
        st4(rs, (lg == 0 && rb < 10 && r + 8 < NT) ? OFF_CSP + (job * 10 + myc) * 1280 + ((G::unit_of(0) + r + 8) * 16 + l15) * 4 : OOB, v2);
      }
    }
    if (i == 6) {
      asm volatile("global_load_dwordx2 %0, %1, off sc1" : "=v"(tf) : "v"(tfp) : "memory");
      if (w == 5)
        asm volatile("global_load_dwordx4 %0, %2, off sc1\n\tglobal_load_dwordx4 %1, %2, off offset:16 sc1"
                     : "=&v"(fa), "=&v"(fb) : "v"(p.ws + F_EARLY + lane * 32) : "memory");
    }
    xs = nxs; hs = h2;
  }
  {
    waitl();
    __builtin_amdgcn_sched_barrier(0);
    const int h1 = inc(hs), h2 = inc(h1);
    g.kstep(f0, f1, xs, 1, h1, true, 0, 0);
    const int nxs = xs == 2 ? 0 : xs + 1;
    waitv<0>();
    barrier();
    asm volatile("" : "+v"(tf));
    if (!__all(tf[0] == (unsigned)tag && tf[1] == (unsigned)(tag >> 32))) pollq(tfp, tag);
    if (C == 1 && w == 5) {
      asm volatile("" : "+v"(fa), "+v"(fb));
      if (__all(flags_ok(fa, fb, tag))) {
        if (blockIdx.x == 0 && lane == 0) st_flag(p.ws + OFF_FLG, tag);
        set_cnt(smem, 3, lane);
        constexpr int NL = DMAP == 0 ? 370 : 740;
        const char* q2 = p.ws + OFF_W2F + (DMAP == 0 ? (blockIdx.x & 1) * 4 * 378880 : 0) + ((blockIdx.x >> 3) * NL + lane) * 128;
#pragma unroll
        for (int j = 0; j < (NL + 63) / 64; ++j) touch(q2 + (j * 64 + lane < NL ? j * 8192 : 0), lds0 + L_DUMMY);
      }
    }
    const int tid = w * 64 + lane;
    const char* q = p.ws + (tid < 128 ? OFF_STP + rb * 12800 + tid * 4 : OFF_CSP + job * 12800 + min(tid - 128, 303) * 4);
    LD10(pv, q);
    if constexpr (C == 0) { LD10(pw, q + 512); }
    g.kstep(f1, f0, nxs, 0, h2, true, 0, 0);
    xs = nxs; hs = h2;
  }
  waitl();
  __builtin_amdgcn_sched_barrier(0);
  g.kstep(f0, f1, xs, 1, inc(hs), true, 0, 0);
  waitl();
  __builtin_amdgcn_sched_barrier(0);
  g.kstep(f1, f0, 0, 0, 0, false, 0, 0);
  STAMP(2);
  {
    waitv<0>();
    asm volatile("" : "+v"(pv[0]), "+v"(pv[1]), "+v"(pv[2]), "+v"(pv[3]), "+v"(pv[4]), "+v"(pv[5]), "+v"(pv[6]), "+v"(pv[7]), "+v"(pv[8]), "+v"(pv[9]) :: "memory");
    float v = 0.f;
#pragma unroll
    for (int i = 0; i < 10; ++i) v += __uint_as_float(pv[i]);
    const int tid = w * 64 + lane;
    if constexpr (C == 0) {
      asm volatile("" : "+v"(pw[0]), "+v"(pw[1]), "+v"(pw[2]), "+v"(pw[3]), "+v"(pw[4]), "+v"(pw[5]), "+v"(pw[6]), "+v"(pw[7]), "+v"(pw[8]), "+v"(pw[9]) :: "memory");
      if (w < 2) {
        float v2 = 0.f;
#pragma unroll
        for (int i = 0; i < 10; ++i) v2 += __uint_as_float(pw[i]);
        float mean = v * (1.0f / 640.0f);
        float var = fmaxf(fmaf(-mean, mean, v2 * (1.0f / 640.0f)), 0.f);
        float rstd = __builtin_amdgcn_rsqf(var + 1e-5f);
        *(f32x2*)(smem + L_SUM + tid * 8) = f32x2{rstd, -rstd * mean};
      }
    }
    if (tid >= 128 && tid < 432) *(float*)(smem + L_CS + (tid - 128) * 4) = v;
  }
  barrier();
  STAMP(3);
  return tag;
}

constexpr int RING0 = 8192, RINGB = 35840;
DI int slot_of(int w) { return w >= 4 ? RING0 + (w - 4) * RINGB : RING0 + w * RINGB + 18432; }
DI int d_rt() { const int xcd = blockIdx.x & 7, idx = blockIdx.x >> 3; return DMAP == 0 ? (xcd >> 1) * 8 + (idx >> 2) : xcd * 4 + (idx >> 3); }
DI int d_ct() { const int xcd = blockIdx.x & 7, idx = blockIdx.x >> 3; return DMAP == 0 ? (xcd & 1) * 4 + (idx & 3) : (idx & 7); }
DI int d_u0(int w) { return (w - 4) * 14 - (w == 7 ? 1 : 0); }
DI void ring_fill(const P1& p, char* smem, int w, int lane, int s0, int s1) {
  const char* pb = p.ws + OFF_W2F + (d_ct() * 74 + d_u0(w)) * 5120 + lane * 16;
  const unsigned ringa = lds_addr(smem) + slot_of(w);
#pragma unroll
  for (int s = s0; s < s1; ++s) glds5_sc1(pb + s * 5120, ringa + s * 5120);
}
DI bool early_ok(char* smem) {
  unsigned v = __hip_atomic_load((LDS3 unsigned*)(smem + L_CNT + 12), __ATOMIC_RELAXED, __HIP_MEMORY_SCOPE_WORKGROUP);
  return __builtin_amdgcn_readfirstlane(v) != 0;
}

DI void need_early(const P1& p, char* smem, u64 tag, int lane) {
  if (early_ok(smem)) return;
  poll256(p.ws + F_EARLY, tag, lane);
  if (blockIdx.x == 0 && lane == 0) st_flag(p.ws + OFF_FLG, tag);
  set_cnt(smem, 3, lane);
}
DI void poll_word(const char* q) {
#pragma unroll 1
  for (int spin = 0; spin < (SPIN_MAX >> 3); ++spin) {
    unsigned a;
    asm volatile("global_load_dword %0, %1, off sc1\n\ts_waitcnt vmcnt(0)" : "=&v"(a) : "v"(q) : "memory");
    if (__all(a != ~0u)) break;
    __builtin_amdgcn_s_sleep(2);
  }
}

template <int C>
DI u64 body_a(const P1& p, rsrc_t rs, char* smem, int rb, int g8, int w, int lane, bool& filled STAMP_ARG) {
  const int r = w & 3, lg = lane >> 4, l15 = lane & 15;
  Gemm<0, C> g;
  const u64 tag = first_pass<0, C>(g, p, rs, smem, rb, g8, 0, w, lane STAMP_PASS);
  u32x2 kb[5];
  bf16x8 ap[5][3];
  load_aphi(smem, lane, ap);
  if constexpr (C == 0) {
    const f32x2 s0 = stat_of(smem, 32 * r + l15), s1 = stat_of(smem, 32 * r + 16 + l15);
#pragma unroll
    for (int t = 0; t < 5; ++t) {
      u32x2 k1;
      pack_k(smem, g.acc[t][1], t, s1, lg, k1);
      *(u32x2*)(smem + E_KB + (r * 5 + t) * 512 + lane * 8) = k1;
      pack_k(smem, g.acc[t][0], t, s0, lg, kb[t]);
    }
  } else {
    f32x4 sa[2], sb[2];
#pragma unroll
    for (int tt = 0; tt < 2; ++tt) {
      sa[tt] = *(const f32x4*)(smem + L_SUM + (32 * r + 16 * tt + 4 * lg) * 8);
      sb[tt] = *(const f32x4*)(smem + L_SUM + (32 * r + 16 * tt + 4 * lg + 2) * 8);
    }
#pragma unroll
    for (int t = 0; t < 5; ++t) {
      float c1 = *(const float*)(smem + L_CS + ((5 + t) * 16 + l15) * 4);
      unsigned o[4];
#pragma unroll
      for (int tt = 0; tt < 2; ++tt) {
        float y0 = fmaf(g.acc[t][tt][0], sa[tt][0], sa[tt][1] * c1);
        float y1 = fmaf(g.acc[t][tt][1], sa[tt][2], sa[tt][3] * c1);
        float y2 = fmaf(g.acc[t][tt][2], sb[tt][0], sb[tt][1] * c1);
        float y3 = fmaf(g.acc[t][tt][3], sb[tt][2], sb[tt][3] * c1);
        o[2 * tt] = pack2(y0, y1); o[2 * tt + 1] = pack2(y2, y3);
      }
      *(u32x4*)(smem + E_V + (r * 5 + t) * 1024 + lane * 16) = u32x4{o[0], o[1], o[2], o[3]};
    }
  }
  barrier();
  if constexpr (C == 1) {
#pragma unroll
    for (int t = 0; t < 5; ++t) kb[t] = *(const u32x2*)(smem + E_KB + (r * 5 + t) * 512 + lane * 8);
  }
  {
    const int tok = 32 * r + 16 * C + l15;
    u32x2 pos[5], neg[5];
    features(kb, ap, pos, neg);
#pragma unroll
    for (int t = 0; t < 5; ++t) {
      *(u32x2*)(smem + E_PHI + (t * 128 + tok) * 32 + lg * 8) = pos[t];
      *(u32x2*)(smem + E_PHI + ((5 + t) * 128 + tok) * 32 + lg * 8) = neg[t];
    }
  }
  barrier();
  STAMP(4);
  {
    const unsigned pa = E_PHI + (4 * lg + (l15 >> 2)) * 32 + (l15 & 3) * 8;
    const u32x4 one4 = {0x3f803f80u, 0x3f803f80u, 0x3f803f80u, 0x3f803f80u};
    const bool valid = (l15 >> 2) == lg;
    f32x4 a[3][3];
    const char* qa[3]; const char* vb[3]; bool hf[3];
#pragma unroll
    for (int k3 = 0; k3 < 3; ++k3) {
      const int un0 = w + 8 * k3, un = un0 < 20 ? un0 : 19;
      hf[k3] = un & 1;
      qa[k3] = smem + pa + (un >> 1) * 4096;
      vb[k3] = smem + E_V + (un & 1) * 3072 + lane * 16;
#pragma unroll
      for (int j = 0; j < 3; ++j) a[k3][j] = f32x4{0, 0, 0, 0};
    }
#pragma unroll
    for (int ks = 0; ks < 4; ++ks)
#pragma unroll
      for (int k3 = 0; k3 < 3; ++k3) {
        bf16x8 af = tr2(qa[k3] + ks * 1024, qa[k3] + ks * 1024 + 512);
        const char* v = vb[k3] + ks * 5120;
        a[k3][0] = MFMA16(af, *(const bf16x8*)(v), a[k3][0]);
        a[k3][1] = MFMA16(af, *(const bf16x8*)(v + 1024), a[k3][1]);
        u32x4 b2 = *(const u32x4*)(v + (hf[k3] ? 0 : 2048));
        b2 = hf[k3] ? one4 : b2;
        a[k3][2] = MFMA16(af, __builtin_bit_cast(bf16x8, b2), a[k3][2]);
      }
#pragma unroll
    for (int k3 = 0; k3 < 3; ++k3) {
      const int un0 = w + 8 * k3;
      unsigned dst = (valid && un0 < 20) ? OFF_KVP + ((g8 * 16 + rb) * 60 + un0 * 3) * 256 + (lg * 4 + (l15 & 3)) * 16 : OOB;
#pragma unroll
      for (int j = 0; j < 3; ++j) st16(rs, dst + j * 256, __builtin_bit_cast(u32x4, a[k3][j]));
    }
  }
  (void)arrive(smem, 1, 8, lane);
  STAMP(5);
  if (C == 0 && w == 1 && rb < 15) {
    need_early(p, smem, tag, lane);
    f32x4 a;
#pragma unroll 1
    for (int spin = 0; spin < 8; ++spin) {
      poll_word(p.ws + OFF_KVP + (g8 * 16 + l15) * 15360 + rb * 1024 + lg * 256 + 252);
      u32x4 kv[16];
#pragma unroll
      for (int i = 0; i < 16; ++i) kv[i] = ld16(rs, OFF_KVP + (g8 * 16 + i) * 15360 + rb * 1024 + lane * 16);
      bool ok = ready(kv[0]);
      a = __builtin_bit_cast(f32x4, kv[0]);
#pragma unroll
      for (int i = 1; i < 16; ++i) { ok = ok && ready(kv[i]); a += __builtin_bit_cast(f32x4, kv[i]); }
      if (__all(ok)) break;
    }
    int tau = rb * 4 + lg, m = (tau * 43) >> 8, tn = tau - 6 * m;
    int sg = m >= 5 ? 1 : 0, t = m - 5 * sg;
    int ks = t < 4 ? sg * 2 + (t >> 1) : 4, hf = t < 4 ? (t & 1) : sg;
    st8(rs, OFF_KVH + g8 * 7680 + (tn * 5 + ks) * 256 + l15 * 16 + hf * 8, u32x2{pack2(a[0], a[1]), pack2(a[2], a[3])});
    set_cnt(smem, 2, lane);
  } else if (rb < 15) wait_cnt(smem, 2, 1);
  wait_cnt(smem, 1, 8);
  STAMP(6);
  return tag;
}

template <int C>
DI u64 body_b(const P1& p, rsrc_t rs, char* smem, int rb, int g8, int w, int lane, bool& filled STAMP_ARG) {
  const int r = w & 3, lg = lane >> 4, l15 = lane & 15;
  const int t0w = min(14 * g8, 94);
  Gemm<1, C> g;
  const u64 tag = first_pass<1, C>(g, p, rs, smem, rb, g8, t0w, w, lane STAMP_PASS);
  f32x2 st[2] = {stat_of(smem, 32 * r + l15), stat_of(smem, 32 * r + 16 + l15)};
  if constexpr (C == 1) {
    filled = early_ok(smem);
    __builtin_amdgcn_s_setprio(2);
#pragma unroll
    for (int pp = 0; pp < 5; ++pp) {
      if (filled) ring_fill(p, smem, w, lane, pp < 2 ? 2 * pp : pp + 2, pp < 2 ? 2 * pp + 2 : pp + 3);
      __builtin_amdgcn_sched_barrier(0);
      mish_pair(rs, smem, g.acc[2 * pp], g.acc[2 * pp + 1], 9 + 2 * pp, 4 + 2 * pp, t0w + 4 + 2 * pp, false, st, rb * 8 + 2 * r, lane);
      __builtin_amdgcn_sched_barrier(0);
    }
    __builtin_amdgcn_s_setprio(0);
    STAMP(4);
    drain();
    if (arrive(smem, 0, 8, lane) && lane == 0) st_flag(p.ws + F_ACT + (rb * 16 + g8) * 8, tag);
    STAMP(5);
  } else {
    __builtin_amdgcn_s_setprio(2);
    mish_pair(rs, smem, g.acc[5], g.acc[6], 5, 0, t0w, g8 == 7, st, rb * 8 + 2 * r, lane);
    mish_pair(rs, smem, g.acc[7], g.acc[8], 7, 2, t0w + 2, g8 == 7, st, rb * 8 + 2 * r, lane);
    __builtin_amdgcn_s_setprio(0);
    STAMP(4);
    if (w == 0 && lane < 30) *(u32x4*)(smem + E_KV + lane * 272 + 256) = u32x4{0u, 0u, 0u, 0u};
    bf16x8 ap[5][3];
    load_aphi(smem, lane, ap);
    bf16x8 ph[2][5];
#pragma unroll
    for (int tt = 0; tt < 2; ++tt) {
      u32x2 kb[5], pos[5], neg[5];
#pragma unroll
      for (int t = 0; t < 5; ++t) pack_k(smem, g.acc[t][tt], t, st[tt], lg, kb[t]);
      if (tt == 0) {
        drain();
        if (arrive(smem, 0, 8, lane) && lane == 0) st_flag(p.ws + F_ACT + (rb * 16 + g8) * 8, tag);
      }
      features(kb, ap, pos, neg);
      ph[tt][0] = mk8(pos[0], pos[1]); ph[tt][1] = mk8(pos[2], pos[3]);
      ph[tt][2] = mk8(neg[0], neg[1]); ph[tt][3] = mk8(neg[2], neg[3]); ph[tt][4] = mk8(pos[4], neg[4]);
#pragma unroll
      for (int k = 0; k < 5; ++k) asm volatile("" : "+v"(ph[tt][k]));
    }
    STAMP(5);
    if (w == 0) {
      need_early(p, smem, tag, lane);
      poll_word(p.ws + OFF_KVH + g8 * 7680 + (lane < 60 ? (lane >> 1) * 256 + (lane & 1) * 8 : 0));
      set_cnt(smem, 4, lane);
    } else wait_cnt(smem, 4, 1);
    {
      const int e0 = w * 120 + lane, e1 = e0 + 64;
      u32x4 a, b;
#pragma unroll 1
      for (int spin = 0; spin < SPIN_MAX; ++spin) {
        a = ld16(rs, OFF_KVH + g8 * 7680 + e0 * 16);
        b = ld16(rs, lane < 56 ? OFF_KVH + g8 * 7680 + e1 * 16 : OOB);
        if (__all(ready(a) && ready(b))) break;
        __builtin_amdgcn_s_sleep(2);
      }
      *(u32x4*)(smem + E_KV + (e0 >> 4) * 272 + (e0 & 15) * 16) = a;
      if (lane < 56) *(u32x4*)(smem + E_KV + (e1 >> 4) * 272 + (e1 & 15) * 16) = b;
    }
    waitl();
    asm volatile("" ::: "memory");
    (void)arrive(smem, 1, 4, lane);
    wait_cnt(smem, 1, 4);
    STAMP(6);
    const char* ks_ = smem + E_KV + (((l15 >> 2) == lg) ? (lg * 4 + (l15 & 3)) * 16 : 256);
    f32x4 n[2][6];
#pragma unroll
    for (int tn = 0; tn < 6; ++tn) {
      n[0][tn] = f32x4{0, 0, 0, 0}; n[1][tn] = f32x4{0, 0, 0, 0};
#pragma unroll
      for (int ks = 0; ks < 5; ++ks) {
        bf16x8 ka = *(const bf16x8*)(ks_ + (tn * 5 + ks) * 272);
        n[0][tn] = MFMA16(ka, ph[0][ks], n[0][tn]);
        n[1][tn] = MFMA16(ka, ph[1][ks], n[1][tn]);
      }
    }
#pragma unroll
    for (int tt = 0; tt < 2; ++tt) {
      const f32x2 rc = splat2(__builtin_amdgcn_rcpf(n[tt][5][0] + 1e-6f));
      u32x2 o[5];
#pragma unroll
      for (int t = 0; t < 5; ++t) o[t] = u32x2{pack2v(lo2(n[tt][t]) * rc), pack2v(hi2(n[tt][t]) * rc)};
      const unsigned ob = OFF_O + (rb * 8 + 2 * r + tt) * OST + ((5 * g8) >> 1) * 1024 + lane * 16;
      if ((g8 & 1) == 0) {
        st16(rs, ob, u32x4{o[0][0], o[0][1], o[1][0], o[1][1]});
        st16(rs, ob + 1024, u32x4{o[2][0], o[2][1], o[3][0], o[3][1]});
        st8(rs, ob + 2048, o[4]);
      } else {
        st8(rs, ob + 8, o[0]);
        st16(rs, ob + 1024, u32x4{o[1][0], o[1][1], o[2][0], o[2][1]});
        st16(rs, ob + 2048, u32x4{o[3][0], o[3][1], o[4][0], o[4][1]});
      }
    }
  }
  return tag;
}

#define GLD(dst, ptr, off) asm volatile("global_load_dwordx4 %0, %1, off offset:" #off " sc1" : "=v"(dst) : "v"(ptr) : "memory")
#define WAITV(n) do { asm volatile("s_waitcnt vmcnt(%0)" :: "i"(n) : "memory"); __builtin_amdgcn_sched_barrier(0); } while (0)

#define ISSUE_A(A, s) do { GLD(A[s][0], pa, 0); GLD(A[s][1], pa1, 0); GLD(A[s][2], pa2, 0); GLD(A[s][3], pa3, 0); \
    pa += 1024; pa1 += 1024; pa2 += 1024; pa3 += 1024; } while (0)
#define ISSUE_B(B, s) do { GLD(B[s][0], pb, -2048); GLD(B[s][1], pb, -1024); GLD(B[s][2], pb, 0); GLD(B[s][3], pb, 1024); GLD(B[s][4], pb, 2048); \
    pb += 5120; } while (0)
DI void wait_flags(char* smem, int cnt, bool poller, const char* flags, int n, u64 tag, int lane) {
  if (poller) { polln(flags, n, tag, lane); set_cnt(smem, cnt, lane); }
  else { wait_cnt(smem, cnt, 1); drain(); }
}
DI void stream_o(const char* pa, const char* pb, const char* pw, char* smem, bool poller, int lane, int rot, f32x4 (&acc)[5][4] STAMP_ARG) {
  u32x4 A[5][4], B[4][5];
#pragma unroll
  for (int s = 0; s < 4; ++s) ISSUE_B(B, s);
  if (poller) { poll_word(pw); set_cnt(smem, 7, lane); }
  else wait_cnt(smem, 7, 1);
#pragma unroll 1
  for (int spin = 0; spin < (SPIN_MAX >> 2); ++spin) {
    const char* q0 = pa + 2048 + rot * OST; const char* q1 = pa + 2048 + ((rot + 1) & 3) * OST;
    const char* q2 = pa + 2048 + ((rot + 2) & 3) * OST; const char* q3 = pa + 2048 + ((rot + 3) & 3) * OST;
#define LD5(t, q) GLD(A[0][t], q, -2048); GLD(A[1][t], q, -1024); GLD(A[2][t], q, 0); GLD(A[3][t], q, 1024); GLD(A[4][t], q, 2048);
    LD5(0, q0) LD5(1, q1) LD5(2, q2) LD5(3, q3)
    WAITV(0);
    unsigned mx = 0;
#pragma unroll
    for (int s = 0; s < 5; ++s)
#pragma unroll
      for (int t = 0; t < 4; ++t) {
        asm volatile("" : "+v"(A[s][t]));
        mx = max(max(mx, A[s][t][0]), max(A[s][t][1], max(A[s][t][2], A[s][t][3])));
      }
    if (__all(mx != ~0u)) break;
    __builtin_amdgcn_s_sleep(4);
  }
  STAMP(8);
#define OSTEP(sa, sb) \
  _Pragma("unroll") for (int i = 0; i < 5; ++i) asm volatile("" : "+v"(B[sb][i])); \
  _Pragma("unroll") for (int n = 0; n < 5; ++n) _Pragma("unroll") for (int t = 0; t < 4; ++t) \
    acc[n][t] = MFMA16(__builtin_bit_cast(bf16x8, B[sb][n]), __builtin_bit_cast(bf16x8, A[sa][t]), acc[n][t]); \
  __builtin_amdgcn_sched_barrier(0);
  OSTEP(0, 0)
  ISSUE_B(B, 0);
  OSTEP(1, 1) OSTEP(2, 2) OSTEP(3, 3)
  WAITV(0);
  OSTEP(4, 0)
}

template <int NST, int NA, int NB, int S>
DI void lds_steps(const char*& pa, const char*& pa1, const char*& pa2, const char*& pa3, const char*& pb, const char* ring, unsigned ringa,
                  u32x4 (&A)[NA][4], f32x4 (&acc)[5][4]) {
  if constexpr (S < NST) {
    constexpr int ja = S - NA, jb = S - NB;
    auto cnt = [](int j0, bool skipA) constexpr {
      int n = 0;
      for (int j = j0; j < S; ++j) {
        if (j + NA < NST && !(skipA && j == j0)) n += 4;
        if (j + NB < NST) n += 5;
      }
      return n;
    };
    constexpr int ya = ja < 0 ? 4 * ((NA < NST ? NA : NST) - 1 - S) + cnt(0, false) : cnt(ja, true);
    constexpr int yb = jb < 0 ? 1000 : cnt(jb + 1, false);
    constexpr int y = ya < yb ? ya : yb;
    WAITV(y);
    bf16x8 bf[5];
#pragma unroll
    for (int n = 0; n < 5; ++n) bf[n] = *(const bf16x8*)(ring + (S % NB) * 5120 + n * 1024);
#pragma unroll
    for (int i = 0; i < 4; ++i) asm volatile("" : "+v"(A[S % NA][i]));
#define LSTEP(t, ptr) \
    _Pragma("unroll") for (int n = 0; n < 5; ++n) acc[n][t] = MFMA16(bf[n], __builtin_bit_cast(bf16x8, A[S % NA][t]), acc[n][t]); \
    __builtin_amdgcn_sched_barrier(0); \
    if constexpr (S + NA < NST) { GLD(A[S % NA][t], ptr, 0); ptr += 1024; } \
    __builtin_amdgcn_sched_barrier(0);
    LSTEP(0, pa) LSTEP(1, pa1) LSTEP(2, pa2) LSTEP(3, pa3)
    if constexpr (S + NB < NST) { glds5_sc1(pb, ringa + (S % NB) * 5120); pb += 5120; }
    lds_steps<NST, NA, NB, S + 1>(pa, pa1, pa2, pa3, pb, ring, ringa, A, acc);
  }
}
template <int NST, int NA, int NB>
DI void stream_lds(const char* pa, unsigned astr, const char* pb, char* smem, int roff, int cnt, bool poller, const char* flags, int nflags,
                   u64 tag, int lane, int rot, f32x4 (&acc)[5][4] STAMP_ARG) {
  const char* pa1 = pa + ((rot + 1) & 3) * astr; const char* pa2 = pa + ((rot + 2) & 3) * astr; const char* pa3 = pa + ((rot + 3) & 3) * astr;
  pa += rot * astr;
  const unsigned ringa = lds_addr(smem) + roff;
  u32x4 A[NA][4];
  pb += NB * 5120;
  if (poller) { polln(flags, nflags, tag, lane); set_cnt(smem, cnt, lane); }
  else wait_cnt(smem, cnt, 1);
  STAMP(8);
#pragma unroll
  for (int s = 0; s < NA; ++s) ISSUE_A(A, s);
  lds_steps<NST, NA, NB, 0>(pa, pa1, pa2, pa3, pb, smem + roff + lane * 16, ringa, A, acc);
}

template <int C>
DI void finish(const P1& p, char* smem, int rt, int ct, int w, int lane, const f32x4 (&acc)[5][4] STAMP_ARG) {
  constexpr int N0 = C == 0 ? 0 : 3, NN = C == 0 ? 3 : 2;
  const int lg = lane >> 4, l15 = lane & 15;
  const unsigned o0 = (unsigned)((rt * 64 + (w & 3) * 16 + l15) * 640 + ct * 80 + N0 * 16 + 4 * lg);
  u32x2 xv[NN];
#pragma unroll
  for (int m = 0; m < NN; ++m) xv[m] = *(const u32x2*)(p.x + o0 + 16 * m);
  char* dst = smem + slot_of(w) + lane * 16;
#pragma unroll
  for (int i = 1; i < 4; ++i)
#pragma unroll
    for (int n = 0; n < 5; ++n) *(f32x4*)(dst + ((i - 1) * 5 + n) * 1024) = acc[n][i];
#pragma unroll
  for (int m = 0; m < 5 - NN; ++m) *(f32x4*)(dst + (15 + m) * 1024) = acc[(C == 0 ? 3 : 0) + m][0];
  __syncthreads();
  STAMP(10);
  const char* same[3]; const char* other[3];
#pragma unroll
  for (int j = 1; j < 4; ++j) {
    const int v = (w - j) & 3;
    same[j - 1] = smem + slot_of(4 * C + v) + ((j - 1) * 5 + N0) * 1024 + lane * 16;
    other[j - 1] = smem + slot_of(4 * (1 - C) + v) + ((j - 1) * 5 + N0) * 1024 + lane * 16;
  }
  const char* partner = smem + slot_of(w ^ 4) + 15 * 1024 + lane * 16;
#pragma unroll
  for (int m = 0; m < NN; ++m) {
    f32x4 mine = (acc[N0 + m][0] + *(const f32x4*)(same[0] + m * 1024)) + (*(const f32x4*)(same[1] + m * 1024) + *(const f32x4*)(same[2] + m * 1024));
    f32x4 theirs = (*(const f32x4*)(partner + m * 1024) + *(const f32x4*)(other[0] + m * 1024)) +
                   (*(const f32x4*)(other[1] + m * 1024) + *(const f32x4*)(other[2] + m * 1024));
    const f32x4 ssum = C == 0 ? mine : theirs, bsum = C == 0 ? theirs : mine;
    float xs[4] = {bf_lo(xv[m][0]), bf_hi(xv[m][0]), bf_lo(xv[m][1]), bf_hi(xv[m][1])};
    float r[4];
#pragma unroll
    for (int j = 0; j < 4; ++j) {
      float bb = bsum[j];
      float g = bb * __builtin_amdgcn_rcpf(1.0f + __builtin_amdgcn_exp2f(-bb * LOG2E));
      r[j] = fmaf(ssum[j], g, xs[j]);
    }
    *(u32x2*)(p.out + o0 + 16 * m) = u32x2{pack2(r[0], r[1]), pack2(r[2], r[3])};
  }
}

__global__ __launch_bounds__(512, 2) void k_fused(P1 p) {
  extern __shared__ __attribute__((aligned(16))) char smem[];
  const int b = blockIdx.x, xcd = b & 7, idx = b >> 3;
  const int w = __builtin_amdgcn_readfirstlane(threadIdx.x >> 6), lane = threadIdx.x & 63;
  const int rb = (xcd >> 2) * 8 + (idx >> 2);
  const int jl = idx & 3, xc = xcd & 3;
  const int type = jl >> 1, g8 = 2 * xc + (jl & 1);
  const int c = w >> 2;
  STAMP_DECL; STAMP(0);
  const rsrc_t rs = __builtin_amdgcn_make_buffer_rsrc(p.ws, 0, WS_USED, 0x00020000);
  if (w == 7) {
    u64 pc;
    asm volatile("s_getpc_b64 %0" : "=s"(pc));
    touch((const char*)(pc & ~127ull) + (idx * 20 + (lane < 20 ? lane : 0)) * 128, lds_addr(smem) + L_DUMMY);
  }
  u64 tag;
  bool filled = false;
  if (type == 0) {
    if (c == 0) tag = body_a<0>(p, rs, smem, rb, g8, w, lane, filled STAMP_PASS);
    else tag = body_a<1>(p, rs, smem, rb, g8, w, lane, filled STAMP_PASS);
  } else {
    if (c == 0) tag = body_b<0>(p, rs, smem, rb, g8, w, lane, filled STAMP_PASS);
    else tag = body_b<1>(p, rs, smem, rb, g8, w, lane, filled STAMP_PASS);
  }
  STAMP(7);
  if (w == 5) {
    unsigned v = __hip_atomic_load((LDS3 unsigned*)(smem + L_CNT + 12), __ATOMIC_RELAXED, __HIP_MEMORY_SCOPE_WORKGROUP);
    if (__builtin_amdgcn_readfirstlane(v) == 0) {
      poll256(p.ws + F_EARLY, tag, lane);
      if (blockIdx.x == 0 && lane == 0) st_flag(p.ws + OFF_FLG, tag);
      set_cnt(smem, 3, lane);
    }
  } else wait_cnt(smem, 3, 1);
  const int rt = DMAP == 0 ? (xcd >> 1) * 8 + (idx >> 2) : xcd * 4 + (idx >> 3);
  const int ct = DMAP == 0 ? (xcd & 1) * 4 + (idx & 3) : (idx & 7);
  f32x4 acc[5][4];
#pragma unroll
  for (int n = 0; n < 5; ++n)
#pragma unroll
    for (int t = 0; t < 4; ++t) acc[n][t] = f32x4{0, 0, 0, 0};
  if (w >= 4) {
    const int u0 = (w - 4) * 14 - (w == 7 ? 1 : 0);
    const char* pa = p.ws + OFF_ACT + (rt * 4) * AST + u0 * 1024 + lane * 16;
    const char* pb = p.ws + OFF_W2F + (ct * 74 + u0) * 5120 + lane * 16;
    const char* fact = p.ws + F_ACT + (rt >> 1) * 128;
    bool poller = w == 4;
    if (!filled) {
      if (poller) { polln(fact, 8, tag, lane); set_cnt(smem, 5, lane); poller = false; }
      ring_fill(p, smem, w, lane, 0, 7);
    }
    if (w < 6) stream_lds<14, NA_ACT, 7>(pa, AST, pb, smem, slot_of(w), 5, poller, fact, 8, tag, lane, w & 3, acc STAMP_PASS);
    else stream_lds<13, NA_ACT, 7>(pa, AST, pb, smem, slot_of(w), 5, false, fact, 8, tag, lane, w & 3, acc STAMP_PASS);
    (void)arrive(smem, 8, 4, lane);
  } else {
    stream_o(p.ws + OFF_O + (rt * 4) * OST + 5 * w * 1024 + lane * 16,
             p.ws + OFF_W2F + (ct * 74 + 54 + 5 * w) * 5120 + lane * 16 + 2048,
             p.ws + OFF_O + (rt * 4 + 1 + 2 * ((lane >> 3) & 1)) * OST + (((5 * (lane & 7)) >> 1) + 2) * 1024 + 1008 - ((lane & 1) ? 0 : 1024) , smem, w == 0, lane, w & 3, acc STAMP_PASS);
    wait_cnt(smem, 8, 4);
  }
  STAMP(9);
  if (w >= 4) finish<1>(p, smem, rt, ct, w, lane, acc STAMP_PASS);
  else finish<0>(p, smem, rt, ct, w, lane, acc STAMP_PASS);
  STAMP(11);
  STAMP_FLUSH(p.ws, blockIdx.x * 8 + w);
}
}

extern "C" void kernel_launch(void* const* d_in, const int* in_sizes, int n_in, void* d_out, int out_size, void* d_ws,
                              size_t ws_size, hipStream_t stream) {
  using namespace hk;
  P1 p;
  p.x = (const u16*)d_in[0]; p.wqkv = (const u16*)d_in[1]; p.wphi = (const u16*)d_in[2]; p.wo = (const u16*)d_in[3];
  p.w1 = (const u16*)d_in[5]; p.b1 = (const u16*)d_in[6]; p.w2 = (const u16*)d_in[7]; p.ws = (char*)d_ws; p.out = (u16*)d_out;
  (void)hipFuncSetAttribute((const void*)k_fused, hipFuncAttributeMaxDynamicSharedMemorySize, L_END);
  k_fused<<<256, 512, L_END, stream>>>(p);
}
```

```cpp
#include <hip/hip_runtime.h>
#include <hip/hip_bf16.h>

typedef unsigned short u16;
typedef __bf16 bf16x2 __attribute__((ext_vector_type(2)));
typedef __bf16 bf16x4 __attribute__((ext_vector_type(4)));
typedef __bf16 bf16x8 __attribute__((ext_vector_type(8)));
typedef float f32x2 __attribute__((ext_vector_type(2)));
typedef float f32x4 __attribute__((ext_vector_type(4)));
typedef unsigned u32x2 __attribute__((ext_vector_type(2)));
typedef unsigned u32x4 __attribute__((ext_vector_type(4)));
#define LDS3 __attribute__((address_space(3)))
#define DI __device__ __forceinline__
#define MFMA16(a, b, c) __builtin_amdgcn_mfma_f32_16x16x32_bf16((a), (b), (c), 0, 0, 0)

#define STAMP(i)
#define STAMP_DECL
#define STAMP_FLUSH(ws, slot)
namespace hk {
typedef unsigned long long u64;
constexpr float LOG2E = 1.4426950408889634f;
constexpr float LN2 = 0.6931471805599453f;
#define ROTP 2
#define NA_ACT 3
#define DMAP 0
constexpr int FLAG_I = ROTP + 2 > 8 ? 8 : ROTP + 2;
#define APAD 256
constexpr unsigned AST = 54 * 1024 + APAD, OST = 20 * 1024 + APAD;
constexpr unsigned OFF_ACT = 0;
constexpr unsigned OFF_O = OFF_ACT + 128 * AST;
constexpr unsigned OFF_W2F = OFF_O + 128 * OST;
constexpr unsigned OFF_KVP = OFF_W2F + 8 * 74 * 5 * 1024;
constexpr unsigned OFF_KVH = OFF_KVP + 8 * 16 * 60 * 256;
constexpr unsigned OFF_STP = OFF_KVH + 8 * 30 * 256;
constexpr unsigned OFF_CSP = OFF_STP + 16 * 12800;
constexpr unsigned OFF_FLG = OFF_CSP + 16 * 12800;
constexpr unsigned F_EARLY = OFF_FLG + 128;
constexpr unsigned F_ACT = F_EARLY + 2048;
constexpr unsigned F_KVP = F_ACT + 2048;
constexpr unsigned F_KVH = F_KVP + 1024;
constexpr unsigned F_O = F_KVH + 1024;
constexpr unsigned F_ST = F_O + 2048;
constexpr unsigned F_CS = F_ST + 2048;
constexpr unsigned WS_USED = F_CS + 2048;
constexpr unsigned OOB = 0x7ffff000u;
constexpr int XB = 16384, WB0 = 3 * XB;
constexpr int L_PT = 118784;
constexpr int L_DUMMY = 151552;
constexpr int L_RAW = 155648;
constexpr int L_BIAS = L_RAW + 3200;
constexpr int L_ZERO = L_BIAS + 448;
constexpr int L_CNT = L_ZERO + 64;
constexpr int L_BIASL = L_CNT + 64;
constexpr int L_CS = L_BIASL + 896;
constexpr int L_SUM = L_CS + 1216;
constexpr int L_END = 163840;
static_assert(L_SUM + 1024 <= L_END - 256, "lds");
constexpr int E_PHI = 0;
constexpr int E_V = 40960;
constexpr int E_KB = 61440;
constexpr int E_KV = 0;

DI unsigned pack2(float a, float b) {
  f32x2 v = {a, b};
  return __builtin_bit_cast(unsigned, __builtin_convertvector(v, bf16x2));
}
DI unsigned pack2v(f32x2 v) { return __builtin_bit_cast(unsigned, __builtin_convertvector(v, bf16x2)); }
DI bf16x2 as_bf2(unsigned u) { return __builtin_bit_cast(bf16x2, u); }
DI float bf_lo(unsigned u) { return __uint_as_float(u << 16); }
DI float bf_hi(unsigned u) { return __uint_as_float(u & 0xffff0000u); }
DI float rscatter_add(float m0, float m1, float m2, float m3) {
  auto a = __builtin_amdgcn_permlane16_swap(__float_as_uint(m0), __float_as_uint(m1), false, false);
  auto b = __builtin_amdgcn_permlane16_swap(__float_as_uint(m2), __float_as_uint(m3), false, false);
  float A = __uint_as_float(a[0]) + __uint_as_float(a[1]);
  float B = __uint_as_float(b[0]) + __uint_as_float(b[1]);
  auto c = __builtin_amdgcn_permlane32_swap(__float_as_uint(A), __float_as_uint(B), false, false);
  return __uint_as_float(c[0]) + __uint_as_float(c[1]);
}
DI f32x2 lo2(f32x4 v) { return __builtin_shufflevector(v, v, 0, 1); }
DI f32x2 hi2(f32x4 v) { return __builtin_shufflevector(v, v, 2, 3); }
DI f32x2 fma2(f32x2 a, f32x2 b, f32x2 c) { return __builtin_elementwise_fma(a, b, c); }
DI f32x2 splat2(float a) { return f32x2{a, a}; }
DI f32x2 exp2v(f32x2 a) { return f32x2{__builtin_amdgcn_exp2f(a[0]), __builtin_amdgcn_exp2f(a[1])}; }
DI f32x2 rcpv(f32x2 a) { return f32x2{__builtin_amdgcn_rcpf(a[0]), __builtin_amdgcn_rcpf(a[1])}; }
DI void dual_softmax(const f32x4 (&p)[5], u32x2 (&pos)[5], u32x2 (&neg)[5]) {
  float mx = fmaxf(p[0][0], p[0][1]), mn = fminf(p[0][0], p[0][1]);
  mx = fmaxf(fmaxf(mx, p[0][2]), p[0][3]); mn = fminf(fminf(mn, p[0][2]), p[0][3]);
#pragma unroll
  for (int t = 1; t < 5; ++t) {
    mx = fmaxf(fmaxf(mx, p[t][0]), p[t][1]); mx = fmaxf(fmaxf(mx, p[t][2]), p[t][3]);
    mn = fminf(fminf(mn, p[t][0]), p[t][1]); mn = fminf(fminf(mn, p[t][2]), p[t][3]);
  }
  const f32x2 L = splat2(LOG2E), nL = splat2(-LOG2E), cx = splat2(-mx * LOG2E), cn = splat2(mn * LOG2E);
  f32x2 ep[10], en[10];
#pragma unroll
  for (int t = 0; t < 5; ++t) {
    ep[2 * t] = exp2v(fma2(lo2(p[t]), L, cx)); ep[2 * t + 1] = exp2v(fma2(hi2(p[t]), L, cx));
    en[2 * t] = exp2v(fma2(lo2(p[t]), nL, cn)); en[2 * t + 1] = exp2v(fma2(hi2(p[t]), nL, cn));
  }
  f32x2 sp = ep[0], sn = en[0];
#pragma unroll
  for (int i = 1; i < 10; ++i) { sp += ep[i]; sn += en[i]; }
  const f32x2 ip = splat2(__builtin_amdgcn_rcpf(sp[0] + sp[1])), in_ = splat2(__builtin_amdgcn_rcpf(sn[0] + sn[1]));
#pragma unroll
  for (int t = 0; t < 5; ++t) {
    pos[t] = u32x2{pack2v(ep[2 * t] * ip), pack2v(ep[2 * t + 1] * ip)};
    neg[t] = u32x2{pack2v(en[2 * t] * in_), pack2v(en[2 * t + 1] * in_)};
  }
}
DI f32x2 mish2(f32x2 hl) {
  f32x2 e = exp2v(hl);
  f32x2 d = fma2(e, e + splat2(2.0f), splat2(2.0f));
  f32x2 t = fma2(rcpv(d), splat2(-2.0f * LN2), splat2(LN2));
  return hl * t;
}
DI bf16x8 mk8(u32x2 a, u32x2 b) {
  u32x4 v = {a[0], a[1], b[0], b[1]};
  return __builtin_bit_cast(bf16x8, v);
}
DI bf16x8 tr2(const char* a, const char* b) {
  bf16x4 lo = __builtin_amdgcn_ds_read_tr16_b64_v4bf16((LDS3 bf16x4*)(a));
  bf16x4 hi = __builtin_amdgcn_ds_read_tr16_b64_v4bf16((LDS3 bf16x4*)(b));
  return __builtin_shufflevector(lo, hi, 0, 1, 2, 3, 4, 5, 6, 7);
}

struct P1 {
  const u16* x; const u16* wqkv; const u16* wphi; const u16* wo; const u16* w1; const u16* b1; const u16* w2;
  char* ws; u16* out;
};
typedef __amdgpu_buffer_rsrc_t rsrc_t;

DI unsigned lds_addr(const void* p) { return (unsigned)(size_t)(LDS3 const char*)p; }
DI void glds1(const void* g, unsigned lds) {
  unsigned keep;
  asm volatile("s_mov_b32 %0, m0\n\ts_mov_b32 m0, %2\n\ts_nop 0\n\tglobal_load_lds_dwordx4 %1, off\n\ts_mov_b32 m0, %0"
               : "=&s"(keep) : "v"(g), "s"(lds) : "memory");
}
DI void glds5_sc1(const void* g, unsigned lds) {
  unsigned keep;
  asm volatile("s_mov_b32 %0, m0\n\t"
               "s_mov_b32 m0, %2\n\ts_nop 0\n\tglobal_load_lds_dwordx4 %1, off sc1\n\t"
               "global_load_lds_dwordx4 %1, off offset:1024 sc1\n\t"
               "global_load_lds_dwordx4 %1, off offset:2048 sc1\n\t"
               "global_load_lds_dwordx4 %1, off offset:3072 sc1\n\t"
               "s_add_u32 m0, %2, 0x1000\n\ts_nop 0\n\tglobal_load_lds_dwordx4 %3, off sc1\n\t"
               "s_mov_b32 m0, %0"
               : "=&s"(keep) : "v"(g), "s"(lds), "v"((const char*)g + 4096) : "memory", "scc");
}
DI void touch(const void* g, unsigned lds) {
  unsigned keep;
  asm volatile("s_mov_b32 %0, m0\n\ts_mov_b32 m0, %2\n\ts_nop 0\n\tglobal_load_lds_dword %1, off\n\ts_mov_b32 m0, %0"
               : "=&s"(keep) : "v"(g), "s"(lds) : "memory");
}
DI u64 next_tag(u64 e) { return e * 0x9E3779B97F4A7C15ull + 0x632BE59BD9B4E019ull; }
DI void st_flag(char* p, u64 v) { __hip_atomic_store((u64*)p, v, __ATOMIC_RELAXED, __HIP_MEMORY_SCOPE_AGENT); }
DI void st16(rsrc_t rs, unsigned off, u32x4 v) { __builtin_amdgcn_raw_buffer_store_b128(v, rs, off, 0, 16); }
DI void st8(rsrc_t rs, unsigned off, u32x2 v) { __builtin_amdgcn_raw_buffer_store_b64(v, rs, off, 0, 16); }
DI void st4(rsrc_t rs, unsigned off, float v) { __builtin_amdgcn_raw_buffer_store_b32(__float_as_uint(v), rs, off, 0, 16); }
DI u32x4 ld16(rsrc_t rs, unsigned off) { return __builtin_amdgcn_raw_buffer_load_b128(rs, off, 0, 16); }
DI bool ready(u32x4 v) { return v[0] != 0xffffffffu && v[1] != 0xffffffffu && v[2] != 0xffffffffu && v[3] != 0xffffffffu; }
DI void drain() { asm volatile("s_waitcnt vmcnt(0)" ::: "memory"); }
constexpr int SPIN_MAX = 1 << 18;
DI bool flags_ok(u32x4 a, u32x4 b, u64 tag) {
  const unsigned tl = (unsigned)tag, th = (unsigned)(tag >> 32);
  return a[0] == tl && a[1] == th && a[2] == tl && a[3] == th && b[0] == tl && b[1] == th && b[2] == tl && b[3] == th;
}
DI void poll256(const char* flags, u64 tag, int lane) {
  const char* q = flags + lane * 32;
#pragma unroll 1
  for (int spin = 0; spin < SPIN_MAX; ++spin) {
    u32x4 a, b;
    asm volatile("global_load_dwordx4 %0, %2, off sc1\n\tglobal_load_dwordx4 %1, %2, off offset:16 sc1\n\ts_waitcnt vmcnt(0)"
                 : "=&v"(a), "=&v"(b) : "v"(q) : "memory");
    if (__all(flags_ok(a, b, tag))) break;
    __builtin_amdgcn_s_sleep(8);
  }
}
DI void pollq(const char* q, u64 tag) {
#pragma unroll 1
  for (int spin = 0; spin < SPIN_MAX; ++spin) {
    u32x2 a;
    asm volatile("global_load_dwordx2 %0, %1, off sc1\n\ts_waitcnt vmcnt(0)" : "=&v"(a) : "v"(q) : "memory");
    if (__all(a[0] == (unsigned)tag && a[1] == (unsigned)(tag >> 32))) break;
    __builtin_amdgcn_s_sleep(3);
  }
}
DI void polln(const char* flags, int n, u64 tag, int lane) { pollq(flags + (lane < n ? lane : 0) * 8, tag); }
DI bool arrive(char* smem, int idx, int n, int lane) {
  unsigned old = 0;
  if (lane == 0) old = __hip_atomic_fetch_add((LDS3 unsigned*)(smem + L_CNT + idx * 4), 1u, __ATOMIC_RELAXED, __HIP_MEMORY_SCOPE_WORKGROUP);
  return __builtin_amdgcn_readfirstlane(old) == (unsigned)(n - 1);
}
DI void set_cnt(char* smem, int idx, int lane) {
  if (lane == 0) __hip_atomic_store((LDS3 unsigned*)(smem + L_CNT + idx * 4), 1u, __ATOMIC_RELAXED, __HIP_MEMORY_SCOPE_WORKGROUP);
}
DI void wait_cnt(char* smem, int idx, unsigned n) {
#pragma unroll 1
  for (int spin = 0; spin < (SPIN_MAX << 4); ++spin) {
    unsigned v = __hip_atomic_load((LDS3 unsigned*)(smem + L_CNT + idx * 4), __ATOMIC_RELAXED, __HIP_MEMORY_SCOPE_WORKGROUP);
    if (__builtin_amdgcn_readfirstlane(v) >= n) break;
    __builtin_amdgcn_s_sleep(1);
  }
  asm volatile("" ::: "memory");
}

template <int NT> struct Frags { bf16x8 x[2]; bf16x8 w[NT]; };

#define STAMP_ARG
#define STAMP_PASS
template <int N> DI void waitv() { asm volatile("s_waitcnt vmcnt(%0)" :: "i"(N) : "memory"); }
DI void waitl() { __builtin_amdgcn_s_waitcnt(0xC07F); }

template <int TYPE, int C>
struct Gemm {
  static constexpr int NT = TYPE == 0 ? 5 : (C == 0 ? 9 : 10);
  static constexpr int NU = TYPE == 0 ? 11 : 19;
  static constexpr int WROW = NU * 32, NC = NU * 2, HB = 32 * WROW;
  static constexpr int NWH = TYPE == 0 ? 6 : 5;
  static constexpr int NWS = (NU + 7) / 8;
  static constexpr int NFULL = NU - (NWS - 1) * 8;
  static constexpr int MAGIC = (65536 + NC - 1) / NC;
  static constexpr bool VW = (TYPE == 0 && C == 1);
  static constexpr int unit_of(int t) { return TYPE == 0 ? t + 5 * C : t + 9 * C; }

  const char* xsrc[2];
  const char* wsrc[NWS];
  unsigned winc[NWS];
  unsigned xa0, wa, lds0;
  int cx, hw, hslot, w;
  bool full;
  char* smem;
  f32x4 acc[NT][2];

  DI void init(const P1& p, char* smem_, int rb, int g8, int t0w, int rot, int w_, int lane) {
    smem = smem_; w = w_; cx = rot; hw = 2 * rot; hslot = 0; lds0 = lds_addr(smem_); full = w < NFULL;
    const int r = w & 3, lg = lane >> 4, l15 = lane & 15;
#pragma unroll
    for (int i = 0; i < 2; ++i) {
      int row = (i * 8 + w) * 8 + (lane >> 3);
      int ch = (lane & 7) ^ ((row >> 1) & 7);
      xsrc[i] = (const char*)p.x + (unsigned)((rb * 128 + row) * 1280 + ch * 16 + rot * 128);
    }
#pragma unroll
    for (int i = 0; i < NWS; ++i) {
      int ci = (i * 8 + w) * 64 + lane;
      ci = ci < 32 * NC ? ci : lane;
      int pos = (ci * MAGIC) >> 16, c16 = ci - pos * NC;
      int kk = pos & 15;
      int k = (kk >= 4 && kk < 12) ? (pos ^ 12) : pos;
      bool isq = TYPE == 0 || c16 < 10;
      int colb;
      if (TYPE == 0) { int c = min(c16, 19); colb = 1280 + 160 * g8 + c * 16 + (c >= 10 ? 1120 : 0); }
      else colb = isq ? (160 * g8 + c16 * 16) : (32 * t0w + (c16 - 10) * 16);
      unsigned stride = isq ? 3840u : 3456u;
      wsrc[i] = (isq ? (const char*)p.wqkv : (const char*)p.w1) + (unsigned)((k + rot * 64) * stride + colb);
      winc[i] = stride * 32;
    }
    xa0 = (32 * r + l15) * 128 + ((lg ^ ((l15 >> 1) & 7)) * 16);
    wa = WB0 + (16 * (lg >> 1) + 4 * (lg & 1) + (l15 >> 2)) * WROW + (VW ? 160 + (l15 & 3) * 40 : (l15 & 3) * 8);
#pragma unroll
    for (int t = 0; t < NT; ++t) { acc[t][0] = f32x4{0, 0, 0, 0}; acc[t][1] = f32x4{0, 0, 0, 0}; }
  }
  DI void issue_x1(int s, int xslot) {
    glds1(xsrc[s], lds0 + xslot * XB + w * 1024 + s * 0x2000);
    xsrc[s] += 128;
    if (s == 1 && ++cx == 10) { cx = 0; xsrc[0] -= 1280; xsrc[1] -= 1280; }
  }
  DI void issue_w1(int s) {
    if (s < NWS - 1 || full) { glds1(wsrc[s], lds0 + WB0 + hslot * HB + w * 1024 + s * 0x2000); wsrc[s] += winc[s]; }
    if (s == NWS - 1) {
      hslot = hslot == NWH - 1 ? 0 : hslot + 1;
      if (++hw == 20) {
        hw = 0;
#pragma unroll
        for (int i = 0; i < NWS; ++i) wsrc[i] -= 20 * winc[i];
      }
    }
  }
  DI void issue_x(int xslot) { issue_x1(0, xslot); issue_x1(1, xslot); }
  DI void issue_w() {
#pragma unroll
    for (int s = 0; s < NWS; ++s) issue_w1(s);
  }
  template <int NX, int NH, int E> DI void wait() {
    if (full) waitv<2 * NX + NH * NWS + E>(); else waitv<2 * NX + NH * (NWS - 1) + E>();
  }
  DI void rd_x(Frags<NT>& f, int xslot, int ks, int tt) { f.x[tt] = *(const bf16x8*)(smem + xslot * XB + (xa0 ^ (ks * 64)) + tt * 2048); }
  DI void rd_w(Frags<NT>& f, int hs, int t) {
    const char* q = smem + hs * HB + wa + (VW ? t * 8 : unit_of(t) * 32);
    f.w[t] = tr2(q, q + 8 * WROW);
  }
  DI void kstep(const Frags<NT>& f, Frags<NT>& g, int nxs, int nks, int nhs, bool do_rd, int nreq, int ixs) {
    constexpr int NS = 2 + 2 * NWS;
#pragma unroll
    for (int t = 0; t < NT; ++t) {
      if (do_rd) {
        if (t < 2) rd_x(g, nxs, nks, t);
        rd_w(g, nhs, t);
      }
#pragma unroll
      for (int tt = 0; tt < 2; ++tt)
        acc[t][tt] = VW ? MFMA16(f.x[tt], f.w[t], acc[t][tt]) : MFMA16(f.w[t], f.x[tt], acc[t][tt]);
#pragma unroll
      for (int s = (t * NS) / NT; s < ((t + 1) * NS) / NT; ++s) {
        if (s < NWS) { if (nreq > 0) issue_w1(s); }
        else if (s < NWS + 2) { if (nreq > 1) issue_x1(s - NWS, ixs); }
        else { if (nreq > 1) issue_w1(s - NWS - 2); }
      }
      __builtin_amdgcn_sched_barrier(0);
    }
  }
};

DI void dots2(float& c0, float& c1, const bf16x8& wa_, const bf16x8& wb_) {
  u32x4 a = __builtin_bit_cast(u32x4, wa_), b = __builtin_bit_cast(u32x4, wb_);
  unsigned a0 = a[0], a1 = a[1], a2 = a[2], a3 = a[3], b0 = b[0], b1 = b[1], b2 = b[2], b3 = b[3];
  asm volatile("s_nop 1\n\t"
               "v_dot2c_f32_bf16 %0, 0x3f803f80, %2\n\tv_dot2c_f32_bf16 %1, 0x3f803f80, %6\n\t"
               "v_dot2c_f32_bf16 %0, 0x3f803f80, %3\n\tv_dot2c_f32_bf16 %1, 0x3f803f80, %7\n\t"
               "v_dot2c_f32_bf16 %0, 0x3f803f80, %4\n\tv_dot2c_f32_bf16 %1, 0x3f803f80, %8\n\t"
               "v_dot2c_f32_bf16 %0, 0x3f803f80, %5\n\tv_dot2c_f32_bf16 %1, 0x3f803f80, %9\n\ts_nop 3"
               : "+v"(c0), "+v"(c1) : "v"(a0), "v"(a1), "v"(a2), "v"(a3), "v"(b0), "v"(b1), "v"(b2), "v"(b3));
}
DI void dots_x(float& s1, float& s2, const bf16x8& x) {
  u32x4 a = __builtin_bit_cast(u32x4, x);
  unsigned a0 = a[0], a1 = a[1], a2 = a[2], a3 = a[3];
  asm volatile("s_nop 1\n\t"
               "v_dot2c_f32_bf16 %0, 0x3f803f80, %2\n\tv_dot2c_f32_bf16 %1, %2, %2\n\t"
               "v_dot2c_f32_bf16 %0, 0x3f803f80, %3\n\tv_dot2c_f32_bf16 %1, %3, %3\n\t"
               "v_dot2c_f32_bf16 %0, 0x3f803f80, %4\n\tv_dot2c_f32_bf16 %1, %4, %4\n\t"
               "v_dot2c_f32_bf16 %0, 0x3f803f80, %5\n\tv_dot2c_f32_bf16 %1, %5, %5\n\ts_nop 3"
               : "+v"(s1), "+v"(s2) : "v"(a0), "v"(a1), "v"(a2), "v"(a3));
}
DI f32x2 stat_of(const char* smem, int tok) { return *(const f32x2*)(smem + L_SUM + tok * 8); }
DI void mish_pair(rsrc_t rs, const char* smem, const f32x4 (&a0)[2], const f32x4 (&a1)[2], int ucs, int wl, int wt, bool skip,
                  const f32x2 (&st)[2], int tt0, int lane) {
  const int lg = lane >> 4;
  f32x4 bb[2], c4[2];
#pragma unroll
  for (int h = 0; h < 2; ++h) {
    bb[h] = *(const f32x4*)(smem + L_BIASL + ((wl + h) * 16 + 4 * lg) * 4);
    c4[h] = *(const f32x4*)(smem + L_CS + ((ucs + h) * 16 + 4 * lg) * 4);
  }
#pragma unroll
  for (int tt = 0; tt < 2; ++tt) {
    const f32x2 rsL = splat2(st[tt][0] * LOG2E), nbL = splat2(st[tt][1] * LOG2E);
    u32x4 o;
    o[0] = pack2v(mish2(fma2(lo2(a0[tt]), rsL, fma2(nbL, lo2(c4[0]), lo2(bb[0])))));
    o[1] = pack2v(mish2(fma2(hi2(a0[tt]), rsL, fma2(nbL, hi2(c4[0]), hi2(bb[0])))));
    o[2] = pack2v(mish2(fma2(lo2(a1[tt]), rsL, fma2(nbL, lo2(c4[1]), lo2(bb[1])))));
    o[3] = pack2v(mish2(fma2(hi2(a1[tt]), rsL, fma2(nbL, hi2(c4[1]), hi2(bb[1])))));
    st16(rs, skip ? OOB : OFF_ACT + (tt0 + tt) * AST + (wt >> 1) * 1024 + lane * 16, o);
  }
}
DI void load_aphi(const char* smem, int lane, bf16x8 (&ap)[5][3]) {
  const int lg = lane >> 4, l15 = lane & 15;
  const char* pa[6];
#pragma unroll
  for (int i = 0; i < 6; ++i) {
    int d0 = 16 * i + 4 * lg;
    int h = (d0 * 13) >> 8;
    bool ok = (h == (l15 & 3)) && i < 5;
    pa[i] = smem + (ok ? L_RAW + h * 800 + (d0 - 20 * h + (l15 >> 2)) * 40 : L_ZERO);
  }
#pragma unroll
  for (int t = 0; t < 5; ++t)
#pragma unroll
    for (int ks = 0; ks < 3; ++ks) ap[t][ks] = tr2(pa[2 * ks] + t * 8, pa[2 * ks + 1] + t * 8);
}
DI void pack_k(const char* smem, const f32x4& a, int t, f32x2 st, int lg, u32x2& kb) {
  f32x4 c4 = *(const f32x4*)(smem + L_CS + (t * 16 + 4 * lg) * 4);
  const f32x2 rsv = splat2(st[0]), nb = splat2(st[1]);
  kb = u32x2{pack2v(fma2(lo2(a), rsv, nb * lo2(c4))), pack2v(fma2(hi2(a), rsv, nb * hi2(c4)))};
}
DI void features(const u32x2 (&kb)[5], const bf16x8 (&ap)[5][3], u32x2 (&pos)[5], u32x2 (&neg)[5]) {
  bf16x8 b0 = mk8(kb[0], kb[1]), b1 = mk8(kb[2], kb[3]), b2 = mk8(kb[4], u32x2{0u, 0u});
  f32x4 pk[5];
#pragma unroll
  for (int t = 0; t < 5; ++t) pk[t] = MFMA16(ap[t][2], b2, MFMA16(ap[t][1], b1, MFMA16(ap[t][0], b0, (f32x4{0, 0, 0, 0}))));
  dual_softmax(pk, pos, neg);
}
DI void barrier() {
  waitl();
  __builtin_amdgcn_s_barrier();
  __builtin_amdgcn_sched_barrier(0);
}

template <int TYPE, int C, class G>
DI u64 first_pass(G& g, const P1& p, rsrc_t rs, char* smem, int rb, int g8, int t0w, int w, int lane STAMP_ARG) {
  constexpr int NT = G::NT, NWH = G::NWH;
  const int r = w & 3, lg = lane >> 4, l15 = lane & 15;
  const unsigned lds0 = lds_addr(smem);
  u32x2 ev;
  asm volatile("global_load_dwordx2 %0, %1, off sc1" : "=v"(ev) : "v"(p.ws + OFF_FLG) : "memory");
  const int job = TYPE * 8 + g8;
  const int myc = (rb + job) % 10;
  const int rot = (myc / ROTP) * ROTP;
  const int my_i = myc - rot;
  g.init(p, smem, rb, g8, t0w, rot, w, lane);
  if (C == 1) {
    int ci = r * 64 + lane;
    if (ci < 200) glds1((const char*)p.wphi + g8 * 3200 + ci * 16, lds0 + L_RAW + r * 1024);
  } else if (w == 3) {
    if (TYPE == 1 && lane < 28) glds1((const char*)p.b1 + t0w * 32 + lane * 16, lds0 + L_BIAS);
    if (lane < 32) *(unsigned*)(smem + L_ZERO + lane * 4) = 0u;
  }
  g.issue_w(); g.issue_x(0); g.issue_w();
  __builtin_amdgcn_s_barrier();
  g.issue_w(); g.issue_x(1); g.issue_w();
  g.issue_w(); g.issue_x(2);
  if (NWH == 6) g.issue_w();
  constexpr int AH = NWH - 4;
  const int ptj = w * 128 + rb * 8 + g8;
  const bool has_pt = TYPE == 0 && w < 6 && ptj < 740;
  const int ptu = ptj / 10, ptL = ptj - ptu * 10;
  if (has_pt) {
#pragma unroll
    for (int i = 0; i < 4; ++i) {
      int rho = i * 8 + (lane >> 3);
      const u16* src;
      if (ptu < 54) src = p.w2 + (unsigned)((32 * ptu + rho) * 640);
      else {
        int T = 2 * (ptu - 54) + (rho >> 4), Gq = (T * 13) >> 6;
        src = p.wo + (unsigned)(((4 * Gq + ((rho >> 2) & 3)) * 20 + 4 * (T - 5 * Gq) + (rho & 3)) * 640);
      }
      glds1((const char*)src + ptL * 128 + (lane & 7) * 16, lds0 + L_PT + w * 4096 + i * 1024);
    }
  }
  float cs[3] = {0.f, 0.f, 0.f}, sx[2] = {0.f, 0.f}, cd = 0.f;
  auto do_dots = [&](const Frags<NT>& f) {
    dots_x(sx[0], sx[1], f.x[C]);
    if (r == 0) { dots2(cs[0], cs[1], f.w[0], f.w[4]); if (NT > 8) dots2(cs[2], cd, f.w[NT > 8 ? 8 : 0], f.w[0]); }
    else if (r == 1) { dots2(cs[0], cs[1], f.w[1], f.w[NT > 5 ? 5 : 0]); if (NT > 9) dots2(cs[2], cd, f.w[NT > 9 ? 9 : 0], f.w[0]); }
    else if (r == 2) dots2(cs[0], cs[1], f.w[2], f.w[NT > 6 ? 6 : 0]);
    else dots2(cs[0], cs[1], f.w[3], f.w[NT > 7 ? 7 : 0]);
  };
  Frags<NT> f0, f1;
  if (has_pt) g.template wait<2, NWH - 2, 4>(); else g.template wait<2, NWH - 2, 0>();
  barrier();
  STAMP(1);
  g.rd_x(f0, 0, 0, 0); g.rd_x(f0, 0, 0, 1);
#pragma unroll
  for (int t = 0; t < NT; ++t) g.rd_w(f0, 0, t);
  if (TYPE == 1) {
    int tid = w * 64 + lane;
    if (tid < 224) *(float*)(smem + L_BIASL + tid * 4) = bf_lo(*(const u16*)(smem + L_BIAS + tid * 2)) * LOG2E;
  }
  asm volatile("" : "+v"(ev));
  const u64 tag = next_tag(((u64)(unsigned)__builtin_amdgcn_readfirstlane(ev[1]) << 32) | (unsigned)__builtin_amdgcn_readfirstlane(ev[0]));
  int xs = 0, hs = 0;
  u32x4 fa, fb;
  u32x2 tf;
  unsigned pv[10], pw[10];
  const char* tfp = p.ws + (lane < 10 ? F_ST + (rb * 16 + lane) * 8 : F_CS + (job * 16 + (lane < 20 ? lane - 10 : 0)) * 8);
  auto inc = [](int s) { return s == NWH - 1 ? 0 : s + 1; };
#define LD10(pv, q) \
    asm volatile("global_load_dword %0, %10, off sc1\n\tglobal_load_dword %1, %10, off offset:1280 sc1\n\t" \
                 "global_load_dword %2, %10, off offset:2560 sc1\n\tglobal_load_dword %3, %10, off offset:3840 sc1\n\t" \
                 "global_load_dword %4, %11, off sc1\n\tglobal_load_dword %5, %11, off offset:1280 sc1\n\t" \
                 "global_load_dword %6, %11, off offset:2560 sc1\n\tglobal_load_dword %7, %11, off offset:3840 sc1\n\t" \
                 "global_load_dword %8, %12, off sc1\n\tglobal_load_dword %9, %12, off offset:1280 sc1" \
                 : "=&v"(pv[0]), "=&v"(pv[1]), "=&v"(pv[2]), "=&v"(pv[3]), "=&v"(pv[4]), "=&v"(pv[5]), "=&v"(pv[6]), "=&v"(pv[7]), "=&v"(pv[8]), "=&v"(pv[9]) \
                 : "v"(q), "v"((q) + 5120), "v"((q) + 10240) : "memory")
#pragma unroll 1
  for (int i = 0; i < 8; ++i) {
    waitl();
    __builtin_amdgcn_sched_barrier(0);
    const int h1 = inc(hs), h2 = inc(h1);
    g.kstep(f0, f1, xs, 1, h1, true, 0, 0);
    if (i == my_i) do_dots(f0);
    const int nxs = xs == 2 ? 0 : xs + 1;
    if (TYPE == 0) {
      if (i == 0 && has_pt) g.template wait<1, AH, 4>();
      else if (i == 1) g.template wait<1, AH, 2>();
      else if (i == 2 && has_pt) g.template wait<1, AH, 6>();
      else if (i == 2) g.template wait<1, AH, 2>();
      else if (i == 3 && has_pt) g.template wait<1, AH, 4>();
      else g.template wait<1, AH, 0>();
    } else {
      if (i == 1) g.template wait<1, AH, 3>();
      else g.template wait<1, AH, 0>();
    }
    barrier();
    if (i == FLAG_I && w == 0 && lane < 3)
      st_flag(p.ws + (lane == 0 ? F_EARLY + blockIdx.x * 8 : (lane == 1 ? F_ST + (rb * 16 + job) * 8 : F_CS + (job * 16 + rb) * 8)), tag);
    g.kstep(f1, f0, nxs, 0, h2, true, i < 7 ? 3 : (NWH == 5 ? 1 : 0), xs);
    if (i == 0) {
      const u32x4 sent = {~0u, ~0u, ~0u, ~0u};
      if (TYPE == 0) {
        unsigned kb = OFF_KVP + (g8 * 16 + rb) * 15360 + lane * 16;
        st16(rs, kb + w * 1024, sent);
        st16(rs, w < 7 ? kb + (8 + w) * 1024 : (lane < 30 ? OFF_KVH + g8 * 7680 + rb * 480 + lane * 16 : OOB), sent);
      } else {
        const unsigned ob = OFF_O + (rb * 8 + w) * OST + ((5 * g8) >> 1) * 1024 + lane * 16;
        st16(rs, ob + 1024, sent);
        st16(rs, ob + ((g8 & 1) ? 2048 : 0), sent);
        st8(rs, ob + ((g8 & 1) ? 8 : 2048), u32x2{~0u, ~0u});
      }
    }
    if (i == 1 && has_pt) {
      const char* q = smem + L_PT + w * 4096 + (4 * lg + (l15 >> 2)) * 128 + (l15 & 3) * 8;
#pragma unroll
      for (int n = 0; n < 4; ++n) {
        bf16x8 f = tr2(q + n * 32, q + 2048 + n * 32);
        int N = 4 * ptL + n, ct = (N * 13) >> 6;
        st16(rs, OFF_W2F + ((ct * 74 + ptu) * 5 + (N - 5 * ct)) * 1024 + lane * 16, __builtin_bit_cast(u32x4, f));
      }
    }
    if (i == my_i) {
      do_dots(f1);
      float v = rscatter_add(sx[0], sx[1], cs[0], cs[1]);
      unsigned a = lg < 2 ? OFF_STP + (rb * 10 + myc) * 1280 + lg * 512 + (32 * r + 16 * C + l15) * 4
                          : OFF_CSP + (job * 10 + myc) * 1280 + ((G::unit_of(0) + r + (lg == 2 ? 0 : 4)) * 16 + l15) * 4;
      bool on = lg < 2 ? job < 10 : (rb < 10 && (lg == 2 || r + 4 < NT));
      st4(rs, on ? a : OOB, v);
      if (NT > 8) {
        float v2 = rscatter_add(cs[2], 0.f, 0.f, 0.f);
        st4(rs, (lg == 0 && rb < 10 && r + 8 < NT) ? OFF_CSP + (job * 10 + myc) * 1280 + ((G::unit_of(0) + r + 8) * 16 + l15) * 4 : OOB, v2);
      }
    }
    if (i == 6) {
      asm volatile("global_load_dwordx2 %0, %1, off sc1" : "=v"(tf) : "v"(tfp) : "memory");
      if (w == 5)
        asm volatile("global_load_dwordx4 %0, %2, off sc1\n\tglobal_load_dwordx4 %1, %2, off offset:16 sc1"
                     : "=&v"(fa), "=&v"(fb) : "v"(p.ws + F_EARLY + lane * 32) : "memory");
    }
    xs = nxs; hs = h2;
  }
  {
    waitl();
    __builtin_amdgcn_sched_barrier(0);
    const int h1 = inc(hs), h2 = inc(h1);
    g.kstep(f0, f1, xs, 1, h1, true, 0, 0);
    const int nxs = xs == 2 ? 0 : xs + 1;
    waitv<0>();
    barrier();
    asm volatile("" : "+v"(tf));
    if (!__all(tf[0] == (unsigned)tag && tf[1] == (unsigned)(tag >> 32))) pollq(tfp, tag);
    if (C == 1 && w == 5) {
      asm volatile("" : "+v"(fa), "+v"(fb));
      if (__all(flags_ok(fa, fb, tag))) {
        if (blockIdx.x == 0 && lane == 0) st_flag(p.ws + OFF_FLG, tag);
        set_cnt(smem, 3, lane);
        constexpr int NL = DMAP == 0 ? 370 : 740;
        const char* q2 = p.ws + OFF_W2F + (DMAP == 0 ? (blockIdx.x & 1) * 4 * 378880 : 0) + ((blockIdx.x >> 3) * NL + lane) * 128;
#pragma unroll
        for (int j = 0; j < (NL + 63) / 64; ++j) touch(q2 + (j * 64 + lane < NL ? j * 8192 : 0), lds0 + L_DUMMY);
      }
    }
    const int tid = w * 64 + lane;
    const char* q = p.ws + (tid < 128 ? OFF_STP + rb * 12800 + tid * 4 : OFF_CSP + job * 12800 + min(tid - 128, 303) * 4);
    LD10(pv, q);
    if constexpr (C == 0) { LD10(pw, q + 512); }
    g.kstep(f1, f0, nxs, 0, h2, true, 0, 0);
    xs = nxs; hs = h2;
  }
  waitl();
  __builtin_amdgcn_sched_barrier(0);
  g.kstep(f0, f1, xs, 1, inc(hs), true, 0, 0);
  waitl();
  __builtin_amdgcn_sched_barrier(0);
  g.kstep(f1, f0, 0, 0, 0, false, 0, 0);
  STAMP(2);
  {
    waitv<0>();
    asm volatile("" : "+v"(pv[0]), "+v"(pv[1]), "+v"(pv[2]), "+v"(pv[3]), "+v"(pv[4]), "+v"(pv[5]), "+v"(pv[6]), "+v"(pv[7]), "+v"(pv[8]), "+v"(pv[9]) :: "memory");
    float v = 0.f;
#pragma unroll
    for (int i = 0; i < 10; ++i) v += __uint_as_float(pv[i]);
    const int tid = w * 64 + lane;
    if constexpr (C == 0) {
      asm volatile("" : "+v"(pw[0]), "+v"(pw[1]), "+v"(pw[2]), "+v"(pw[3]), "+v"(pw[4]), "+v"(pw[5]), "+v"(pw[6]), "+v"(pw[7]), "+v"(pw[8]), "+v"(pw[9]) :: "memory");
      if (w < 2) {
        float v2 = 0.f;
#pragma unroll
        for (int i = 0; i < 10; ++i) v2 += __uint_as_float(pw[i]);
        float mean = v * (1.0f / 640.0f);
        float var = fmaxf(fmaf(-mean, mean, v2 * (1.0f / 640.0f)), 0.f);
        float rstd = __builtin_amdgcn_rsqf(var + 1e-5f);
        *(f32x2*)(smem + L_SUM + tid * 8) = f32x2{rstd, -rstd * mean};
      }
    }
    if (tid >= 128 && tid < 432) *(float*)(smem + L_CS + (tid - 128) * 4) = v;
  }
  barrier();
  STAMP(3);
  return tag;
}

constexpr int RING0 = 8192, RINGB = 35840;
DI int slot_of(int w) { return w >= 4 ? RING0 + (w - 4) * RINGB : RING0 + w * RINGB + 18432; }
DI int d_rt() { const int xcd = blockIdx.x & 7, idx = blockIdx.x >> 3; return DMAP == 0 ? (xcd >> 1) * 8 + (idx >> 2) : xcd * 4 + (idx >> 3); }
DI int d_ct() { const int xcd = blockIdx.x & 7, idx = blockIdx.x >> 3; return DMAP == 0 ? (xcd & 1) * 4 + (idx & 3) : (idx & 7); }
DI int d_u0(int w) { return (w - 4) * 14 - (w == 7 ? 1 : 0); }
DI void ring_fill(const P1& p, char* smem, int w, int lane, int s0, int s1) {
  const char* pb = p.ws + OFF_W2F + (d_ct() * 74 + d_u0(w)) * 5120 + lane * 16;
  const unsigned ringa = lds_addr(smem) + slot_of(w);
#pragma unroll
  for (int s = s0; s < s1; ++s) glds5_sc1(pb + s * 5120, ringa + s * 5120);
}
DI bool early_ok(char* smem) {
  unsigned v = __hip_atomic_load((LDS3 unsigned*)(smem + L_CNT + 12), __ATOMIC_RELAXED, __HIP_MEMORY_SCOPE_WORKGROUP);
  return __builtin_amdgcn_readfirstlane(v) != 0;
}

DI void need_early(const P1& p, char* smem, u64 tag, int lane) {
  if (early_ok(smem)) return;
  poll256(p.ws + F_EARLY, tag, lane);
  if (blockIdx.x == 0 && lane == 0) st_flag(p.ws + OFF_FLG, tag);
  set_cnt(smem, 3, lane);
}
DI void poll_word(const char* q) {
#pragma unroll 1
  for (int spin = 0; spin < (SPIN_MAX >> 3); ++spin) {
    unsigned a;
    asm volatile("global_load_dword %0, %1, off sc1\n\ts_waitcnt vmcnt(0)" : "=&v"(a) : "v"(q) : "memory");
    if (__all(a != ~0u)) break;
    __builtin_amdgcn_s_sleep(2);
  }
}

template <int C>
DI u64 body_a(const P1& p, rsrc_t rs, char* smem, int rb, int g8, int w, int lane, bool& filled STAMP_ARG) {
  const int r = w & 3, lg = lane >> 4, l15 = lane & 15;
  Gemm<0, C> g;
  const u64 tag = first_pass<0, C>(g, p, rs, smem, rb, g8, 0, w, lane STAMP_PASS);
  u32x2 kb[5];
  bf16x8 ap[5][3];
  load_aphi(smem, lane, ap);
  if constexpr (C == 0) {
    const f32x2 s0 = stat_of(smem, 32 * r + l15), s1 = stat_of(smem, 32 * r + 16 + l15);
#pragma unroll
    for (int t = 0; t < 5; ++t) {
      u32x2 k1;
      pack_k(smem, g.acc[t][1], t, s1, lg, k1);
      *(u32x2*)(smem + E_KB + (r * 5 + t) * 512 + lane * 8) = k1;
      pack_k(smem, g.acc[t][0], t, s0, lg, kb[t]);
    }
  } else {
    f32x4 sa[2], sb[2];
#pragma unroll
    for (int tt = 0; tt < 2; ++tt) {
      sa[tt] = *(const f32x4*)(smem + L_SUM + (32 * r + 16 * tt + 4 * lg) * 8);
      sb[tt] = *(const f32x4*)(smem + L_SUM + (32 * r + 16 * tt + 4 * lg + 2) * 8);
    }
#pragma unroll
    for (int t = 0; t < 5; ++t) {
      float c1 = *(const float*)(smem + L_CS + ((5 + t) * 16 + l15) * 4);
      unsigned o[4];
#pragma unroll
      for (int tt = 0; tt < 2; ++tt) {
        float y0 = fmaf(g.acc[t][tt][0], sa[tt][0], sa[tt][1] * c1);
        float y1 = fmaf(g.acc[t][tt][1], sa[tt][2], sa[tt][3] * c1);
        float y2 = fmaf(g.acc[t][tt][2], sb[tt][0], sb[tt][1] * c1);
        float y3 = fmaf(g.acc[t][tt][3], sb[tt][2], sb[tt][3] * c1);
        o[2 * tt] = pack2(y0, y1); o[2 * tt + 1] = pack2(y2, y3);
      }
      *(u32x4*)(smem + E_V + (r * 5 + t) * 1024 + lane * 16) = u32x4{o[0], o[1], o[2], o[3]};
    }
  }
  barrier();
  if constexpr (C == 1) {
#pragma unroll
    for (int t = 0; t < 5; ++t) kb[t] = *(const u32x2*)(smem + E_KB + (r * 5 + t) * 512 + lane * 8);
  }
  {
    const int tok = 32 * r + 16 * C + l15;
    u32x2 pos[5], neg[5];
    features(kb, ap, pos, neg);
#pragma unroll
    for (int t = 0; t < 5; ++t) {
      *(u32x2*)(smem + E_PHI + (t * 128 + tok) * 32 + lg * 8) = pos[t];
      *(u32x2*)(smem + E_PHI + ((5 + t) * 128 + tok) * 32 + lg * 8) = neg[t];
    }
  }
  barrier();
  STAMP(4);
  {
    const unsigned pa = E_PHI + (4 * lg + (l15 >> 2)) * 32 + (l15 & 3) * 8;
    const u32x4 one4 = {0x3f803f80u, 0x3f803f80u, 0x3f803f80u, 0x3f803f80u};
    const bool valid = (l15 >> 2) == lg;
    f32x4 a[3][3];
    const char* qa[3]; const char* vb[3]; bool hf[3];
#pragma unroll
    for (int k3 = 0; k3 < 3; ++k3) {
      const int un0 = w + 8 * k3, un = un0 < 20 ? un0 : 19;
      hf[k3] = un & 1;
      qa[k3] = smem + pa + (un >> 1) * 4096;
      vb[k3] = smem + E_V + (un & 1) * 3072 + lane * 16;
#pragma unroll
      for (int j = 0; j < 3; ++j) a[k3][j] = f32x4{0, 0, 0, 0};
    }
#pragma unroll
    for (int ks = 0; ks < 4; ++ks)
#pragma unroll
      for (int k3 = 0; k3 < (C == 0 ? 3 : 2); ++k3) {
        bf16x8 af = tr2(qa[k3] + ks * 1024, qa[k3] + ks * 1024 + 512);
        const char* v = vb[k3] + ks * 5120;
        a[k3][0] = MFMA16(af, *(const bf16x8*)(v), a[k3][0]);
        a[k3][1] = MFMA16(af, *(const bf16x8*)(v + 1024), a[k3][1]);
        u32x4 b2 = *(const u32x4*)(v + (hf[k3] ? 0 : 2048));
        b2 = hf[k3] ? one4 : b2;
        a[k3][2] = MFMA16(af, __builtin_bit_cast(bf16x8, b2), a[k3][2]);
      }
#pragma unroll
    for (int k3 = 0; k3 < (C == 0 ? 3 : 2); ++k3) {
      const int un0 = w + 8 * k3;
      unsigned dst = (valid && un0 < 20) ? OFF_KVP + ((g8 * 16 + rb) * 60 + un0 * 3) * 256 + (lg * 4 + (l15 & 3)) * 16 : OOB;
#pragma unroll
      for (int j = 0; j < 3; ++j) st16(rs, dst + j * 256, __builtin_bit_cast(u32x4, a[k3][j]));
    }
  }
  (void)arrive(smem, 1, 8, lane);
  STAMP(5);
  if (C == 0 && w == 1 && rb < 15) {
    need_early(p, smem, tag, lane);
    f32x4 a;
#pragma unroll 1
    for (int spin = 0; spin < 8; ++spin) {
      poll_word(p.ws + OFF_KVP + (g8 * 16 + l15) * 15360 + rb * 1024 + lg * 256 + 252);
      asm volatile("" ::: "memory");
      u32x4 kv[16];
#pragma unroll
      for (int i = 0; i < 16; ++i) kv[i] = ld16(rs, OFF_KVP + (g8 * 16 + i) * 15360 + rb * 1024 + lane * 16);
      bool ok = ready(kv[0]);
      a = __builtin_bit_cast(f32x4, kv[0]);
#pragma unroll
      for (int i = 1; i < 16; ++i) { ok = ok && ready(kv[i]); a += __builtin_bit_cast(f32x4, kv[i]); }
      if (__all(ok)) break;
    }
    int tau = rb * 4 + lg, m = (tau * 43) >> 8, tn = tau - 6 * m;
    int sg = m >= 5 ? 1 : 0, t = m - 5 * sg;
    int ks = t < 4 ? sg * 2 + (t >> 1) : 4, hf = t < 4 ? (t & 1) : sg;
    st8(rs, OFF_KVH + g8 * 7680 + (tn * 5 + ks) * 256 + l15 * 16 + hf * 8, u32x2{pack2(a[0], a[1]), pack2(a[2], a[3])});
    set_cnt(smem, 2, lane);
  } else if (rb < 15) wait_cnt(smem, 2, 1);
  wait_cnt(smem, 1, 8);
  STAMP(6);
  return tag;
}

template <int C>
DI u64 body_b(const P1& p, rsrc_t rs, char* smem, int rb, int g8, int w, int lane, bool& filled STAMP_ARG) {
  const int r = w & 3, lg = lane >> 4, l15 = lane & 15;
  const int t0w = min(14 * g8, 94);
  Gemm<1, C> g;
  const u64 tag = first_pass<1, C>(g, p, rs, smem, rb, g8, t0w, w, lane STAMP_PASS);
  f32x2 st[2] = {stat_of(smem, 32 * r + l15), stat_of(smem, 32 * r + 16 + l15)};
  if constexpr (C == 1) {
    filled = early_ok(smem);
    __builtin_amdgcn_s_setprio(2);
#pragma unroll
    for (int pp = 0; pp < 5; ++pp) {
      if (filled) ring_fill(p, smem, w, lane, pp < 2 ? 2 * pp : pp + 2, pp < 2 ? 2 * pp + 2 : pp + 3);
      __builtin_amdgcn_sched_barrier(0);
      mish_pair(rs, smem, g.acc[2 * pp], g.acc[2 * pp + 1], 9 + 2 * pp, 4 + 2 * pp, t0w + 4 + 2 * pp, false, st, rb * 8 + 2 * r, lane);
      __builtin_amdgcn_sched_barrier(0);
    }
    __builtin_amdgcn_s_setprio(0);
    STAMP(4);
    drain();
    if (arrive(smem, 0, 8, lane) && lane == 0) st_flag(p.ws + F_ACT + (rb * 16 + g8) * 8, tag);
    STAMP(5);
  } else {
    __builtin_amdgcn_s_setprio(2);
    mish_pair(rs, smem, g.acc[5], g.acc[6], 5, 0, t0w, g8 == 7, st, rb * 8 + 2 * r, lane);
    mish_pair(rs, smem, g.acc[7], g.acc[8], 7, 2, t0w + 2, g8 == 7, st, rb * 8 + 2 * r, lane);
    __builtin_amdgcn_s_setprio(0);
    STAMP(4);
    if (w == 0 && lane < 30) *(u32x4*)(smem + E_KV + lane * 272 + 256) = u32x4{0u, 0u, 0u, 0u};
    bf16x8 ap[5][3];
    load_aphi(smem, lane, ap);
    bf16x8 ph[2][5];
#pragma unroll
    for (int tt = 0; tt < 2; ++tt) {
      u32x2 kb[5], pos[5], neg[5];
#pragma unroll
      for (int t = 0; t < 5; ++t) pack_k(smem, g.acc[t][tt], t, st[tt], lg, kb[t]);
      if (tt == 0) {
        drain();
        if (arrive(smem, 0, 8, lane) && lane == 0) st_flag(p.ws + F_ACT + (rb * 16 + g8) * 8, tag);
      }
      features(kb, ap, pos, neg);
      ph[tt][0] = mk8(pos[0], pos[1]); ph[tt][1] = mk8(pos[2], pos[3]);
      ph[tt][2] = mk8(neg[0], neg[1]); ph[tt][3] = mk8(neg[2], neg[3]); ph[tt][4] = mk8(pos[4], neg[4]);
#pragma unroll
      for (int k = 0; k < 5; ++k) asm volatile("" : "+v"(ph[tt][k]));
    }
    STAMP(5);
    if (w == 0) {
      need_early(p, smem, tag, lane);
      poll_word(p.ws + OFF_KVH + g8 * 7680 + (lane < 60 ? (lane >> 1) * 256 + (lane & 1) * 8 : 0));
      set_cnt(smem, 4, lane);
    } else wait_cnt(smem, 4, 1);
    {
      const int e0 = w * 120 + lane, e1 = e0 + 64;
      u32x4 a, b;
#pragma unroll 1
      for (int spin = 0; spin < SPIN_MAX; ++spin) {
        asm volatile("" ::: "memory");
        a = ld16(rs, OFF_KVH + g8 * 7680 + e0 * 16);
        b = ld16(rs, lane < 56 ? OFF_KVH + g8 * 7680 + e1 * 16 : OOB);
        if (__all(ready(a) && ready(b))) break;
        __builtin_amdgcn_s_sleep(2);
      }
      *(u32x4*)(smem + E_KV + (e0 >> 4) * 272 + (e0 & 15) * 16) = a;
      if (lane < 56) *(u32x4*)(smem + E_KV + (e1 >> 4) * 272 + (e1 & 15) * 16) = b;
    }
    waitl();
    asm volatile("" ::: "memory");
    (void)arrive(smem, 1, 4, lane);
    wait_cnt(smem, 1, 4);
    STAMP(6);
    const char* ks_ = smem + E_KV + (((l15 >> 2) == lg) ? (lg * 4 + (l15 & 3)) * 16 : 256);
    f32x4 n[2][6];
#pragma unroll
    for (int tn = 0; tn < 6; ++tn) { n[0][tn] = f32x4{0, 0, 0, 0}; n[1][tn] = f32x4{0, 0, 0, 0}; }
    bf16x8 ka[2][6];
#pragma unroll
    for (int tn = 0; tn < 6; ++tn) ka[0][tn] = *(const bf16x8*)(ks_ + (tn * 5) * 272);
#pragma unroll
    for (int ks = 0; ks < 5; ++ks) {
      if (ks < 4) {
#pragma unroll
        for (int tn = 0; tn < 6; ++tn) ka[(ks + 1) & 1][tn] = *(const bf16x8*)(ks_ + (tn * 5 + ks + 1) * 272);
      }
#pragma unroll
      for (int tn = 0; tn < 6; ++tn) {
        n[0][tn] = MFMA16(ka[ks & 1][tn], ph[0][ks], n[0][tn]);
        n[1][tn] = MFMA16(ka[ks & 1][tn], ph[1][ks], n[1][tn]);
      }
    }
#pragma unroll
    for (int tt = 0; tt < 2; ++tt) {
      const f32x2 rc = splat2(__builtin_amdgcn_rcpf(n[tt][5][0] + 1e-6f));
      u32x2 o[5];
#pragma unroll
      for (int t = 0; t < 5; ++t) o[t] = u32x2{pack2v(lo2(n[tt][t]) * rc), pack2v(hi2(n[tt][t]) * rc)};
      const unsigned ob = OFF_O + (rb * 8 + 2 * r + tt) * OST + ((5 * g8) >> 1) * 1024 + lane * 16;
      if ((g8 & 1) == 0) {
        st16(rs, ob, u32x4{o[0][0], o[0][1], o[1][0], o[1][1]});
        st16(rs, ob + 1024, u32x4{o[2][0], o[2][1], o[3][0], o[3][1]});
        st8(rs, ob + 2048, o[4]);
      } else {
        st8(rs, ob + 8, o[0]);
        st16(rs, ob + 1024, u32x4{o[1][0], o[1][1], o[2][0], o[2][1]});
        st16(rs, ob + 2048, u32x4{o[3][0], o[3][1], o[4][0], o[4][1]});
      }
    }
  }
  return tag;
}

#define GLD(dst, ptr, off) asm volatile("global_load_dwordx4 %0, %1, off offset:" #off " sc1" : "=v"(dst) : "v"(ptr) : "memory")
#define WAITV(n) do { asm volatile("s_waitcnt vmcnt(%0)" :: "i"(n) : "memory"); __builtin_amdgcn_sched_barrier(0); } while (0)

#define ISSUE_A(A, s) do { GLD(A[s][0], pa, 0); GLD(A[s][1], pa1, 0); GLD(A[s][2], pa2, 0); GLD(A[s][3], pa3, 0); \
    pa += 1024; pa1 += 1024; pa2 += 1024; pa3 += 1024; } while (0)
#define ISSUE_B(B, s) do { GLD(B[s][0], pb, -2048); GLD(B[s][1], pb, -1024); GLD(B[s][2], pb, 0); GLD(B[s][3], pb, 1024); GLD(B[s][4], pb, 2048); \
    pb += 5120; } while (0)
DI void wait_flags(char* smem, int cnt, bool poller, const char* flags, int n, u64 tag, int lane) {
  if (poller) { polln(flags, n, tag, lane); set_cnt(smem, cnt, lane); }
  else { wait_cnt(smem, cnt, 1); drain(); }
}
DI void stream_o(const char* pa, const char* pb, const char* pw, char* smem, bool poller, int lane, int rot, f32x4 (&acc)[5][4] STAMP_ARG) {
  u32x4 A[5][4], B[4][5];
#pragma unroll
  for (int s = 0; s < 4; ++s) ISSUE_B(B, s);
  if (poller) { poll_word(pw); set_cnt(smem, 7, lane); }
  else wait_cnt(smem, 7, 1);
#pragma unroll 1
  for (int spin = 0; spin < (SPIN_MAX >> 2); ++spin) {
    const char* q0 = pa + 2048 + rot * OST; const char* q1 = pa + 2048 + ((rot + 1) & 3) * OST;
    const char* q2 = pa + 2048 + ((rot + 2) & 3) * OST; const char* q3 = pa + 2048 + ((rot + 3) & 3) * OST;
#define LD5(t, q) GLD(A[0][t], q, -2048); GLD(A[1][t], q, -1024); GLD(A[2][t], q, 0); GLD(A[3][t], q, 1024); GLD(A[4][t], q, 2048);
    LD5(0, q0) LD5(1, q1) LD5(2, q2) LD5(3, q3)
    WAITV(0);
    unsigned mx = 0;
#pragma unroll
    for (int s = 0; s < 5; ++s)
#pragma unroll
      for (int t = 0; t < 4; ++t) {
        asm volatile("" : "+v"(A[s][t]));
        mx = max(max(mx, A[s][t][0]), max(A[s][t][1], max(A[s][t][2], A[s][t][3])));
      }
    if (__all(mx != ~0u)) break;
    __builtin_amdgcn_s_sleep(4);
  }
  STAMP(8);
#define OSTEP(sa, sb) \
  _Pragma("unroll") for (int i = 0; i < 5; ++i) asm volatile("" : "+v"(B[sb][i])); \
  _Pragma("unroll") for (int n = 0; n < 5; ++n) _Pragma("unroll") for (int t = 0; t < 4; ++t) \
    acc[n][t] = MFMA16(__builtin_bit_cast(bf16x8, B[sb][n]), __builtin_bit_cast(bf16x8, A[sa][t]), acc[n][t]); \
  __builtin_amdgcn_sched_barrier(0);
  OSTEP(0, 0)
  ISSUE_B(B, 0);
  OSTEP(1, 1) OSTEP(2, 2) OSTEP(3, 3)
  WAITV(0);
  OSTEP(4, 0)
}

template <int NST, int NA, int NB, int S>
DI void lds_steps(const char*& pa, const char*& pa1, const char*& pa2, const char*& pa3, const char*& pb, const char* ring, unsigned ringa,
                  u32x4 (&A)[NA][4], f32x4 (&acc)[5][4]) {
  if constexpr (S < NST) {
    constexpr int ja = S - NA, jb = S - NB;
    auto cnt = [](int j0, bool skipA) constexpr {
      int n = 0;
      for (int j = j0; j < S; ++j) {
        if (j + NA < NST && !(skipA && j == j0)) n += 4;
        if (j + NB < NST) n += 5;
      }
      return n;
    };
    constexpr int ya = ja < 0 ? 4 * ((NA < NST ? NA : NST) - 1 - S) + cnt(0, false) : cnt(ja, true);
    constexpr int yb = jb < 0 ? 1000 : cnt(jb + 1, false);
    constexpr int y = ya < yb ? ya : yb;
    WAITV(y);
    bf16x8 bf[5];
#pragma unroll
    for (int n = 0; n < 5; ++n) bf[n] = *(const bf16x8*)(ring + (S % NB) * 5120 + n * 1024);
#pragma unroll
    for (int i = 0; i < 4; ++i) asm volatile("" : "+v"(A[S % NA][i]));
#define LSTEP(t, ptr) \
    _Pragma("unroll") for (int n = 0; n < 5; ++n) acc[n][t] = MFMA16(bf[n], __builtin_bit_cast(bf16x8, A[S % NA][t]), acc[n][t]); \
    __builtin_amdgcn_sched_barrier(0); \
    if constexpr (S + NA < NST) { GLD(A[S % NA][t], ptr, 0); ptr += 1024; } \
    __builtin_amdgcn_sched_barrier(0);
    LSTEP(0, pa) LSTEP(1, pa1) LSTEP(2, pa2) LSTEP(3, pa3)
    if constexpr (S + NB < NST) { glds5_sc1(pb, ringa + (S % NB) * 5120); pb += 5120; }
    lds_steps<NST, NA, NB, S + 1>(pa, pa1, pa2, pa3, pb, ring, ringa, A, acc);
  }
}
template <int NST, int NA, int NB>
DI void stream_lds(const char* pa, unsigned astr, const char* pb, char* smem, int roff, int cnt, bool poller, const char* flags, int nflags,
                   u64 tag, int lane, int rot, f32x4 (&acc)[5][4] STAMP_ARG) {
  const char* pa1 = pa + ((rot + 1) & 3) * astr; const char* pa2 = pa + ((rot + 2) & 3) * astr; const char* pa3 = pa + ((rot + 3) & 3) * astr;
  pa += rot * astr;
  const unsigned ringa = lds_addr(smem) + roff;
  u32x4 A[NA][4];
  pb += NB * 5120;
  if (poller) { polln(flags, nflags, tag, lane); set_cnt(smem, cnt, lane); }
  else wait_cnt(smem, cnt, 1);
  STAMP(8);
#pragma unroll
  for (int s = 0; s < NA; ++s) ISSUE_A(A, s);
  lds_steps<NST, NA, NB, 0>(pa, pa1, pa2, pa3, pb, smem + roff + lane * 16, ringa, A, acc);
}

template <int C>
DI void finish(const P1& p, char* smem, int rt, int ct, int w, int lane, const f32x4 (&acc)[5][4] STAMP_ARG) {
  constexpr int N0 = C == 0 ? 0 : 3, NN = C == 0 ? 3 : 2;
  const int lg = lane >> 4, l15 = lane & 15;
  const unsigned o0 = (unsigned)((rt * 64 + (w & 3) * 16 + l15) * 640 + ct * 80 + N0 * 16 + 4 * lg);
  u32x2 xv[NN];
#pragma unroll
  for (int m = 0; m < NN; ++m) xv[m] = *(const u32x2*)(p.x + o0 + 16 * m);
  char* dst = smem + slot_of(w) + lane * 16;
#pragma unroll
  for (int i = 1; i < 4; ++i)
#pragma unroll
    for (int n = 0; n < 5; ++n) *(f32x4*)(dst + ((i - 1) * 5 + n) * 1024) = acc[n][i];
#pragma unroll
  for (int m = 0; m < 5 - NN; ++m) *(f32x4*)(dst + (15 + m) * 1024) = acc[(C == 0 ? 3 : 0) + m][0];
  __syncthreads();
  STAMP(10);
  const char* same[3]; const char* other[3];
#pragma unroll
  for (int j = 1; j < 4; ++j) {
    const int v = (w - j) & 3;
    same[j - 1] = smem + slot_of(4 * C + v) + ((j - 1) * 5 + N0) * 1024 + lane * 16;
    other[j - 1] = smem + slot_of(4 * (1 - C) + v) + ((j - 1) * 5 + N0) * 1024 + lane * 16;
  }
  const char* partner = smem + slot_of(w ^ 4) + 15 * 1024 + lane * 16;
#pragma unroll
  for (int m = 0; m < NN; ++m) {
    f32x4 mine = (acc[N0 + m][0] + *(const f32x4*)(same[0] + m * 1024)) + (*(const f32x4*)(same[1] + m * 1024) + *(const f32x4*)(same[2] + m * 1024));
    f32x4 theirs = (*(const f32x4*)(partner + m * 1024) + *(const f32x4*)(other[0] + m * 1024)) +
                   (*(const f32x4*)(other[1] + m * 1024) + *(const f32x4*)(other[2] + m * 1024));
    const f32x4 ssum = C == 0 ? mine : theirs, bsum = C == 0 ? theirs : mine;
    float xs[4] = {bf_lo(xv[m][0]), bf_hi(xv[m][0]), bf_lo(xv[m][1]), bf_hi(xv[m][1])};
    float r[4];
#pragma unroll
    for (int j = 0; j < 4; ++j) {
      float bb = bsum[j];
      float g = bb * __builtin_amdgcn_rcpf(1.0f + __builtin_amdgcn_exp2f(-bb * LOG2E));
      r[j] = fmaf(ssum[j], g, xs[j]);
    }
    *(u32x2*)(p.out + o0 + 16 * m) = u32x2{pack2(r[0], r[1]), pack2(r[2], r[3])};
  }
}

__global__ __launch_bounds__(512, 2) void k_fused(P1 p) {
  extern __shared__ __attribute__((aligned(16))) char smem[];
  const int b = blockIdx.x, xcd = b & 7, idx = b >> 3;
  const int w = __builtin_amdgcn_readfirstlane(threadIdx.x >> 6), lane = threadIdx.x & 63;
  const int rb = (xcd >> 2) * 8 + (idx >> 2);
  const int jl = idx & 3, xc = xcd & 3;
  const int type = jl >> 1, g8 = 2 * xc + (jl & 1);
  const int c = w >> 2;
  STAMP_DECL; STAMP(0);
  const rsrc_t rs = __builtin_amdgcn_make_buffer_rsrc(p.ws, 0, WS_USED, 0x00020000);
  if (w == 7) {
    u64 pc;
    asm volatile("s_getpc_b64 %0" : "=s"(pc));
    touch((const char*)(pc & ~127ull) + (idx * 20 + (lane < 20 ? lane : 0)) * 128, lds_addr(smem) + L_DUMMY);
  }
  u64 tag;
  bool filled = false;
  if (type == 0) {
    if (c == 0) tag = body_a<0>(p, rs, smem, rb, g8, w, lane, filled STAMP_PASS);
    else tag = body_a<1>(p, rs, smem, rb, g8, w, lane, filled STAMP_PASS);
  } else {
    if (c == 0) tag = body_b<0>(p, rs, smem, rb, g8, w, lane, filled STAMP_PASS);
    else tag = body_b<1>(p, rs, smem, rb, g8, w, lane, filled STAMP_PASS);
  }
  STAMP(7);
  if (w == 5) {
    unsigned v = __hip_atomic_load((LDS3 unsigned*)(smem + L_CNT + 12), __ATOMIC_RELAXED, __HIP_MEMORY_SCOPE_WORKGROUP);
    if (__builtin_amdgcn_readfirstlane(v) == 0) {
      poll256(p.ws + F_EARLY, tag, lane);
      if (blockIdx.x == 0 && lane == 0) st_flag(p.ws + OFF_FLG, tag);
      set_cnt(smem, 3, lane);
    }
  } else wait_cnt(smem, 3, 1);
  const int rt = DMAP == 0 ? (xcd >> 1) * 8 + (idx >> 2) : xcd * 4 + (idx >> 3);
  const int ct = DMAP == 0 ? (xcd & 1) * 4 + (idx & 3) : (idx & 7);
  f32x4 acc[5][4];
#pragma unroll
  for (int n = 0; n < 5; ++n)
#pragma unroll
    for (int t = 0; t < 4; ++t) acc[n][t] = f32x4{0, 0, 0, 0};
  if (w >= 4) {
    const int u0 = (w - 4) * 14 - (w == 7 ? 1 : 0);
    const char* pa = p.ws + OFF_ACT + (rt * 4) * AST + u0 * 1024 + lane * 16;
    const char* pb = p.ws + OFF_W2F + (ct * 74 + u0) * 5120 + lane * 16;
    const char* fact = p.ws + F_ACT + (rt >> 1) * 128;
    bool poller = w == 4;
    if (!filled) {
      if (poller) { polln(fact, 8, tag, lane); set_cnt(smem, 5, lane); poller = false; }
      ring_fill(p, smem, w, lane, 0, 7);
    }
    if (w < 6) stream_lds<14, NA_ACT, 7>(pa, AST, pb, smem, slot_of(w), 5, poller, fact, 8, tag, lane, w & 3, acc STAMP_PASS);
    else stream_lds<13, NA_ACT, 7>(pa, AST, pb, smem, slot_of(w), 5, false, fact, 8, tag, lane, w & 3, acc STAMP_PASS);
    (void)arrive(smem, 8, 4, lane);
  } else {
    stream_o(p.ws + OFF_O + (rt * 4) * OST + 5 * w * 1024 + lane * 16,
             p.ws + OFF_W2F + (ct * 74 + 54 + 5 * w) * 5120 + lane * 16 + 2048,
             p.ws + OFF_O + (rt * 4 + 1 + 2 * ((lane >> 3) & 1)) * OST + (((5 * (lane & 7)) >> 1) + 2) * 1024 + 1008 - ((lane & 1) ? 0 : 1024) , smem, w == 0, lane, w & 3, acc STAMP_PASS);
    wait_cnt(smem, 8, 4);
  }
  STAMP(9);
  if (w >= 4) finish<1>(p, smem, rt, ct, w, lane, acc STAMP_PASS);
  else finish<0>(p, smem, rt, ct, w, lane, acc STAMP_PASS);
  STAMP(11);
  STAMP_FLUSH(p.ws, blockIdx.x * 8 + w);
}
}

extern "C" void kernel_launch(void* const* d_in, const int* in_sizes, int n_in, void* d_out, int out_size, void* d_ws,
                              size_t ws_size, hipStream_t stream) {
  using namespace hk;
  P1 p;
  p.x = (const u16*)d_in[0]; p.wqkv = (const u16*)d_in[1]; p.wphi = (const u16*)d_in[2]; p.wo = (const u16*)d_in[3];
  p.w1 = (const u16*)d_in[5]; p.b1 = (const u16*)d_in[6]; p.w2 = (const u16*)d_in[7]; p.ws = (char*)d_ws; p.out = (u16*)d_out;
  (void)hipFuncSetAttribute((const void*)k_fused, hipFuncAttributeMaxDynamicSharedMemorySize, L_END);
  k_fused<<<256, 512, L_END, stream>>>(p);
}
```

```cpp
#include <hip/hip_runtime.h>
#include <hip/hip_bf16.h>

typedef unsigned short u16;
typedef __bf16 bf16x2 __attribute__((ext_vector_type(2)));
typedef __bf16 bf16x4 __attribute__((ext_vector_type(4)));
typedef __bf16 bf16x8 __attribute__((ext_vector_type(8)));
typedef float f32x2 __attribute__((ext_vector_type(2)));
typedef float f32x4 __attribute__((ext_vector_type(4)));
typedef unsigned u32x2 __attribute__((ext_vector_type(2)));
typedef unsigned u32x4 __attribute__((ext_vector_type(4)));
#define LDS3 __attribute__((address_space(3)))
#define DI __device__ __forceinline__
#define MFMA16(a, b, c) __builtin_amdgcn_mfma_f32_16x16x32_bf16((a), (b), (c), 0, 0, 0)

namespace hk {
typedef unsigned long long u64;
constexpr float LOG2E = 1.4426950408889634f;
constexpr float LN2 = 0.6931471805599453f;
#define ROTP 2
#define NA_ACT 3
#define DMAP 0
constexpr int FLAG_I = ROTP + 2 > 8 ? 8 : ROTP + 2;
#define APAD 256
constexpr unsigned AST = 54 * 1024 + APAD, OST = 20 * 1024 + APAD;
constexpr unsigned OFF_ACT = 0;
constexpr unsigned OFF_O = OFF_ACT + 128 * AST;
constexpr unsigned OFF_W2F = OFF_O + 128 * OST;
constexpr unsigned OFF_KVP = OFF_W2F + 8 * 74 * 5 * 1024;
constexpr unsigned OFF_KVH = OFF_KVP + 8 * 16 * 60 * 256;
constexpr unsigned OFF_STP = OFF_KVH + 8 * 30 * 256;
constexpr unsigned OFF_CSP = OFF_STP + 16 * 12800;
constexpr unsigned OFF_FLG = OFF_CSP + 16 * 12800;
constexpr unsigned F_EARLY = OFF_FLG + 128;
constexpr unsigned F_ACT = F_EARLY + 2048;
constexpr unsigned F_KVP = F_ACT + 2048;
constexpr unsigned F_KVH = F_KVP + 1024;
constexpr unsigned F_O = F_KVH + 1024;
constexpr unsigned F_ST = F_O + 2048;
constexpr unsigned F_CS = F_ST + 2048;
constexpr unsigned WS_USED = F_CS + 2048;
constexpr unsigned OOB = 0x7ffff000u;
constexpr int XB = 16384, WB0 = 3 * XB;
constexpr int L_PT = 118784;
constexpr int L_DUMMY = 151552;
constexpr int L_RAW = 155648;
constexpr int L_BIAS = L_RAW + 3200;
constexpr int L_ZERO = L_BIAS + 448;
constexpr int L_CNT = L_ZERO + 64;
constexpr int L_BIASL = L_CNT + 64;
constexpr int L_CS = L_BIASL + 896;
constexpr int L_SUM = L_CS + 1216;
constexpr int L_END = 163840;
static_assert(L_SUM + 1024 <= L_END - 256, "lds");
constexpr int E_PHI = 0;
constexpr int E_V = 40960;
constexpr int E_KB = 61440;
constexpr int E_KV = 0;

DI unsigned pack2(float a, float b) {
  f32x2 v = {a, b};
  return __builtin_bit_cast(unsigned, __builtin_convertvector(v, bf16x2));
}
DI unsigned pack2v(f32x2 v) { return __builtin_bit_cast(unsigned, __builtin_convertvector(v, bf16x2)); }
DI bf16x2 as_bf2(unsigned u) { return __builtin_bit_cast(bf16x2, u); }
DI float bf_lo(unsigned u) { return __uint_as_float(u << 16); }
DI float bf_hi(unsigned u) { return __uint_as_float(u & 0xffff0000u); }
DI float rscatter_add(float m0, float m1, float m2, float m3) {
  auto a = __builtin_amdgcn_permlane16_swap(__float_as_uint(m0), __float_as_uint(m1), false, false);
  auto b = __builtin_amdgcn_permlane16_swap(__float_as_uint(m2), __float_as_uint(m3), false, false);
  float A = __uint_as_float(a[0]) + __uint_as_float(a[1]);
  float B = __uint_as_float(b[0]) + __uint_as_float(b[1]);
  auto c = __builtin_amdgcn_permlane32_swap(__float_as_uint(A), __float_as_uint(B), false, false);
  return __uint_as_float(c[0]) + __uint_as_float(c[1]);
}
DI f32x2 lo2(f32x4 v) { return __builtin_shufflevector(v, v, 0, 1); }
DI f32x2 hi2(f32x4 v) { return __builtin_shufflevector(v, v, 2, 3); }
DI f32x2 fma2(f32x2 a, f32x2 b, f32x2 c) { return __builtin_elementwise_fma(a, b, c); }
DI f32x2 splat2(float a) { return f32x2{a, a}; }
DI f32x2 exp2v(f32x2 a) { return f32x2{__builtin_amdgcn_exp2f(a[0]), __builtin_amdgcn_exp2f(a[1])}; }
DI f32x2 rcpv(f32x2 a) { return f32x2{__builtin_amdgcn_rcpf(a[0]), __builtin_amdgcn_rcpf(a[1])}; }
DI void dual_softmax(const f32x4 (&p)[5], u32x2 (&pos)[5], u32x2 (&neg)[5]) {
  float mx = fmaxf(p[0][0], p[0][1]), mn = fminf(p[0][0], p[0][1]);
  mx = fmaxf(fmaxf(mx, p[0][2]), p[0][3]); mn = fminf(fminf(mn, p[0][2]), p[0][3]);
#pragma unroll
  for (int t = 1; t < 5; ++t) {
    mx = fmaxf(fmaxf(mx, p[t][0]), p[t][1]); mx = fmaxf(fmaxf(mx, p[t][2]), p[t][3]);
    mn = fminf(fminf(mn, p[t][0]), p[t][1]); mn = fminf(fminf(mn, p[t][2]), p[t][3]);
  }
  const f32x2 L = splat2(LOG2E), nL = splat2(-LOG2E), cx = splat2(-mx * LOG2E), cn = splat2(mn * LOG2E);
  f32x2 ep[10], en[10];
#pragma unroll
  for (int t = 0; t < 5; ++t) {
    ep[2 * t] = exp2v(fma2(lo2(p[t]), L, cx)); ep[2 * t + 1] = exp2v(fma2(hi2(p[t]), L, cx));
    en[2 * t] = exp2v(fma2(lo2(p[t]), nL, cn)); en[2 * t + 1] = exp2v(fma2(hi2(p[t]), nL, cn));
  }
  f32x2 sp = ep[0], sn = en[0];
#pragma unroll
  for (int i = 1; i < 10; ++i) { sp += ep[i]; sn += en[i]; }
  const f32x2 ip = splat2(__builtin_amdgcn_rcpf(sp[0] + sp[1])), in_ = splat2(__builtin_amdgcn_rcpf(sn[0] + sn[1]));
#pragma unroll
  for (int t = 0; t < 5; ++t) {
    pos[t] = u32x2{pack2v(ep[2 * t] * ip), pack2v(ep[2 * t + 1] * ip)};
    neg[t] = u32x2{pack2v(en[2 * t] * in_), pack2v(en[2 * t + 1] * in_)};
  }
}
DI f32x2 mish2(f32x2 hl) {
  f32x2 e = exp2v(hl);
  f32x2 d = fma2(e, e + splat2(2.0f), splat2(2.0f));
  f32x2 t = fma2(rcpv(d), splat2(-2.0f * LN2), splat2(LN2));
  return hl * t;
}
DI bf16x8 mk8(u32x2 a, u32x2 b) {
  u32x4 v = {a[0], a[1], b[0], b[1]};
  return __builtin_bit_cast(bf16x8, v);
}
DI bf16x8 tr2(const char* a, const char* b) {
  bf16x4 lo = __builtin_amdgcn_ds_read_tr16_b64_v4bf16((LDS3 bf16x4*)(a));
  bf16x4 hi = __builtin_amdgcn_ds_read_tr16_b64_v4bf16((LDS3 bf16x4*)(b));
  return __builtin_shufflevector(lo, hi, 0, 1, 2, 3, 4, 5, 6, 7);
}

struct P1 {
  const u16* x; const u16* wqkv; const u16* wphi; const u16* wo; const u16* w1; const u16* b1; const u16* w2;
  char* ws; u16* out;
};
typedef __amdgpu_buffer_rsrc_t rsrc_t;

DI unsigned lds_addr(const void* p) { return (unsigned)(size_t)(LDS3 const char*)p; }
DI void glds1(const void* g, unsigned lds) {
  unsigned keep;
  asm volatile("s_mov_b32 %0, m0\n\ts_mov_b32 m0, %2\n\ts_nop 0\n\tglobal_load_lds_dwordx4 %1, off\n\ts_mov_b32 m0, %0"
               : "=&s"(keep) : "v"(g), "s"(lds) : "memory");
}
DI void glds5_sc1(const void* g, unsigned lds) {
  unsigned keep;
  asm volatile("s_mov_b32 %0, m0\n\t"
               "s_mov_b32 m0, %2\n\ts_nop 0\n\tglobal_load_lds_dwordx4 %1, off sc1\n\t"
               "global_load_lds_dwordx4 %1, off offset:1024 sc1\n\t"
               "global_load_lds_dwordx4 %1, off offset:2048 sc1\n\t"
               "global_load_lds_dwordx4 %1, off offset:3072 sc1\n\t"
               "s_add_u32 m0, %2, 0x1000\n\ts_nop 0\n\tglobal_load_lds_dwordx4 %3, off sc1\n\t"
               "s_mov_b32 m0, %0"
               : "=&s"(keep) : "v"(g), "s"(lds), "v"((const char*)g + 4096) : "memory", "scc");
}
DI void touch(const void* g, unsigned lds) {
  unsigned keep;
  asm volatile("s_mov_b32 %0, m0\n\ts_mov_b32 m0, %2\n\ts_nop 0\n\tglobal_load_lds_dword %1, off sc1\n\ts_mov_b32 m0, %0"
               : "=&s"(keep) : "v"(g), "s"(lds) : "memory");
}
DI u64 next_tag(u64 e) { return e * 0x9E3779B97F4A7C15ull + 0x632BE59BD9B4E019ull; }
DI void st_flag(char* p, u64 v) { __hip_atomic_store((u64*)p, v, __ATOMIC_RELAXED, __HIP_MEMORY_SCOPE_AGENT); }
DI void st16(rsrc_t rs, unsigned off, u32x4 v) { __builtin_amdgcn_raw_buffer_store_b128(v, rs, off, 0, 16); }
DI void st8(rsrc_t rs, unsigned off, u32x2 v) { __builtin_amdgcn_raw_buffer_store_b64(v, rs, off, 0, 16); }
DI void st4(rsrc_t rs, unsigned off, float v) { __builtin_amdgcn_raw_buffer_store_b32(__float_as_uint(v), rs, off, 0, 16); }
DI u32x4 ld16(rsrc_t rs, unsigned off) { return __builtin_amdgcn_raw_buffer_load_b128(rs, off, 0, 16); }
DI bool ready(u32x4 v) { return v[0] != 0xffffffffu && v[1] != 0xffffffffu && v[2] != 0xffffffffu && v[3] != 0xffffffffu; }
DI void drain() { asm volatile("s_waitcnt vmcnt(0)" ::: "memory"); }
constexpr int SPIN_MAX = 1 << 18;
DI bool flags_ok(u32x4 a, u32x4 b, u64 tag) {
  const unsigned tl = (unsigned)tag, th = (unsigned)(tag >> 32);
  return a[0] == tl && a[1] == th && a[2] == tl && a[3] == th && b[0] == tl && b[1] == th && b[2] == tl && b[3] == th;
}
DI void poll256(const char* flags, u64 tag, int lane) {
  const char* q = flags + lane * 32;
#pragma unroll 1
  for (int spin = 0; spin < SPIN_MAX; ++spin) {
    u32x4 a, b;
    asm volatile("global_load_dwordx4 %0, %2, off sc1\n\tglobal_load_dwordx4 %1, %2, off offset:16 sc1\n\ts_waitcnt vmcnt(0)"
                 : "=&v"(a), "=&v"(b) : "v"(q) : "memory");
    if (__all(flags_ok(a, b, tag))) break;
    __builtin_amdgcn_s_sleep(8);
  }
}
DI void pollq(const char* q, u64 tag) {
#pragma unroll 1
  for (int spin = 0; spin < SPIN_MAX; ++spin) {
    u32x2 a;
    asm volatile("global_load_dwordx2 %0, %1, off sc1\n\ts_waitcnt vmcnt(0)" : "=&v"(a) : "v"(q) : "memory");
    if (__all(a[0] == (unsigned)tag && a[1] == (unsigned)(tag >> 32))) break;
    __builtin_amdgcn_s_sleep(3);
  }
}
DI void polln(const char* flags, int n, u64 tag, int lane) { pollq(flags + (lane < n ? lane : 0) * 8, tag); }
DI bool arrive(char* smem, int idx, int n, int lane) {
  unsigned old = 0;
  if (lane == 0) old = __hip_atomic_fetch_add((LDS3 unsigned*)(smem + L_CNT + idx * 4), 1u, __ATOMIC_RELAXED, __HIP_MEMORY_SCOPE_WORKGROUP);
  return __builtin_amdgcn_readfirstlane(old) == (unsigned)(n - 1);
}
DI void set_cnt(char* smem, int idx, int lane) {
  if (lane == 0) __hip_atomic_store((LDS3 unsigned*)(smem + L_CNT + idx * 4), 1u, __ATOMIC_RELAXED, __HIP_MEMORY_SCOPE_WORKGROUP);
}
DI void wait_cnt(char* smem, int idx, unsigned n) {
#pragma unroll 1
  for (int spin = 0; spin < (SPIN_MAX << 4); ++spin) {
    unsigned v = __hip_atomic_load((LDS3 unsigned*)(smem + L_CNT + idx * 4), __ATOMIC_RELAXED, __HIP_MEMORY_SCOPE_WORKGROUP);
    if (__builtin_amdgcn_readfirstlane(v) >= n) break;
    __builtin_amdgcn_s_sleep(1);
  }
  asm volatile("" ::: "memory");
}

template <int NT> struct Frags { bf16x8 x[2]; bf16x8 w[NT]; };

template <int N> DI void waitv() { asm volatile("s_waitcnt vmcnt(%0)" :: "i"(N) : "memory"); }
DI void waitl() { __builtin_amdgcn_s_waitcnt(0xC07F); }

template <int TYPE, int C>
struct Gemm {
  static constexpr int NT = TYPE == 0 ? 5 : (C == 0 ? 9 : 10);
  static constexpr int NU = TYPE == 0 ? 11 : 19;
  static constexpr int WROW = NU * 32, NC = NU * 2, HB = 32 * WROW;
  static constexpr int NWH = TYPE == 0 ? 6 : 5;
  static constexpr int NWS = (NU + 7) / 8;
  static constexpr int NFULL = NU - (NWS - 1) * 8;
  static constexpr int MAGIC = (65536 + NC - 1) / NC;
  static constexpr bool VW = (TYPE == 0 && C == 1);
  static constexpr int unit_of(int t) { return TYPE == 0 ? t + 5 * C : t + 9 * C; }

  const char* xsrc[2];
  const char* wsrc[NWS];
  unsigned winc[NWS];
  unsigned xa0, wa, lds0;
  int cx, hw, hslot, w;
  bool full;
  char* smem;
  f32x4 acc[NT][2];

  DI void init(const P1& p, char* smem_, int rb, int g8, int t0w, int rot, int w_, int lane) {
    smem = smem_; w = w_; cx = rot; hw = 2 * rot; hslot = 0; lds0 = lds_addr(smem_); full = w < NFULL;
    const int r = w & 3, lg = lane >> 4, l15 = lane & 15;
#pragma unroll
    for (int i = 0; i < 2; ++i) {
      int row = (i * 8 + w) * 8 + (lane >> 3);
      int ch = (lane & 7) ^ ((row >> 1) & 7);
      xsrc[i] = (const char*)p.x + (unsigned)((rb * 128 + row) * 1280 + ch * 16 + rot * 128);
    }
#pragma unroll
    for (int i = 0; i < NWS; ++i) {
      int ci = (i * 8 + w) * 64 + lane;
      ci = ci < 32 * NC ? ci : lane;
      int pos = (ci * MAGIC) >> 16, c16 = ci - pos * NC;
      int kk = pos & 15;
      int k = (kk >= 4 && kk < 12) ? (pos ^ 12) : pos;
      bool isq = TYPE == 0 || c16 < 10;
      int colb;
      if (TYPE == 0) { int c = min(c16, 19); colb = 1280 + 160 * g8 + c * 16 + (c >= 10 ? 1120 : 0); }
      else colb = isq ? (160 * g8 + c16 * 16) : (32 * t0w + (c16 - 10) * 16);
      unsigned stride = isq ? 3840u : 3456u;
      wsrc[i] = (isq ? (const char*)p.wqkv : (const char*)p.w1) + (unsigned)((k + rot * 64) * stride + colb);
      winc[i] = stride * 32;
    }
    xa0 = (32 * r + l15) * 128 + ((lg ^ ((l15 >> 1) & 7)) * 16);
    wa = WB0 + (16 * (lg >> 1) + 4 * (lg & 1) + (l15 >> 2)) * WROW + (VW ? 160 + (l15 & 3) * 40 : (l15 & 3) * 8);
#pragma unroll
    for (int t = 0; t < NT; ++t) { acc[t][0] = f32x4{0, 0, 0, 0}; acc[t][1] = f32x4{0, 0, 0, 0}; }
  }
  DI void issue_x1(int s, int xslot) {
    glds1(xsrc[s], lds0 + xslot * XB + w * 1024 + s * 0x2000);
    xsrc[s] += 128;
    if (s == 1 && ++cx == 10) { cx = 0; xsrc[0] -= 1280; xsrc[1] -= 1280; }
  }
  DI void issue_w1(int s) {
    if (s < NWS - 1 || full) { glds1(wsrc[s], lds0 + WB0 + hslot * HB + w * 1024 + s * 0x2000); wsrc[s] += winc[s]; }
    if (s == NWS - 1) {
      hslot = hslot == NWH - 1 ? 0 : hslot + 1;
      if (++hw == 20) {
        hw = 0;
#pragma unroll
        for (int i = 0; i < NWS; ++i) wsrc[i] -= 20 * winc[i];
      }
    }
  }
  DI void issue_x(int xslot) { issue_x1(0, xslot); issue_x1(1, xslot); }
  DI void issue_w() {
#pragma unroll
    for (int s = 0; s < NWS; ++s) issue_w1(s);
  }
  template <int NX, int NH, int E> DI void wait() {
    if (full) waitv<2 * NX + NH * NWS + E>(); else waitv<2 * NX + NH * (NWS - 1) + E>();
  }
  DI void rd_x(Frags<NT>& f, int xslot, int ks, int tt) { f.x[tt] = *(const bf16x8*)(smem + xslot * XB + (xa0 ^ (ks * 64)) + tt * 2048); }
  DI void rd_w(Frags<NT>& f, int hs, int t) {
    const char* q = smem + hs * HB + wa + (VW ? t * 8 : unit_of(t) * 32);
    f.w[t] = tr2(q, q + 8 * WROW);
  }
  DI void kstep(const Frags<NT>& f, Frags<NT>& g, int nxs, int nks, int nhs, bool do_rd, int nreq, int ixs) {
    constexpr int NS = 2 + 2 * NWS;
#pragma unroll
    for (int t = 0; t < NT; ++t) {
      if (do_rd) {
        if (t < 2) rd_x(g, nxs, nks, t);
        rd_w(g, nhs, t);
      }
#pragma unroll
      for (int tt = 0; tt < 2; ++tt)
        acc[t][tt] = VW ? MFMA16(f.x[tt], f.w[t], acc[t][tt]) : MFMA16(f.w[t], f.x[tt], acc[t][tt]);
#pragma unroll
      for (int s = (t * NS) / NT; s < ((t + 1) * NS) / NT; ++s) {
        if (s < NWS) { if (nreq > 0) issue_w1(s); }
        else if (s < NWS + 2) { if (nreq > 1) issue_x1(s - NWS, ixs); }
        else { if (nreq > 1) issue_w1(s - NWS - 2); }
      }
      __builtin_amdgcn_sched_barrier(0);
    }
  }
};

DI void dots2(float& c0, float& c1, const bf16x8& wa_, const bf16x8& wb_) {
  u32x4 a = __builtin_bit_cast(u32x4, wa_), b = __builtin_bit_cast(u32x4, wb_);
  unsigned a0 = a[0], a1 = a[1], a2 = a[2], a3 = a[3], b0 = b[0], b1 = b[1], b2 = b[2], b3 = b[3];
  asm volatile("s_nop 1\n\t"
               "v_dot2c_f32_bf16 %0, 0x3f803f80, %2\n\tv_dot2c_f32_bf16 %1, 0x3f803f80, %6\n\t"
               "v_dot2c_f32_bf16 %0, 0x3f803f80, %3\n\tv_dot2c_f32_bf16 %1, 0x3f803f80, %7\n\t"
               "v_dot2c_f32_bf16 %0, 0x3f803f80, %4\n\tv_dot2c_f32_bf16 %1, 0x3f803f80, %8\n\t"
               "v_dot2c_f32_bf16 %0, 0x3f803f80, %5\n\tv_dot2c_f32_bf16 %1, 0x3f803f80, %9\n\ts_nop 3"
               : "+v"(c0), "+v"(c1) : "v"(a0), "v"(a1), "v"(a2), "v"(a3), "v"(b0), "v"(b1), "v"(b2), "v"(b3));
}
DI void dots_x(float& s1, float& s2, const bf16x8& x) {
  u32x4 a = __builtin_bit_cast(u32x4, x);
  unsigned a0 = a[0], a1 = a[1], a2 = a[2], a3 = a[3];
  asm volatile("s_nop 1\n\t"
               "v_dot2c_f32_bf16 %0, 0x3f803f80, %2\n\tv_dot2c_f32_bf16 %1, %2, %2\n\t"
               "v_dot2c_f32_bf16 %0, 0x3f803f80, %3\n\tv_dot2c_f32_bf16 %1, %3, %3\n\t"
               "v_dot2c_f32_bf16 %0, 0x3f803f80, %4\n\tv_dot2c_f32_bf16 %1, %4, %4\n\t"
               "v_dot2c_f32_bf16 %0, 0x3f803f80, %5\n\tv_dot2c_f32_bf16 %1, %5, %5\n\ts_nop 3"
               : "+v"(s1), "+v"(s2) : "v"(a0), "v"(a1), "v"(a2), "v"(a3));
}
DI f32x2 stat_of(const char* smem, int tok) { return *(const f32x2*)(smem + L_SUM + tok * 8); }
DI void mish_pair(rsrc_t rs, const char* smem, const f32x4 (&a0)[2], const f32x4 (&a1)[2], int ucs, int wl, int wt, bool skip,
                  const f32x2 (&st)[2], int tt0, int lane) {
  const int lg = lane >> 4;
  f32x4 bb[2], c4[2];
#pragma unroll
  for (int h = 0; h < 2; ++h) {
    bb[h] = *(const f32x4*)(smem + L_BIASL + ((wl + h) * 16 + 4 * lg) * 4);
    c4[h] = *(const f32x4*)(smem + L_CS + ((ucs + h) * 16 + 4 * lg) * 4);
  }
#pragma unroll
  for (int tt = 0; tt < 2; ++tt) {
    const f32x2 rsL = splat2(st[tt][0] * LOG2E), nbL = splat2(st[tt][1] * LOG2E);
    u32x4 o;
    o[0] = pack2v(mish2(fma2(lo2(a0[tt]), rsL, fma2(nbL, lo2(c4[0]), lo2(bb[0])))));
    o[1] = pack2v(mish2(fma2(hi2(a0[tt]), rsL, fma2(nbL, hi2(c4[0]), hi2(bb[0])))));
    o[2] = pack2v(mish2(fma2(lo2(a1[tt]), rsL, fma2(nbL, lo2(c4[1]), lo2(bb[1])))));
    o[3] = pack2v(mish2(fma2(hi2(a1[tt]), rsL, fma2(nbL, hi2(c4[1]), hi2(bb[1])))));
    st16(rs, skip ? OOB : OFF_ACT + (tt0 + tt) * AST + (wt >> 1) * 1024 + lane * 16, o);
  }
}
DI void load_aphi(const char* smem, int lane, bf16x8 (&ap)[5][3]) {
  const int lg = lane >> 4, l15 = lane & 15;
  const char* pa[6];
#pragma unroll
  for (int i = 0; i < 6; ++i) {
    int d0 = 16 * i + 4 * lg;
    int h = (d0 * 13) >> 8;
    bool ok = (h == (l15 & 3)) && i < 5;
    pa[i] = smem + (ok ? L_RAW + h * 800 + (d0 - 20 * h + (l15 >> 2)) * 40 : L_ZERO);
  }
#pragma unroll
  for (int t = 0; t < 5; ++t)
#pragma unroll
    for (int ks = 0; ks < 3; ++ks) ap[t][ks] = tr2(pa[2 * ks] + t * 8, pa[2 * ks + 1] + t * 8);
}
DI void pack_k(const char* smem, const f32x4& a, int t, f32x2 st, int lg, u32x2& kb) {
  f32x4 c4 = *(const f32x4*)(smem + L_CS + (t * 16 + 4 * lg) * 4);
  const f32x2 rsv = splat2(st[0]), nb = splat2(st[1]);
  kb = u32x2{pack2v(fma2(lo2(a), rsv, nb * lo2(c4))), pack2v(fma2(hi2(a), rsv, nb * hi2(c4)))};
}
DI void features(const u32x2 (&kb)[5], const bf16x8 (&ap)[5][3], u32x2 (&pos)[5], u32x2 (&neg)[5]) {
  bf16x8 b0 = mk8(kb[0], kb[1]), b1 = mk8(kb[2], kb[3]), b2 = mk8(kb[4], u32x2{0u, 0u});
  f32x4 pk[5];
#pragma unroll
  for (int t = 0; t < 5; ++t) pk[t] = MFMA16(ap[t][2], b2, MFMA16(ap[t][1], b1, MFMA16(ap[t][0], b0, (f32x4{0, 0, 0, 0}))));
  dual_softmax(pk, pos, neg);
}
DI void barrier() {
  waitl();
  __builtin_amdgcn_s_barrier();
  __builtin_amdgcn_sched_barrier(0);
}

template <int TYPE, int C, class G>
DI u64 first_pass(G& g, const P1& p, rsrc_t rs, char* smem, int rb, int g8, int t0w, int w, int lane) {
  constexpr int NT = G::NT, NWH = G::NWH;
  const int r = w & 3, lg = lane >> 4, l15 = lane & 15;
  const unsigned lds0 = lds_addr(smem);
  u32x2 ev;
  asm volatile("global_load_dwordx2 %0, %1, off sc1" : "=v"(ev) : "v"(p.ws + OFF_FLG) : "memory");
  const int job = TYPE * 8 + g8;
  const int myc = (rb + job) % 10;
  const int rot = (myc / ROTP) * ROTP;
  const int my_i = myc - rot;
  g.init(p, smem, rb, g8, t0w, rot, w, lane);
  if (C == 1) {
    int ci = r * 64 + lane;
    if (ci < 200) glds1((const char*)p.wphi + g8 * 3200 + ci * 16, lds0 + L_RAW + r * 1024);
  } else if (w == 3) {
    if (TYPE == 1 && lane < 28) glds1((const char*)p.b1 + t0w * 32 + lane * 16, lds0 + L_BIAS);
    if (lane < 32) *(unsigned*)(smem + L_ZERO + lane * 4) = 0u;
  }
  g.issue_w(); g.issue_x(0); g.issue_w();
  __builtin_amdgcn_s_barrier();
  g.issue_w(); g.issue_x(1); g.issue_w();
  g.issue_w(); g.issue_x(2);
  if (NWH == 6) g.issue_w();
  constexpr int AH = NWH - 4;
  const int ptj = w * 128 + rb * 8 + g8;
  const bool has_pt = TYPE == 0 && w < 6 && ptj < 740;
  const int ptu = ptj / 10, ptL = ptj - ptu * 10;
  if (has_pt) {
#pragma unroll
    for (int i = 0; i < 4; ++i) {
      int rho = i * 8 + (lane >> 3);
      const u16* src;
      if (ptu < 54) src = p.w2 + (unsigned)((32 * ptu + rho) * 640);
      else {
        int T = 2 * (ptu - 54) + (rho >> 4), Gq = (T * 13) >> 6;
        src = p.wo + (unsigned)(((4 * Gq + ((rho >> 2) & 3)) * 20 + 4 * (T - 5 * Gq) + (rho & 3)) * 640);
      }
      glds1((const char*)src + ptL * 128 + (lane & 7) * 16, lds0 + L_PT + w * 4096 + i * 1024);
    }
  }
  float cs[3] = {0.f, 0.f, 0.f}, sx[2] = {0.f, 0.f}, cd = 0.f;
  auto do_dots = [&](const Frags<NT>& f) {
    dots_x(sx[0], sx[1], f.x[C]);
    if (r == 0) { dots2(cs[0], cs[1], f.w[0], f.w[4]); if (NT > 8) dots2(cs[2], cd, f.w[NT > 8 ? 8 : 0], f.w[0]); }
    else if (r == 1) { dots2(cs[0], cs[1], f.w[1], f.w[NT > 5 ? 5 : 0]); if (NT > 9) dots2(cs[2], cd, f.w[NT > 9 ? 9 : 0], f.w[0]); }
    else if (r == 2) dots2(cs[0], cs[1], f.w[2], f.w[NT > 6 ? 6 : 0]);
    else dots2(cs[0], cs[1], f.w[3], f.w[NT > 7 ? 7 : 0]);
  };
  Frags<NT> f0, f1;
  if (has_pt) g.template wait<2, NWH - 2, 4>(); else g.template wait<2, NWH - 2, 0>();
  barrier();
  g.rd_x(f0, 0, 0, 0); g.rd_x(f0, 0, 0, 1);
#pragma unroll
  for (int t = 0; t < NT; ++t) g.rd_w(f0, 0, t);
  if (TYPE == 1) {
    int tid = w * 64 + lane;
    if (tid < 224) *(float*)(smem + L_BIASL + tid * 4) = bf_lo(*(const u16*)(smem + L_BIAS + tid * 2)) * LOG2E;
  }
  asm volatile("" : "+v"(ev));
  const u64 tag = next_tag(((u64)(unsigned)__builtin_amdgcn_readfirstlane(ev[1]) << 32) | (unsigned)__builtin_amdgcn_readfirstlane(ev[0]));
  int xs = 0, hs = 0;
  u32x4 fa, fb;
  u32x2 tf;
  unsigned pv[10], pw[10];
  const char* tfp = p.ws + (lane < 10 ? F_ST + (rb * 16 + lane) * 8 : F_CS + (job * 16 + (lane < 20 ? lane - 10 : 0)) * 8);
  auto inc = [](int s) { return s == NWH - 1 ? 0 : s + 1; };
#define LD10(pv, q) \
    asm volatile("global_load_dword %0, %10, off sc1\n\tglobal_load_dword %1, %10, off offset:1280 sc1\n\t" \
                 "global_load_dword %2, %10, off offset:2560 sc1\n\tglobal_load_dword %3, %10, off offset:3840 sc1\n\t" \
                 "global_load_dword %4, %11, off sc1\n\tglobal_load_dword %5, %11, off offset:1280 sc1\n\t" \
                 "global_load_dword %6, %11, off offset:2560 sc1\n\tglobal_load_dword %7, %11, off offset:3840 sc1\n\t" \
                 "global_load_dword %8, %12, off sc1\n\tglobal_load_dword %9, %12, off offset:1280 sc1" \
                 : "=&v"(pv[0]), "=&v"(pv[1]), "=&v"(pv[2]), "=&v"(pv[3]), "=&v"(pv[4]), "=&v"(pv[5]), "=&v"(pv[6]), "=&v"(pv[7]), "=&v"(pv[8]), "=&v"(pv[9]) \
                 : "v"(q), "v"((q) + 5120), "v"((q) + 10240) : "memory")
#pragma unroll 1
  for (int i = 0; i < 8; ++i) {
    waitl();
    __builtin_amdgcn_sched_barrier(0);
    const int h1 = inc(hs), h2 = inc(h1);
    g.kstep(f0, f1, xs, 1, h1, true, 0, 0);
    if (i == my_i) do_dots(f0);
    const int nxs = xs == 2 ? 0 : xs + 1;
    if (TYPE == 0) {
      if (i == 0 && has_pt) g.template wait<1, AH, 4>();
      else if (i == 1) g.template wait<1, AH, 2>();
      else if (i == 2 && has_pt) g.template wait<1, AH, 6>();
      else if (i == 2) g.template wait<1, AH, 2>();
      else if (i == 3 && has_pt) g.template wait<1, AH, 4>();
      else g.template wait<1, AH, 0>();
    } else {
      if (i == 1) g.template wait<1, AH, 3>();
      else g.template wait<1, AH, 0>();
    }
    barrier();
    if (i == FLAG_I && w == 0 && lane < 3)
      st_flag(p.ws + (lane == 0 ? F_EARLY + blockIdx.x * 8 : (lane == 1 ? F_ST + (rb * 16 + job) * 8 : F_CS + (job * 16 + rb) * 8)), tag);
    g.kstep(f1, f0, nxs, 0, h2, true, i < 7 ? 3 : (NWH == 5 ? 1 : 0), xs);
    if (i == 0) {
      const u32x4 sent = {~0u, ~0u, ~0u, ~0u};
      if (TYPE == 0) {
        unsigned kb = OFF_KVP + (g8 * 16 + rb) * 15360 + lane * 16;
        st16(rs, kb + w * 1024, sent);
        st16(rs, w < 7 ? kb + (8 + w) * 1024 : (lane < 30 ? OFF_KVH + g8 * 7680 + rb * 480 + lane * 16 : OOB), sent);
      } else {
        const unsigned ob = OFF_O + (rb * 8 + w) * OST + ((5 * g8) >> 1) * 1024 + lane * 16;
        st16(rs, ob + 1024, sent);
        st16(rs, ob + ((g8 & 1) ? 2048 : 0), sent);
        st8(rs, ob + ((g8 & 1) ? 8 : 2048), u32x2{~0u, ~0u});
      }
    }
    if (i == 1 && has_pt) {
      const char* q = smem + L_PT + w * 4096 + (4 * lg + (l15 >> 2)) * 128 + (l15 & 3) * 8;
#pragma unroll
      for (int n = 0; n < 4; ++n) {
        bf16x8 f = tr2(q + n * 32, q + 2048 + n * 32);
        int N = 4 * ptL + n, ct = (N * 13) >> 6;
        st16(rs, OFF_W2F + ((ct * 74 + ptu) * 5 + (N - 5 * ct)) * 1024 + lane * 16, __builtin_bit_cast(u32x4, f));
      }
    }
    if (i == my_i) {
      do_dots(f1);
      float v = rscatter_add(sx[0], sx[1], cs[0], cs[1]);
      unsigned a = lg < 2 ? OFF_STP + (rb * 10 + myc) * 1280 + lg * 512 + (32 * r + 16 * C + l15) * 4
                          : OFF_CSP + (job * 10 + myc) * 1280 + ((G::unit_of(0) + r + (lg == 2 ? 0 : 4)) * 16 + l15) * 4;
      bool on = lg < 2 ? job < 10 : (rb < 10 && (lg == 2 || r + 4 < NT));
      st4(rs, on ? a : OOB, v);
      if (NT > 8) {
        float v2 = rscatter_add(cs[2], 0.f, 0.f, 0.f);
        st4(rs, (lg == 0 && rb < 10 && r + 8 < NT) ? OFF_CSP + (job * 10 + myc) * 1280 + ((G::unit_of(0) + r + 8) * 16 + l15) * 4 : OOB, v2);
      }
    }
    if (i == 6) {
      asm volatile("global_load_dwordx2 %0, %1, off sc1" : "=v"(tf) : "v"(tfp) : "memory");
      if (w == 5)
        asm volatile("global_load_dwordx4 %0, %2, off sc1\n\tglobal_load_dwordx4 %1, %2, off offset:16 sc1"
                     : "=&v"(fa), "=&v"(fb) : "v"(p.ws + F_EARLY + lane * 32) : "memory");
    }
    xs = nxs; hs = h2;
  }
  {
    waitl();
    __builtin_amdgcn_sched_barrier(0);
    const int h1 = inc(hs), h2 = inc(h1);
    g.kstep(f0, f1, xs, 1, h1, true, 0, 0);
    const int nxs = xs == 2 ? 0 : xs + 1;
    waitv<0>();
    barrier();
    asm volatile("" : "+v"(tf));
    if (!__all(tf[0] == (unsigned)tag && tf[1] == (unsigned)(tag >> 32))) pollq(tfp, tag);
    if (C == 1 && w == 5) {
      asm volatile("" : "+v"(fa), "+v"(fb));
      if (__all(flags_ok(fa, fb, tag))) {
        if (blockIdx.x == 0 && lane == 0) st_flag(p.ws + OFF_FLG, tag);
        set_cnt(smem, 3, lane);
        constexpr int NL = DMAP == 0 ? 370 : 740;
        const char* q2 = p.ws + OFF_W2F + (DMAP == 0 ? (blockIdx.x & 1) * 4 * 378880 : 0) + ((blockIdx.x >> 3) * NL + lane) * 128;
#pragma unroll
        for (int j = 0; j < (NL + 63) / 64; ++j) touch(q2 + (j * 64 + lane < NL ? j * 8192 : 0), lds0 + L_DUMMY);
      }
    }
    const int tid = w * 64 + lane;
    const char* q = p.ws + (tid < 128 ? OFF_STP + rb * 12800 + tid * 4 : OFF_CSP + job * 12800 + min(tid - 128, 303) * 4);
    LD10(pv, q);
    if constexpr (C == 0) { LD10(pw, q + 512); }
    g.kstep(f1, f0, nxs, 0, h2, true, 0, 0);
    xs = nxs; hs = h2;
  }
  waitl();
  __builtin_amdgcn_sched_barrier(0);
  g.kstep(f0, f1, xs, 1, inc(hs), true, 0, 0);
  waitl();
  __builtin_amdgcn_sched_barrier(0);
  g.kstep(f1, f0, 0, 0, 0, false, 0, 0);
  {
    waitv<0>();
    asm volatile("" : "+v"(pv[0]), "+v"(pv[1]), "+v"(pv[2]), "+v"(pv[3]), "+v"(pv[4]), "+v"(pv[5]), "+v"(pv[6]), "+v"(pv[7]), "+v"(pv[8]), "+v"(pv[9]) :: "memory");
    float v = 0.f;
#pragma unroll
    for (int i = 0; i < 10; ++i) v += __uint_as_float(pv[i]);
    const int tid = w * 64 + lane;
    if constexpr (C == 0) {
      asm volatile("" : "+v"(pw[0]), "+v"(pw[1]), "+v"(pw[2]), "+v"(pw[3]), "+v"(pw[4]), "+v"(pw[5]), "+v"(pw[6]), "+v"(pw[7]), "+v"(pw[8]), "+v"(pw[9]) :: "memory");
      if (w < 2) {
        float v2 = 0.f;
#pragma unroll
        for (int i = 0; i < 10; ++i) v2 += __uint_as_float(pw[i]);
        float mean = v * (1.0f / 640.0f);
        float var = fmaxf(fmaf(-mean, mean, v2 * (1.0f / 640.0f)), 0.f);
        float rstd = __builtin_amdgcn_rsqf(var + 1e-5f);
        *(f32x2*)(smem + L_SUM + tid * 8) = f32x2{rstd, -rstd * mean};
      }
    }
    if (tid >= 128 && tid < 432) *(float*)(smem + L_CS + (tid - 128) * 4) = v;
  }
  barrier();
  return tag;
}

constexpr int RING0 = 8192, RINGB = 35840;
DI int slot_of(int w) { return w >= 4 ? RING0 + (w - 4) * RINGB : RING0 + w * RINGB + 18432; }
DI int d_rt() { const int xcd = blockIdx.x & 7, idx = blockIdx.x >> 3; return DMAP == 0 ? (xcd >> 1) * 8 + (idx >> 2) : xcd * 4 + (idx >> 3); }
DI int d_ct() { const int xcd = blockIdx.x & 7, idx = blockIdx.x >> 3; return DMAP == 0 ? (xcd & 1) * 4 + (idx & 3) : (idx & 7); }
DI int d_u0(int w) { return (w - 4) * 14 - (w == 7 ? 1 : 0); }
DI void ring_fill(const P1& p, char* smem, int w, int lane, int s0, int s1) {
  const char* pb = p.ws + OFF_W2F + (d_ct() * 74 + d_u0(w)) * 5120 + lane * 16;
  const unsigned ringa = lds_addr(smem) + slot_of(w);
#pragma unroll
  for (int s = s0; s < s1; ++s) glds5_sc1(pb + s * 5120, ringa + s * 5120);
}
DI bool early_ok(char* smem) {
  unsigned v = __hip_atomic_load((LDS3 unsigned*)(smem + L_CNT + 12), __ATOMIC_RELAXED, __HIP_MEMORY_SCOPE_WORKGROUP);
  return __builtin_amdgcn_readfirstlane(v) != 0;
}

DI void need_early(const P1& p, char* smem, u64 tag, int lane) {
  if (early_ok(smem)) return;
  poll256(p.ws + F_EARLY, tag, lane);
  if (blockIdx.x == 0 && lane == 0) st_flag(p.ws + OFF_FLG, tag);
  set_cnt(smem, 3, lane);
}
DI void poll_word(const char* q) {
#pragma unroll 1
  for (int spin = 0; spin < (SPIN_MAX >> 3); ++spin) {
    unsigned a;
    asm volatile("global_load_dword %0, %1, off sc1\n\ts_waitcnt vmcnt(0)" : "=&v"(a) : "v"(q) : "memory");
    if (__all(a != ~0u)) break;
    __builtin_amdgcn_s_sleep(2);
  }
}

template <int C>
DI u64 body_a(const P1& p, rsrc_t rs, char* smem, int rb, int g8, int w, int lane, bool& filled) {
  const int r = w & 3, lg = lane >> 4, l15 = lane & 15;
  Gemm<0, C> g;
  const u64 tag = first_pass<0, C>(g, p, rs, smem, rb, g8, 0, w, lane);
  if (C == 1 && w >= 6 && early_ok(smem)) { ring_fill(p, smem, w, lane, 0, 7); filled = true; }
  u32x2 kb[5];
  bf16x8 ap[5][3];
  load_aphi(smem, lane, ap);
  if constexpr (C == 0) {
    const f32x2 s0 = stat_of(smem, 32 * r + l15), s1 = stat_of(smem, 32 * r + 16 + l15);
#pragma unroll
    for (int t = 0; t < 5; ++t) {
      u32x2 k1;
      pack_k(smem, g.acc[t][1], t, s1, lg, k1);
      *(u32x2*)(smem + E_KB + (r * 5 + t) * 512 + lane * 8) = k1;
      pack_k(smem, g.acc[t][0], t, s0, lg, kb[t]);
    }
  } else {
    f32x4 sa[2], sb[2];
#pragma unroll
    for (int tt = 0; tt < 2; ++tt) {
      sa[tt] = *(const f32x4*)(smem + L_SUM + (32 * r + 16 * tt + 4 * lg) * 8);
      sb[tt] = *(const f32x4*)(smem + L_SUM + (32 * r + 16 * tt + 4 * lg + 2) * 8);
    }
#pragma unroll
    for (int t = 0; t < 5; ++t) {
      float c1 = *(const float*)(smem + L_CS + ((5 + t) * 16 + l15) * 4);
      unsigned o[4];
#pragma unroll
      for (int tt = 0; tt < 2; ++tt) {
        float y0 = fmaf(g.acc[t][tt][0], sa[tt][0], sa[tt][1] * c1);
        float y1 = fmaf(g.acc[t][tt][1], sa[tt][2], sa[tt][3] * c1);
        float y2 = fmaf(g.acc[t][tt][2], sb[tt][0], sb[tt][1] * c1);
        float y3 = fmaf(g.acc[t][tt][3], sb[tt][2], sb[tt][3] * c1);
        o[2 * tt] = pack2(y0, y1); o[2 * tt + 1] = pack2(y2, y3);
      }
      *(u32x4*)(smem + E_V + (r * 5 + t) * 1024 + lane * 16) = u32x4{o[0], o[1], o[2], o[3]};
    }
  }
  barrier();
  if constexpr (C == 1) {
#pragma unroll
    for (int t = 0; t < 5; ++t) kb[t] = *(const u32x2*)(smem + E_KB + (r * 5 + t) * 512 + lane * 8);
  }
  {
    const int tok = 32 * r + 16 * C + l15;
    u32x2 pos[5], neg[5];
    features(kb, ap, pos, neg);
#pragma unroll
    for (int t = 0; t < 5; ++t) {
      *(u32x2*)(smem + E_PHI + (t * 128 + tok) * 32 + lg * 8) = pos[t];
      *(u32x2*)(smem + E_PHI + ((5 + t) * 128 + tok) * 32 + lg * 8) = neg[t];
    }
  }
  barrier();
  {
    const unsigned pa = E_PHI + (4 * lg + (l15 >> 2)) * 32 + (l15 & 3) * 8;
    const u32x4 one4 = {0x3f803f80u, 0x3f803f80u, 0x3f803f80u, 0x3f803f80u};
    const bool valid = (l15 >> 2) == lg;
    f32x4 a[3][3];
    const char* qa[3]; const char* vb[3]; bool hf[3];
#pragma unroll
    for (int k3 = 0; k3 < 3; ++k3) {
      const int un0 = w + 8 * k3, un = un0 < 20 ? un0 : 19;
      hf[k3] = un & 1;
      qa[k3] = smem + pa + (un >> 1) * 4096;
      vb[k3] = smem + E_V + (un & 1) * 3072 + lane * 16;
#pragma unroll
      for (int j = 0; j < 3; ++j) a[k3][j] = f32x4{0, 0, 0, 0};
    }
#pragma unroll
    for (int ks = 0; ks < 4; ++ks)
#pragma unroll
      for (int k3 = 0; k3 < (C == 0 ? 3 : 2); ++k3) {
        bf16x8 af = tr2(qa[k3] + ks * 1024, qa[k3] + ks * 1024 + 512);
        const char* v = vb[k3] + ks * 5120;
        a[k3][0] = MFMA16(af, *(const bf16x8*)(v), a[k3][0]);
        a[k3][1] = MFMA16(af, *(const bf16x8*)(v + 1024), a[k3][1]);
        u32x4 b2 = *(const u32x4*)(v + (hf[k3] ? 0 : 2048));
        b2 = hf[k3] ? one4 : b2;
        a[k3][2] = MFMA16(af, __builtin_bit_cast(bf16x8, b2), a[k3][2]);
      }
#pragma unroll
    for (int k3 = 0; k3 < (C == 0 ? 3 : 2); ++k3) {
      const int un0 = w + 8 * k3;
      unsigned dst = (valid && un0 < 20) ? OFF_KVP + ((g8 * 16 + rb) * 60 + un0 * 3) * 256 + (lg * 4 + (l15 & 3)) * 16 : OOB;
#pragma unroll
      for (int j = 0; j < 3; ++j) st16(rs, dst + j * 256, __builtin_bit_cast(u32x4, a[k3][j]));
    }
  }
  (void)arrive(smem, 1, 8, lane);
  if (C == 0 && w == 1 && rb < 15) {
    need_early(p, smem, tag, lane);
    f32x4 a;
#pragma unroll 1
    for (int spin = 0; spin < 8; ++spin) {
      poll_word(p.ws + OFF_KVP + (g8 * 16 + l15) * 15360 + rb * 1024 + lg * 256 + 252);
      asm volatile("" ::: "memory");
      u32x4 kv[16];
#pragma unroll
      for (int i = 0; i < 16; ++i) kv[i] = ld16(rs, OFF_KVP + (g8 * 16 + i) * 15360 + rb * 1024 + lane * 16);
      bool ok = ready(kv[0]);
      a = __builtin_bit_cast(f32x4, kv[0]);
#pragma unroll
      for (int i = 1; i < 16; ++i) { ok = ok && ready(kv[i]); a += __builtin_bit_cast(f32x4, kv[i]); }
      if (__all(ok)) break;
    }
    int tau = rb * 4 + lg, m = (tau * 43) >> 8, tn = tau - 6 * m;
    int sg = m >= 5 ? 1 : 0, t = m - 5 * sg;
    int ks = t < 4 ? sg * 2 + (t >> 1) : 4, hf = t < 4 ? (t & 1) : sg;
    st8(rs, OFF_KVH + g8 * 7680 + (tn * 5 + ks) * 256 + l15 * 16 + hf * 8, u32x2{pack2(a[0], a[1]), pack2(a[2], a[3])});
    set_cnt(smem, 2, lane);
  } else if (rb < 15) wait_cnt(smem, 2, 1);
  wait_cnt(smem, 1, 8);
  return tag;
}

template <int C>
DI u64 body_b(const P1& p, rsrc_t rs, char* smem, int rb, int g8, int w, int lane, bool& filled) {
  const int r = w & 3, lg = lane >> 4, l15 = lane & 15;
  const int t0w = min(14 * g8, 94);
  Gemm<1, C> g;
  const u64 tag = first_pass<1, C>(g, p, rs, smem, rb, g8, t0w, w, lane);
  f32x2 st[2] = {stat_of(smem, 32 * r + l15), stat_of(smem, 32 * r + 16 + l15)};
  if constexpr (C == 1) {
    filled = early_ok(smem);
    __builtin_amdgcn_s_setprio(2);
#pragma unroll
    for (int pp = 0; pp < 5; ++pp) {
      if (filled) ring_fill(p, smem, w, lane, pp < 2 ? 2 * pp : pp + 2, pp < 2 ? 2 * pp + 2 : pp + 3);
      __builtin_amdgcn_sched_barrier(0);
      mish_pair(rs, smem, g.acc[2 * pp], g.acc[2 * pp + 1], 9 + 2 * pp, 4 + 2 * pp, t0w + 4 + 2 * pp, false, st, rb * 8 + 2 * r, lane);
      __builtin_amdgcn_sched_barrier(0);
    }
    __builtin_amdgcn_s_setprio(0);
    drain();
    if (arrive(smem, 0, 8, lane) && lane == 0) st_flag(p.ws + F_ACT + (rb * 16 + g8) * 8, tag);
  } else {
    __builtin_amdgcn_s_setprio(2);
    mish_pair(rs, smem, g.acc[5], g.acc[6], 5, 0, t0w, g8 == 7, st, rb * 8 + 2 * r, lane);
    mish_pair(rs, smem, g.acc[7], g.acc[8], 7, 2, t0w + 2, g8 == 7, st, rb * 8 + 2 * r, lane);
    __builtin_amdgcn_s_setprio(0);
    if (w == 0 && lane < 30) *(u32x4*)(smem + E_KV + lane * 272 + 256) = u32x4{0u, 0u, 0u, 0u};
    bf16x8 ap[5][3];
    load_aphi(smem, lane, ap);
    bf16x8 ph[2][5];
#pragma unroll
    for (int tt = 0; tt < 2; ++tt) {
      u32x2 kb[5], pos[5], neg[5];
#pragma unroll
      for (int t = 0; t < 5; ++t) pack_k(smem, g.acc[t][tt], t, st[tt], lg, kb[t]);
      if (tt == 0) {
        drain();
        if (arrive(smem, 0, 8, lane) && lane == 0) st_flag(p.ws + F_ACT + (rb * 16 + g8) * 8, tag);
      }
      features(kb, ap, pos, neg);
      ph[tt][0] = mk8(pos[0], pos[1]); ph[tt][1] = mk8(pos[2], pos[3]);
      ph[tt][2] = mk8(neg[0], neg[1]); ph[tt][3] = mk8(neg[2], neg[3]); ph[tt][4] = mk8(pos[4], neg[4]);
#pragma unroll
      for (int k = 0; k < 5; ++k) asm volatile("" : "+v"(ph[tt][k]));
    }
    if (w == 0) {
      need_early(p, smem, tag, lane);
      poll_word(p.ws + OFF_KVH + g8 * 7680 + (lane < 60 ? (lane >> 1) * 256 + (lane & 1) * 8 : 0));
      set_cnt(smem, 4, lane);
    } else wait_cnt(smem, 4, 1);
    {
      const int e0 = w * 120 + lane, e1 = e0 + 64;
      u32x4 a, b;
#pragma unroll 1
      for (int spin = 0; spin < SPIN_MAX; ++spin) {
        asm volatile("" ::: "memory");
        a = ld16(rs, OFF_KVH + g8 * 7680 + e0 * 16);
        b = ld16(rs, lane < 56 ? OFF_KVH + g8 * 7680 + e1 * 16 : OOB);
        if (__all(ready(a) && ready(b))) break;
        __builtin_amdgcn_s_sleep(2);
      }
      *(u32x4*)(smem + E_KV + (e0 >> 4) * 272 + (e0 & 15) * 16) = a;
      if (lane < 56) *(u32x4*)(smem + E_KV + (e1 >> 4) * 272 + (e1 & 15) * 16) = b;
    }
    waitl();
    asm volatile("" ::: "memory");
    (void)arrive(smem, 1, 4, lane);
    wait_cnt(smem, 1, 4);
    const char* ks_ = smem + E_KV + (((l15 >> 2) == lg) ? (lg * 4 + (l15 & 3)) * 16 : 256);
    f32x4 n[2][6];
#pragma unroll
    for (int tn = 0; tn < 6; ++tn) { n[0][tn] = f32x4{0, 0, 0, 0}; n[1][tn] = f32x4{0, 0, 0, 0}; }
    bf16x8 ka[2][6];
#pragma unroll
    for (int tn = 0; tn < 6; ++tn) ka[0][tn] = *(const bf16x8*)(ks_ + (tn * 5) * 272);
#pragma unroll
    for (int ks = 0; ks < 5; ++ks) {
      if (ks < 4) {
#pragma unroll
        for (int tn = 0; tn < 6; ++tn) ka[(ks + 1) & 1][tn] = *(const bf16x8*)(ks_ + (tn * 5 + ks + 1) * 272);
      }
#pragma unroll
      for (int tn = 0; tn < 6; ++tn) {
        n[0][tn] = MFMA16(ka[ks & 1][tn], ph[0][ks], n[0][tn]);
        n[1][tn] = MFMA16(ka[ks & 1][tn], ph[1][ks], n[1][tn]);
      }
    }
#pragma unroll
    for (int tt = 0; tt < 2; ++tt) {
      const f32x2 rc = splat2(__builtin_amdgcn_rcpf(n[tt][5][0] + 1e-6f));
      u32x2 o[5];
#pragma unroll
      for (int t = 0; t < 5; ++t) o[t] = u32x2{pack2v(lo2(n[tt][t]) * rc), pack2v(hi2(n[tt][t]) * rc)};
      const unsigned ob = OFF_O + (rb * 8 + 2 * r + tt) * OST + ((5 * g8) >> 1) * 1024 + lane * 16;
      if ((g8 & 1) == 0) {
        st16(rs, ob, u32x4{o[0][0], o[0][1], o[1][0], o[1][1]});
        st16(rs, ob + 1024, u32x4{o[2][0], o[2][1], o[3][0], o[3][1]});
        st8(rs, ob + 2048, o[4]);
      } else {
        st8(rs, ob + 8, o[0]);
        st16(rs, ob + 1024, u32x4{o[1][0], o[1][1], o[2][0], o[2][1]});
        st16(rs, ob + 2048, u32x4{o[3][0], o[3][1], o[4][0], o[4][1]});
      }
    }
  }
  return tag;
}

#define GLD(dst, ptr, off) asm volatile("global_load_dwordx4 %0, %1, off offset:" #off " sc1" : "=v"(dst) : "v"(ptr) : "memory")
#define WAITV(n) do { asm volatile("s_waitcnt vmcnt(%0)" :: "i"(n) : "memory"); __builtin_amdgcn_sched_barrier(0); } while (0)

#define ISSUE_A(A, s) do { GLD(A[s][0], pa, 0); GLD(A[s][1], pa1, 0); GLD(A[s][2], pa2, 0); GLD(A[s][3], pa3, 0); \
    pa += 1024; pa1 += 1024; pa2 += 1024; pa3 += 1024; } while (0)
#define ISSUE_B(B, s) do { GLD(B[s][0], pb, -2048); GLD(B[s][1], pb, -1024); GLD(B[s][2], pb, 0); GLD(B[s][3], pb, 1024); GLD(B[s][4], pb, 2048); \
    pb += 5120; } while (0)
DI void wait_flags(char* smem, int cnt, bool poller, const char* flags, int n, u64 tag, int lane) {
  if (poller) { polln(flags, n, tag, lane); set_cnt(smem, cnt, lane); }
  else { wait_cnt(smem, cnt, 1); drain(); }
}
DI void stream_o(const char* pa, const char* pb, const char* pw, char* smem, bool poller, int lane, int rot, f32x4 (&acc)[5][4]) {
  u32x4 A[5][4], B[4][5];
#pragma unroll
  for (int s = 0; s < 4; ++s) ISSUE_B(B, s);
  if (poller) { poll_word(pw); set_cnt(smem, 7, lane); }
  else wait_cnt(smem, 7, 1);
#pragma unroll 1
  for (int spin = 0; spin < (SPIN_MAX >> 2); ++spin) {
    const char* q0 = pa + 2048 + rot * OST; const char* q1 = pa + 2048 + ((rot + 1) & 3) * OST;
    const char* q2 = pa + 2048 + ((rot + 2) & 3) * OST; const char* q3 = pa + 2048 + ((rot + 3) & 3) * OST;
#define LD5(t, q) GLD(A[0][t], q, -2048); GLD(A[1][t], q, -1024); GLD(A[2][t], q, 0); GLD(A[3][t], q, 1024); GLD(A[4][t], q, 2048);
    LD5(0, q0) LD5(1, q1) LD5(2, q2) LD5(3, q3)
    WAITV(0);
    unsigned mx = 0;
#pragma unroll
    for (int s = 0; s < 5; ++s)
#pragma unroll
      for (int t = 0; t < 4; ++t) {
        asm volatile("" : "+v"(A[s][t]));
        mx = max(max(mx, A[s][t][0]), max(A[s][t][1], max(A[s][t][2], A[s][t][3])));
      }
    if (__all(mx != ~0u)) break;
    __builtin_amdgcn_s_sleep(4);
  }
#define OSTEP(sa, sb) \
  _Pragma("unroll") for (int i = 0; i < 5; ++i) asm volatile("" : "+v"(B[sb][i])); \
  _Pragma("unroll") for (int n = 0; n < 5; ++n) _Pragma("unroll") for (int t = 0; t < 4; ++t) \
    acc[n][t] = MFMA16(__builtin_bit_cast(bf16x8, B[sb][n]), __builtin_bit_cast(bf16x8, A[sa][t]), acc[n][t]); \
  __builtin_amdgcn_sched_barrier(0);
  OSTEP(0, 0)
  ISSUE_B(B, 0);
  OSTEP(1, 1) OSTEP(2, 2) OSTEP(3, 3)
  WAITV(0);
  OSTEP(4, 0)
}

template <int NST, int NA, int NB, int S>
DI void lds_steps(const char*& pa, const char*& pa1, const char*& pa2, const char*& pa3, const char*& pb, const char* ring, unsigned ringa,
                  u32x4 (&A)[NA][4], f32x4 (&acc)[5][4]) {
  if constexpr (S < NST) {
    constexpr int ja = S - NA, jb = S - NB;
    auto cnt = [](int j0, bool skipA) constexpr {
      int n = 0;
      for (int j = j0; j < S; ++j) {
        if (j + NA < NST && !(skipA && j == j0)) n += 4;
        if (j + NB < NST) n += 5;
      }
      return n;
    };
    constexpr int ya = ja < 0 ? 4 * ((NA < NST ? NA : NST) - 1 - S) + cnt(0, false) : cnt(ja, true);
    constexpr int yb = jb < 0 ? 1000 : cnt(jb + 1, false);
    constexpr int y = ya < yb ? ya : yb;
    WAITV(y);
    bf16x8 bf[5];
#pragma unroll
    for (int n = 0; n < 5; ++n) bf[n] = *(const bf16x8*)(ring + (S % NB) * 5120 + n * 1024);
#pragma unroll
    for (int i = 0; i < 4; ++i) asm volatile("" : "+v"(A[S % NA][i]));
#define LSTEP(t, ptr) \
    _Pragma("unroll") for (int n = 0; n < 5; ++n) acc[n][t] = MFMA16(bf[n], __builtin_bit_cast(bf16x8, A[S % NA][t]), acc[n][t]); \
    __builtin_amdgcn_sched_barrier(0); \
    if constexpr (S + NA < NST) { GLD(A[S % NA][t], ptr, 0); ptr += 1024; } \
    __builtin_amdgcn_sched_barrier(0);
    LSTEP(0, pa) LSTEP(1, pa1) LSTEP(2, pa2) LSTEP(3, pa3)
    if constexpr (S + NB < NST) { glds5_sc1(pb, ringa + (S % NB) * 5120); pb += 5120; }
    lds_steps<NST, NA, NB, S + 1>(pa, pa1, pa2, pa3, pb, ring, ringa, A, acc);
  }
}
template <int NST, int NA, int NB>
DI void stream_lds(const char* pa, unsigned astr, const char* pb, char* smem, int roff, int cnt, bool poller, const char* flags, int nflags,
                   u64 tag, int lane, int rot, f32x4 (&acc)[5][4]) {
  const char* pa1 = pa + ((rot + 1) & 3) * astr; const char* pa2 = pa + ((rot + 2) & 3) * astr; const char* pa3 = pa + ((rot + 3) & 3) * astr;
  pa += rot * astr;
  const unsigned ringa = lds_addr(smem) + roff;
  u32x4 A[NA][4];
  pb += NB * 5120;
  if (poller) { polln(flags, nflags, tag, lane); set_cnt(smem, cnt, lane); }
  else wait_cnt(smem, cnt, 1);
#pragma unroll
  for (int s = 0; s < NA; ++s) ISSUE_A(A, s);
  lds_steps<NST, NA, NB, 0>(pa, pa1, pa2, pa3, pb, smem + roff + lane * 16, ringa, A, acc);
}

template <int C>
DI void finish(const P1& p, char* smem, int rt, int ct, int w, int lane, const f32x4 (&acc)[5][4]) {
  constexpr int N0 = C == 0 ? 0 : 3, NN = C == 0 ? 3 : 2;
  const int lg = lane >> 4, l15 = lane & 15;
  const unsigned o0 = (unsigned)((rt * 64 + (w & 3) * 16 + l15) * 640 + ct * 80 + N0 * 16 + 4 * lg);
  u32x2 xv[NN];
#pragma unroll
  for (int m = 0; m < NN; ++m) xv[m] = *(const u32x2*)(p.x + o0 + 16 * m);
  char* dst = smem + slot_of(w) + lane * 16;
#pragma unroll
  for (int i = 1; i < 4; ++i)
#pragma unroll
    for (int n = 0; n < 5; ++n) *(f32x4*)(dst + ((i - 1) * 5 + n) * 1024) = acc[n][i];
#pragma unroll
  for (int m = 0; m < 5 - NN; ++m) *(f32x4*)(dst + (15 + m) * 1024) = acc[(C == 0 ? 3 : 0) + m][0];
  __syncthreads();
  const char* same[3]; const char* other[3];
#pragma unroll
  for (int j = 1; j < 4; ++j) {
    const int v = (w - j) & 3;
    same[j - 1] = smem + slot_of(4 * C + v) + ((j - 1) * 5 + N0) * 1024 + lane * 16;
    other[j - 1] = smem + slot_of(4 * (1 - C) + v) + ((j - 1) * 5 + N0) * 1024 + lane * 16;
  }
  const char* partner = smem + slot_of(w ^ 4) + 15 * 1024 + lane * 16;
#pragma unroll
  for (int m = 0; m < NN; ++m) {
    f32x4 mine = (acc[N0 + m][0] + *(const f32x4*)(same[0] + m * 1024)) + (*(const f32x4*)(same[1] + m * 1024) + *(const f32x4*)(same[2] + m * 1024));
    f32x4 theirs = (*(const f32x4*)(partner + m * 1024) + *(const f32x4*)(other[0] + m * 1024)) +
                   (*(const f32x4*)(other[1] + m * 1024) + *(const f32x4*)(other[2] + m * 1024));
    const f32x4 ssum = C == 0 ? mine : theirs, bsum = C == 0 ? theirs : mine;
    float xs[4] = {bf_lo(xv[m][0]), bf_hi(xv[m][0]), bf_lo(xv[m][1]), bf_hi(xv[m][1])};
    float r[4];
#pragma unroll
    for (int j = 0; j < 4; ++j) {
      float bb = bsum[j];
      float g = bb * __builtin_amdgcn_rcpf(1.0f + __builtin_amdgcn_exp2f(-bb * LOG2E));
      r[j] = fmaf(ssum[j], g, xs[j]);
    }
    *(u32x2*)(p.out + o0 + 16 * m) = u32x2{pack2(r[0], r[1]), pack2(r[2], r[3])};
  }
}

__global__ __launch_bounds__(512, 2) void k_fused(P1 p) {
  extern __shared__ __attribute__((aligned(16))) char smem[];
  const int b = blockIdx.x, xcd = b & 7, idx = b >> 3;
  const int w = __builtin_amdgcn_readfirstlane(threadIdx.x >> 6), lane = threadIdx.x & 63;
  const int rb = (xcd >> 2) * 8 + (idx >> 2);
  const int jl = idx & 3, xc = xcd & 3;
  const int type = jl >> 1, g8 = 2 * xc + (jl & 1);
  const int c = w >> 2;
  const rsrc_t rs = __builtin_amdgcn_make_buffer_rsrc(p.ws, 0, WS_USED, 0x00020000);
  if (w == 7) {
    u64 pc;
    asm volatile("s_getpc_b64 %0" : "=s"(pc));
    touch((const char*)(pc & ~127ull) + (idx * 18 + (lane < 18 ? lane : 0)) * 128, lds_addr(smem) + L_DUMMY);
  }
  u64 tag;
  bool filled = false;
  if (type == 0) {
    if (c == 0) tag = body_a<0>(p, rs, smem, rb, g8, w, lane, filled);
    else tag = body_a<1>(p, rs, smem, rb, g8, w, lane, filled);
  } else {
    if (c == 0) tag = body_b<0>(p, rs, smem, rb, g8, w, lane, filled);
    else tag = body_b<1>(p, rs, smem, rb, g8, w, lane, filled);
  }
  if (w == 5) {
    unsigned v = __hip_atomic_load((LDS3 unsigned*)(smem + L_CNT + 12), __ATOMIC_RELAXED, __HIP_MEMORY_SCOPE_WORKGROUP);
    if (__builtin_amdgcn_readfirstlane(v) == 0) {
      poll256(p.ws + F_EARLY, tag, lane);
      if (blockIdx.x == 0 && lane == 0) st_flag(p.ws + OFF_FLG, tag);
      set_cnt(smem, 3, lane);
    }
  } else wait_cnt(smem, 3, 1);
  const int rt = DMAP == 0 ? (xcd >> 1) * 8 + (idx >> 2) : xcd * 4 + (idx >> 3);
  const int ct = DMAP == 0 ? (xcd & 1) * 4 + (idx & 3) : (idx & 7);
  f32x4 acc[5][4];
#pragma unroll
  for (int n = 0; n < 5; ++n)
#pragma unroll
    for (int t = 0; t < 4; ++t) acc[n][t] = f32x4{0, 0, 0, 0};
  if (w >= 4) {
    const int u0 = (w - 4) * 14 - (w == 7 ? 1 : 0);
    const char* pa = p.ws + OFF_ACT + (rt * 4) * AST + u0 * 1024 + lane * 16;
    const char* pb = p.ws + OFF_W2F + (ct * 74 + u0) * 5120 + lane * 16;
    const char* fact = p.ws + F_ACT + (rt >> 1) * 128;
    bool poller = w == 4;
    if (!filled) {
      if (poller) { polln(fact, 8, tag, lane); set_cnt(smem, 5, lane); poller = false; }
      ring_fill(p, smem, w, lane, 0, 7);
    }
    if (w < 6) stream_lds<14, NA_ACT, 7>(pa, AST, pb, smem, slot_of(w), 5, poller, fact, 8, tag, lane, w & 3, acc);
    else stream_lds<13, NA_ACT, 7>(pa, AST, pb, smem, slot_of(w), 5, false, fact, 8, tag, lane, w & 3, acc);
    (void)arrive(smem, 8, 4, lane);
  } else {
    stream_o(p.ws + OFF_O + (rt * 4) * OST + 5 * w * 1024 + lane * 16,
             p.ws + OFF_W2F + (ct * 74 + 54 + 5 * w) * 5120 + lane * 16 + 2048,
             p.ws + OFF_O + (rt * 4 + 1 + 2 * ((lane >> 3) & 1)) * OST + (((5 * (lane & 7)) >> 1) + 2) * 1024 + 1008 - ((lane & 1) ? 0 : 1024) , smem, w == 0, lane, w & 3, acc);
    wait_cnt(smem, 8, 4);
  }
  if (w >= 4) finish<1>(p, smem, rt, ct, w, lane, acc);
  else finish<0>(p, smem, rt, ct, w, lane, acc);
}
}

extern "C" void kernel_launch(void* const* d_in, const int* in_sizes, int n_in, void* d_out, int out_size, void* d_ws,
                              size_t ws_size, hipStream_t stream) {
  using namespace hk;
  P1 p;
  p.x = (const u16*)d_in[0]; p.wqkv = (const u16*)d_in[1]; p.wphi = (const u16*)d_in[2]; p.wo = (const u16*)d_in[3];
  p.w1 = (const u16*)d_in[5]; p.b1 = (const u16*)d_in[6]; p.w2 = (const u16*)d_in[7]; p.ws = (char*)d_ws; p.out = (u16*)d_out;
  (void)hipFuncSetAttribute((const void*)k_fused, hipFuncAttributeMaxDynamicSharedMemorySize, L_END);
  k_fused<<<256, 512, L_END, stream>>>(p);
}
```

```cpp
#include <hip/hip_runtime.h>
#include <hip/hip_bf16.h>

typedef unsigned short u16;
typedef __bf16 bf16x2 __attribute__((ext_vector_type(2)));
typedef __bf16 bf16x4 __attribute__((ext_vector_type(4)));
typedef __bf16 bf16x8 __attribute__((ext_vector_type(8)));
typedef float f32x2 __attribute__((ext_vector_type(2)));
typedef float f32x4 __attribute__((ext_vector_type(4)));
typedef unsigned u32x2 __attribute__((ext_vector_type(2)));
typedef unsigned u32x4 __attribute__((ext_vector_type(4)));
#define LDS3 __attribute__((address_space(3)))
#define DI __device__ __forceinline__
#define MFMA16(a, b, c) __builtin_amdgcn_mfma_f32_16x16x32_bf16((a), (b), (c), 0, 0, 0)

namespace hk {
typedef unsigned long long u64;
constexpr float LOG2E = 1.4426950408889634f;
constexpr float LN2 = 0.6931471805599453f;
#define ROTP 2
#define NA_ACT 3
#define DMAP 0
constexpr int FLAG_I = ROTP + 2 > 8 ? 8 : ROTP + 2;
#define APAD 256
constexpr unsigned AST = 54 * 1024 + APAD, OST = 20 * 1024 + APAD;
constexpr unsigned OFF_ACT = 0;
constexpr unsigned OFF_O = OFF_ACT + 128 * AST;
constexpr unsigned OFF_W2F = OFF_O + 128 * OST;
constexpr unsigned OFF_KVP = OFF_W2F + 8 * 74 * 5 * 1024;
constexpr unsigned OFF_KVH = OFF_KVP + 8 * 16 * 60 * 256;
constexpr unsigned OFF_STP = OFF_KVH + 8 * 30 * 256;
constexpr unsigned OFF_CSP = OFF_STP + 16 * 12800;
constexpr unsigned OFF_FLG = OFF_CSP + 16 * 12800;
constexpr unsigned F_EARLY = OFF_FLG + 128;
constexpr unsigned F_ACT = F_EARLY + 2048;
constexpr unsigned F_KVP = F_ACT + 2048;
constexpr unsigned F_KVH = F_KVP + 1024;
constexpr unsigned F_O = F_KVH + 1024;
constexpr unsigned F_ST = F_O + 2048;
constexpr unsigned F_CS = F_ST + 2048;
constexpr unsigned WS_USED = F_CS + 2048;
constexpr unsigned OOB = 0x7ffff000u;
constexpr int XB = 16384, WB0 = 3 * XB;
constexpr int L_PT = 118784;
constexpr int L_DUMMY = 151552;
constexpr int L_RAW = 155648;
constexpr int L_BIAS = L_RAW + 3200;
constexpr int L_ZERO = L_BIAS + 448;
constexpr int L_CNT = L_ZERO + 64;
constexpr int L_BIASL = L_CNT + 64;
constexpr int L_CS = L_BIASL + 896;
constexpr int L_SUM = L_CS + 1216;
constexpr int L_END = 163840;
static_assert(L_SUM + 1024 <= L_END - 256, "lds");
constexpr int E_PHI = 0;
constexpr int E_V = 40960;
constexpr int E_KB = 61440;
constexpr int E_KV = 0;

DI unsigned pack2(float a, float b) {
  f32x2 v = {a, b};
  return __builtin_bit_cast(unsigned, __builtin_convertvector(v, bf16x2));
}
DI unsigned pack2v(f32x2 v) { return __builtin_bit_cast(unsigned, __builtin_convertvector(v, bf16x2)); }
DI bf16x2 as_bf2(unsigned u) { return __builtin_bit_cast(bf16x2, u); }
DI float bf_lo(unsigned u) { return __uint_as_float(u << 16); }
DI float bf_hi(unsigned u) { return __uint_as_float(u & 0xffff0000u); }
DI float rscatter_add(float m0, float m1, float m2, float m3) {
  auto a = __builtin_amdgcn_permlane16_swap(__float_as_uint(m0), __float_as_uint(m1), false, false);
  auto b = __builtin_amdgcn_permlane16_swap(__float_as_uint(m2), __float_as_uint(m3), false, false);
  float A = __uint_as_float(a[0]) + __uint_as_float(a[1]);
  float B = __uint_as_float(b[0]) + __uint_as_float(b[1]);
  auto c = __builtin_amdgcn_permlane32_swap(__float_as_uint(A), __float_as_uint(B), false, false);
  return __uint_as_float(c[0]) + __uint_as_float(c[1]);
}
DI f32x2 lo2(f32x4 v) { return __builtin_shufflevector(v, v, 0, 1); }
DI f32x2 hi2(f32x4 v) { return __builtin_shufflevector(v, v, 2, 3); }
DI f32x2 fma2(f32x2 a, f32x2 b, f32x2 c) { return __builtin_elementwise_fma(a, b, c); }
DI f32x2 splat2(float a) { return f32x2{a, a}; }
DI f32x2 exp2v(f32x2 a) { return f32x2{__builtin_amdgcn_exp2f(a[0]), __builtin_amdgcn_exp2f(a[1])}; }
DI f32x2 rcpv(f32x2 a) { return f32x2{__builtin_amdgcn_rcpf(a[0]), __builtin_amdgcn_rcpf(a[1])}; }
DI void dual_softmax(const f32x4 (&p)[5], u32x2 (&pos)[5], u32x2 (&neg)[5]) {
  float mx = fmaxf(p[0][0], p[0][1]), mn = fminf(p[0][0], p[0][1]);
  mx = fmaxf(fmaxf(mx, p[0][2]), p[0][3]); mn = fminf(fminf(mn, p[0][2]), p[0][3]);
#pragma unroll
  for (int t = 1; t < 5; ++t) {
    mx = fmaxf(fmaxf(mx, p[t][0]), p[t][1]); mx = fmaxf(fmaxf(mx, p[t][2]), p[t][3]);
    mn = fminf(fminf(mn, p[t][0]), p[t][1]); mn = fminf(fminf(mn, p[t][2]), p[t][3]);
  }
  const f32x2 L = splat2(LOG2E), nL = splat2(-LOG2E), cx = splat2(-mx * LOG2E), cn = splat2(mn * LOG2E);
  f32x2 ep[10], en[10];
#pragma unroll
  for (int t = 0; t < 5; ++t) {
    ep[2 * t] = exp2v(fma2(lo2(p[t]), L, cx)); ep[2 * t + 1] = exp2v(fma2(hi2(p[t]), L, cx));
    en[2 * t] = exp2v(fma2(lo2(p[t]), nL, cn)); en[2 * t + 1] = exp2v(fma2(hi2(p[t]), nL, cn));
  }
  f32x2 sp = ep[0], sn = en[0];
#pragma unroll
  for (int i = 1; i < 10; ++i) { sp += ep[i]; sn += en[i]; }
  const f32x2 ip = splat2(__builtin_amdgcn_rcpf(sp[0] + sp[1])), in_ = splat2(__builtin_amdgcn_rcpf(sn[0] + sn[1]));
#pragma unroll
  for (int t = 0; t < 5; ++t) {
    pos[t] = u32x2{pack2v(ep[2 * t] * ip), pack2v(ep[2 * t + 1] * ip)};
    neg[t] = u32x2{pack2v(en[2 * t] * in_), pack2v(en[2 * t + 1] * in_)};
  }
}
DI f32x2 mish2(f32x2 hl) {
  f32x2 e = exp2v(hl);
  f32x2 d = fma2(e, e + splat2(2.0f), splat2(2.0f));
  f32x2 t = fma2(rcpv(d), splat2(-2.0f * LN2), splat2(LN2));
  return hl * t;
}
DI bf16x8 mk8(u32x2 a, u32x2 b) {
  u32x4 v = {a[0], a[1], b[0], b[1]};
  return __builtin_bit_cast(bf16x8, v);
}
DI bf16x8 tr2(const char* a, const char* b) {
  bf16x4 lo = __builtin_amdgcn_ds_read_tr16_b64_v4bf16((LDS3 bf16x4*)(a));
  bf16x4 hi = __builtin_amdgcn_ds_read_tr16_b64_v4bf16((LDS3 bf16x4*)(b));
  return __builtin_shufflevector(lo, hi, 0, 1, 2, 3, 4, 5, 6, 7);
}

struct P1 {
  const u16* x; const u16* wqkv; const u16* wphi; const u16* wo; const u16* w1; const u16* b1; const u16* w2;
  char* ws; u16* out;
};
typedef __amdgpu_buffer_rsrc_t rsrc_t;

DI unsigned lds_addr(const void* p) { return (unsigned)(size_t)(LDS3 const char*)p; }
DI void glds1(const void* g, unsigned lds) {
  unsigned keep;
  asm volatile("s_mov_b32 %0, m0\n\ts_mov_b32 m0, %2\n\ts_nop 0\n\tglobal_load_lds_dwordx4 %1, off\n\ts_mov_b32 m0, %0"
               : "=&s"(keep) : "v"(g), "s"(lds) : "memory");
}
DI void glds5_sc1(const void* g, unsigned lds) {
  unsigned keep;
  asm volatile("s_mov_b32 %0, m0\n\t"
               "s_mov_b32 m0, %2\n\ts_nop 0\n\tglobal_load_lds_dwordx4 %1, off sc1\n\t"
               "global_load_lds_dwordx4 %1, off offset:1024 sc1\n\t"
               "global_load_lds_dwordx4 %1, off offset:2048 sc1\n\t"
               "global_load_lds_dwordx4 %1, off offset:3072 sc1\n\t"
               "s_add_u32 m0, %2, 0x1000\n\ts_nop 0\n\tglobal_load_lds_dwordx4 %3, off sc1\n\t"
               "s_mov_b32 m0, %0"
               : "=&s"(keep) : "v"(g), "s"(lds), "v"((const char*)g + 4096) : "memory", "scc");
}
DI void touch(const void* g, unsigned lds) {
  unsigned keep;
  asm volatile("s_mov_b32 %0, m0\n\ts_mov_b32 m0, %2\n\ts_nop 0\n\tglobal_load_lds_dword %1, off sc1\n\ts_mov_b32 m0, %0"
               : "=&s"(keep) : "v"(g), "s"(lds) : "memory");
}
DI u64 next_tag(u64 e) { return e * 0x9E3779B97F4A7C15ull + 0x632BE59BD9B4E019ull; }
DI void st_flag(char* p, u64 v) { __hip_atomic_store((u64*)p, v, __ATOMIC_RELAXED, __HIP_MEMORY_SCOPE_AGENT); }
DI void st16(rsrc_t rs, unsigned off, u32x4 v) { __builtin_amdgcn_raw_buffer_store_b128(v, rs, off, 0, 16); }
DI void st8(rsrc_t rs, unsigned off, u32x2 v) { __builtin_amdgcn_raw_buffer_store_b64(v, rs, off, 0, 16); }
DI void st4(rsrc_t rs, unsigned off, float v) { __builtin_amdgcn_raw_buffer_store_b32(__float_as_uint(v), rs, off, 0, 16); }
DI u32x4 ld16(rsrc_t rs, unsigned off) { return __builtin_amdgcn_raw_buffer_load_b128(rs, off, 0, 16); }
DI bool ready(u32x4 v) { return v[0] != 0xffffffffu && v[1] != 0xffffffffu && v[2] != 0xffffffffu && v[3] != 0xffffffffu; }
DI void drain() { asm volatile("s_waitcnt vmcnt(0)" ::: "memory"); }
constexpr int SPIN_MAX = 1 << 18;
DI bool flags_ok(u32x4 a, u32x4 b, u64 tag) {
  const unsigned tl = (unsigned)tag, th = (unsigned)(tag >> 32);
  return a[0] == tl && a[1] == th && a[2] == tl && a[3] == th && b[0] == tl && b[1] == th && b[2] == tl && b[3] == th;
}
DI void poll256(const char* flags, u64 tag, int lane) {
  const char* q = flags + lane * 32;
#pragma unroll 1
  for (int spin = 0; spin < SPIN_MAX; ++spin) {
    u32x4 a, b;
    asm volatile("global_load_dwordx4 %0, %2, off sc1\n\tglobal_load_dwordx4 %1, %2, off offset:16 sc1\n\ts_waitcnt vmcnt(0)"
                 : "=&v"(a), "=&v"(b) : "v"(q) : "memory");
    if (__all(flags_ok(a, b, tag))) break;
    __builtin_amdgcn_s_sleep(8);
  }
}
DI void pollq(const char* q, u64 tag) {
#pragma unroll 1
  for (int spin = 0; spin < SPIN_MAX; ++spin) {
    u32x2 a;
    asm volatile("global_load_dwordx2 %0, %1, off sc1\n\ts_waitcnt vmcnt(0)" : "=&v"(a) : "v"(q) : "memory");
    if (__all(a[0] == (unsigned)tag && a[1] == (unsigned)(tag >> 32))) break;
    __builtin_amdgcn_s_sleep(3);
  }
}
DI void polln(const char* flags, int n, u64 tag, int lane) { pollq(flags + (lane < n ? lane : 0) * 8, tag); }
DI bool arrive(char* smem, int idx, int n, int lane) {
  unsigned old = 0;
  asm volatile("" ::: "memory");
  if (lane == 0) old = __hip_atomic_fetch_add((LDS3 unsigned*)(smem + L_CNT + idx * 4), 1u, __ATOMIC_RELAXED, __HIP_MEMORY_SCOPE_WORKGROUP);
  return __builtin_amdgcn_readfirstlane(old) == (unsigned)(n - 1);
}
DI void set_cnt(char* smem, int idx, int lane) {
  asm volatile("" ::: "memory");
  if (lane == 0) __hip_atomic_store((LDS3 unsigned*)(smem + L_CNT + idx * 4), 1u, __ATOMIC_RELAXED, __HIP_MEMORY_SCOPE_WORKGROUP);
}
DI void wait_cnt(char* smem, int idx, unsigned n) {
#pragma unroll 1
  for (int spin = 0; spin < (SPIN_MAX << 4); ++spin) {
    unsigned v = __hip_atomic_load((LDS3 unsigned*)(smem + L_CNT + idx * 4), __ATOMIC_RELAXED, __HIP_MEMORY_SCOPE_WORKGROUP);
    if (__builtin_amdgcn_readfirstlane(v) >= n) break;
    __builtin_amdgcn_s_sleep(1);
  }
  asm volatile("" ::: "memory");
}

template <int NT> struct Frags { bf16x8 x[2]; bf16x8 w[NT]; };

template <int N> DI void waitv() { asm volatile("s_waitcnt vmcnt(%0)" :: "i"(N) : "memory"); }
DI void waitl() { __builtin_amdgcn_s_waitcnt(0xC07F); }

template <int TYPE, int C>
struct Gemm {
  static constexpr int NT = TYPE == 0 ? 5 : (C == 0 ? 9 : 10);
  static constexpr int NU = TYPE == 0 ? 11 : 19;
  static constexpr int WROW = NU * 32, NC = NU * 2, HB = 32 * WROW;
  static constexpr int NWH = TYPE == 0 ? 6 : 5;
  static constexpr int NWS = (NU + 7) / 8;
  static constexpr int NFULL = NU - (NWS - 1) * 8;
  static constexpr int MAGIC = (65536 + NC - 1) / NC;
  static constexpr bool VW = (TYPE == 0 && C == 1);
  static constexpr int unit_of(int t) { return TYPE == 0 ? t + 5 * C : t + 9 * C; }

  const char* xsrc[2];
  const char* wsrc[NWS];
  unsigned winc[NWS];
  unsigned xa0, wa, lds0;
  int cx, hw, hslot, w;
  bool full;
  char* smem;
  f32x4 acc[NT][2];

  DI void init(const P1& p, char* smem_, int rb, int g8, int t0w, int rot, int w_, int lane) {
    smem = smem_; w = w_; cx = rot; hw = 2 * rot; hslot = 0; lds0 = lds_addr(smem_); full = w < NFULL;
    const int r = w & 3, lg = lane >> 4, l15 = lane & 15;
#pragma unroll
    for (int i = 0; i < 2; ++i) {
      int row = (i * 8 + w) * 8 + (lane >> 3);
      int ch = (lane & 7) ^ ((row >> 1) & 7);
      xsrc[i] = (const char*)p.x + (unsigned)((rb * 128 + row) * 1280 + ch * 16 + rot * 128);
    }
#pragma unroll
    for (int i = 0; i < NWS; ++i) {
      int ci = (i * 8 + w) * 64 + lane;
      ci = ci < 32 * NC ? ci : lane;
      int pos = (ci * MAGIC) >> 16, c16 = ci - pos * NC;
      int kk = pos & 15;
      int k = (kk >= 4 && kk < 12) ? (pos ^ 12) : pos;
      bool isq = TYPE == 0 || c16 < 10;
      int colb;
      if (TYPE == 0) { int c = min(c16, 19); colb = 1280 + 160 * g8 + c * 16 + (c >= 10 ? 1120 : 0); }
      else colb = isq ? (160 * g8 + c16 * 16) : (32 * t0w + (c16 - 10) * 16);
      unsigned stride = isq ? 3840u : 3456u;
      wsrc[i] = (isq ? (const char*)p.wqkv : (const char*)p.w1) + (unsigned)((k + rot * 64) * stride + colb);
      winc[i] = stride * 32;
    }
    xa0 = (32 * r + l15) * 128 + ((lg ^ ((l15 >> 1) & 7)) * 16);
    wa = WB0 + (16 * (lg >> 1) + 4 * (lg & 1) + (l15 >> 2)) * WROW + (VW ? 160 + (l15 & 3) * 40 : (l15 & 3) * 8);
#pragma unroll
    for (int t = 0; t < NT; ++t) { acc[t][0] = f32x4{0, 0, 0, 0}; acc[t][1] = f32x4{0, 0, 0, 0}; }
  }
  DI void issue_x1(int s, int xslot) {
    glds1(xsrc[s], lds0 + xslot * XB + w * 1024 + s * 0x2000);
    xsrc[s] += 128;
    if (s == 1 && ++cx == 10) { cx = 0; xsrc[0] -= 1280; xsrc[1] -= 1280; }
  }
  DI void issue_w1(int s) {
    if (s < NWS - 1 || full) { glds1(wsrc[s], lds0 + WB0 + hslot * HB + w * 1024 + s * 0x2000); wsrc[s] += winc[s]; }
    if (s == NWS - 1) {
      hslot = hslot == NWH - 1 ? 0 : hslot + 1;
      if (++hw == 20) {
        hw = 0;
#pragma unroll
        for (int i = 0; i < NWS; ++i) wsrc[i] -= 20 * winc[i];
      }
    }
  }
  DI void issue_x(int xslot) { issue_x1(0, xslot); issue_x1(1, xslot); }
  DI void issue_w() {
#pragma unroll
    for (int s = 0; s < NWS; ++s) issue_w1(s);
  }
  template <int NX, int NH, int E> DI void wait() {
    if (full) waitv<2 * NX + NH * NWS + E>(); else waitv<2 * NX + NH * (NWS - 1) + E>();
  }
  DI void rd_x(Frags<NT>& f, int xslot, int ks, int tt) { f.x[tt] = *(const bf16x8*)(smem + xslot * XB + (xa0 ^ (ks * 64)) + tt * 2048); }
  DI void rd_w(Frags<NT>& f, int hs, int t) {
    const char* q = smem + hs * HB + wa + (VW ? t * 8 : unit_of(t) * 32);
    f.w[t] = tr2(q, q + 8 * WROW);
  }
  DI void kstep(const Frags<NT>& f, Frags<NT>& g, int nxs, int nks, int nhs, bool do_rd, int nreq, int ixs) {
    constexpr int NS = 2 + 2 * NWS;
#pragma unroll
    for (int t = 0; t < NT; ++t) {
      if (do_rd) {
        if (t < 2) rd_x(g, nxs, nks, t);
        rd_w(g, nhs, t);
      }
#pragma unroll
      for (int tt = 0; tt < 2; ++tt)
        acc[t][tt] = VW ? MFMA16(f.x[tt], f.w[t], acc[t][tt]) : MFMA16(f.w[t], f.x[tt], acc[t][tt]);
#pragma unroll
      for (int s = (t * NS) / NT; s < ((t + 1) * NS) / NT; ++s) {
        if (s < NWS) { if (nreq > 0) issue_w1(s); }
        else if (s < NWS + 2) { if (nreq > 1) issue_x1(s - NWS, ixs); }
        else { if (nreq > 1) issue_w1(s - NWS - 2); }
      }
      __builtin_amdgcn_sched_barrier(0);
    }
  }
};

DI void dots2(float& c0, float& c1, const bf16x8& wa_, const bf16x8& wb_) {
  u32x4 a = __builtin_bit_cast(u32x4, wa_), b = __builtin_bit_cast(u32x4, wb_);
  unsigned a0 = a[0], a1 = a[1], a2 = a[2], a3 = a[3], b0 = b[0], b1 = b[1], b2 = b[2], b3 = b[3];
  asm volatile("s_nop 1\n\t"
               "v_dot2c_f32_bf16 %0, 0x3f803f80, %2\n\tv_dot2c_f32_bf16 %1, 0x3f803f80, %6\n\t"
               "v_dot2c_f32_bf16 %0, 0x3f803f80, %3\n\tv_dot2c_f32_bf16 %1, 0x3f803f80, %7\n\t"
               "v_dot2c_f32_bf16 %0, 0x3f803f80, %4\n\tv_dot2c_f32_bf16 %1, 0x3f803f80, %8\n\t"
               "v_dot2c_f32_bf16 %0, 0x3f803f80, %5\n\tv_dot2c_f32_bf16 %1, 0x3f803f80, %9\n\ts_nop 3"
               : "+v"(c0), "+v"(c1) : "v"(a0), "v"(a1), "v"(a2), "v"(a3), "v"(b0), "v"(b1), "v"(b2), "v"(b3));
}
DI void dots_x(float& s1, float& s2, const bf16x8& x) {
  u32x4 a = __builtin_bit_cast(u32x4, x);
  unsigned a0 = a[0], a1 = a[1], a2 = a[2], a3 = a[3];
  asm volatile("s_nop 1\n\t"
               "v_dot2c_f32_bf16 %0, 0x3f803f80, %2\n\tv_dot2c_f32_bf16 %1, %2, %2\n\t"
               "v_dot2c_f32_bf16 %0, 0x3f803f80, %3\n\tv_dot2c_f32_bf16 %1, %3, %3\n\t"
               "v_dot2c_f32_bf16 %0, 0x3f803f80, %4\n\tv_dot2c_f32_bf16 %1, %4, %4\n\t"
               "v_dot2c_f32_bf16 %0, 0x3f803f80, %5\n\tv_dot2c_f32_bf16 %1, %5, %5\n\ts_nop 3"
               : "+v"(s1), "+v"(s2) : "v"(a0), "v"(a1), "v"(a2), "v"(a3));
}
DI f32x2 stat_of(const char* smem, int tok) { return *(const f32x2*)(smem + L_SUM + tok * 8); }
DI void mish_pair(rsrc_t rs, const char* smem, const f32x4 (&a0)[2], const f32x4 (&a1)[2], int ucs, int wl, int wt, bool skip,
                  const f32x2 (&st)[2], int tt0, int lane) {
  const int lg = lane >> 4;
  f32x4 bb[2], c4[2];
#pragma unroll
  for (int h = 0; h < 2; ++h) {
    bb[h] = *(const f32x4*)(smem + L_BIASL + ((wl + h) * 16 + 4 * lg) * 4);
    c4[h] = *(const f32x4*)(smem + L_CS + ((ucs + h) * 16 + 4 * lg) * 4);
  }
#pragma unroll
  for (int tt = 0; tt < 2; ++tt) {
    const f32x2 rsL = splat2(st[tt][0] * LOG2E), nbL = splat2(st[tt][1] * LOG2E);
    u32x4 o;
    o[0] = pack2v(mish2(fma2(lo2(a0[tt]), rsL, fma2(nbL, lo2(c4[0]), lo2(bb[0])))));
    o[1] = pack2v(mish2(fma2(hi2(a0[tt]), rsL, fma2(nbL, hi2(c4[0]), hi2(bb[0])))));
    o[2] = pack2v(mish2(fma2(lo2(a1[tt]), rsL, fma2(nbL, lo2(c4[1]), lo2(bb[1])))));
    o[3] = pack2v(mish2(fma2(hi2(a1[tt]), rsL, fma2(nbL, hi2(c4[1]), hi2(bb[1])))));
    st16(rs, skip ? OOB : OFF_ACT + (tt0 + tt) * AST + (wt >> 1) * 1024 + lane * 16, o);
  }
}
DI void load_aphi(const char* smem, int lane, bf16x8 (&ap)[5][3]) {
  const int lg = lane >> 4, l15 = lane & 15;
  const char* pa[6];
#pragma unroll
  for (int i = 0; i < 6; ++i) {
    int d0 = 16 * i + 4 * lg;
    int h = (d0 * 13) >> 8;
    bool ok = (h == (l15 & 3)) && i < 5;
    pa[i] = smem + (ok ? L_RAW + h * 800 + (d0 - 20 * h + (l15 >> 2)) * 40 : L_ZERO);
  }
#pragma unroll
  for (int t = 0; t < 5; ++t)
#pragma unroll
    for (int ks = 0; ks < 3; ++ks) ap[t][ks] = tr2(pa[2 * ks] + t * 8, pa[2 * ks + 1] + t * 8);
}
DI void pack_k(const char* smem, const f32x4& a, int t, f32x2 st, int lg, u32x2& kb) {
  f32x4 c4 = *(const f32x4*)(smem + L_CS + (t * 16 + 4 * lg) * 4);
  const f32x2 rsv = splat2(st[0]), nb = splat2(st[1]);
  kb = u32x2{pack2v(fma2(lo2(a), rsv, nb * lo2(c4))), pack2v(fma2(hi2(a), rsv, nb * hi2(c4)))};
}
DI void features(const u32x2 (&kb)[5], const bf16x8 (&ap)[5][3], u32x2 (&pos)[5], u32x2 (&neg)[5]) {
  bf16x8 b0 = mk8(kb[0], kb[1]), b1 = mk8(kb[2], kb[3]), b2 = mk8(kb[4], u32x2{0u, 0u});
  f32x4 pk[5];
#pragma unroll
  for (int t = 0; t < 5; ++t) pk[t] = MFMA16(ap[t][2], b2, MFMA16(ap[t][1], b1, MFMA16(ap[t][0], b0, (f32x4{0, 0, 0, 0}))));
  dual_softmax(pk, pos, neg);
}
DI void barrier() {
  waitl();
  __builtin_amdgcn_s_barrier();
  __builtin_amdgcn_sched_barrier(0);
}

template <int TYPE, int C, class G>
DI u64 first_pass(G& g, const P1& p, rsrc_t rs, char* smem, int rb, int g8, int t0w, int w, int lane) {
  constexpr int NT = G::NT, NWH = G::NWH;
  const int r = w & 3, lg = lane >> 4, l15 = lane & 15;
  const unsigned lds0 = lds_addr(smem);
  u32x2 ev;
  asm volatile("global_load_dwordx2 %0, %1, off sc1" : "=v"(ev) : "v"(p.ws + OFF_FLG) : "memory");
  const int job = TYPE * 8 + g8;
  const int myc = (rb + job) % 10;
  const int rot = (myc / ROTP) * ROTP;
  const int my_i = myc - rot;
  g.init(p, smem, rb, g8, t0w, rot, w, lane);
  if (C == 1) {
    int ci = r * 64 + lane;
    if (ci < 200) glds1((const char*)p.wphi + g8 * 3200 + ci * 16, lds0 + L_RAW + r * 1024);
  } else if (w == 3) {
    if (TYPE == 1 && lane < 28) glds1((const char*)p.b1 + t0w * 32 + lane * 16, lds0 + L_BIAS);
    if (lane < 32) *(unsigned*)(smem + L_ZERO + lane * 4) = 0u;
  }
  g.issue_w(); g.issue_x(0); g.issue_w();
  __builtin_amdgcn_s_barrier();
  g.issue_w(); g.issue_x(1); g.issue_w();
  g.issue_w(); g.issue_x(2);
  if (NWH == 6) g.issue_w();
  constexpr int AH = NWH - 4;
  const int ptj = w * 128 + rb * 8 + g8;
  const bool has_pt = TYPE == 0 && w < 6 && ptj < 740;
  const int ptu = ptj / 10, ptL = ptj - ptu * 10;
  if (has_pt) {
#pragma unroll
    for (int i = 0; i < 4; ++i) {
      int rho = i * 8 + (lane >> 3);
      const u16* src;
      if (ptu < 54) src = p.w2 + (unsigned)((32 * ptu + rho) * 640);
      else {
        int T = 2 * (ptu - 54) + (rho >> 4), Gq = (T * 13) >> 6;
        src = p.wo + (unsigned)(((4 * Gq + ((rho >> 2) & 3)) * 20 + 4 * (T - 5 * Gq) + (rho & 3)) * 640);
      }
      glds1((const char*)src + ptL * 128 + (lane & 7) * 16, lds0 + L_PT + w * 4096 + i * 1024);
    }
  }
  float cs[3] = {0.f, 0.f, 0.f}, sx[2] = {0.f, 0.f}, cd = 0.f;
  auto do_dots = [&](const Frags<NT>& f) {
    dots_x(sx[0], sx[1], f.x[C]);
    if (r == 0) { dots2(cs[0], cs[1], f.w[0], f.w[4]); if (NT > 8) dots2(cs[2], cd, f.w[NT > 8 ? 8 : 0], f.w[0]); }
    else if (r == 1) { dots2(cs[0], cs[1], f.w[1], f.w[NT > 5 ? 5 : 0]); if (NT > 9) dots2(cs[2], cd, f.w[NT > 9 ? 9 : 0], f.w[0]); }
    else if (r == 2) dots2(cs[0], cs[1], f.w[2], f.w[NT > 6 ? 6 : 0]);
    else dots2(cs[0], cs[1], f.w[3], f.w[NT > 7 ? 7 : 0]);
  };
  Frags<NT> f0, f1;
  if (has_pt) g.template wait<2, NWH - 2, 4>(); else g.template wait<2, NWH - 2, 0>();
  barrier();
  g.rd_x(f0, 0, 0, 0); g.rd_x(f0, 0, 0, 1);
#pragma unroll
  for (int t = 0; t < NT; ++t) g.rd_w(f0, 0, t);
  if (TYPE == 1) {
    int tid = w * 64 + lane;
    if (tid < 224) *(float*)(smem + L_BIASL + tid * 4) = bf_lo(*(const u16*)(smem + L_BIAS + tid * 2)) * LOG2E;
  }
  asm volatile("" : "+v"(ev));
  const u64 tag = next_tag(((u64)(unsigned)__builtin_amdgcn_readfirstlane(ev[1]) << 32) | (unsigned)__builtin_amdgcn_readfirstlane(ev[0]));
  int xs = 0, hs = 0;
  u32x4 fa, fb;
  u32x2 tf;
  unsigned pv[10], pw[10];
  const char* tfp = p.ws + (lane < 10 ? F_ST + (rb * 16 + lane) * 8 : F_CS + (job * 16 + (lane < 20 ? lane - 10 : 0)) * 8);
  auto inc = [](int s) { return s == NWH - 1 ? 0 : s + 1; };
#define LD10(pv, q) \
    asm volatile("global_load_dword %0, %10, off sc1\n\tglobal_load_dword %1, %10, off offset:1280 sc1\n\t" \
                 "global_load_dword %2, %10, off offset:2560 sc1\n\tglobal_load_dword %3, %10, off offset:3840 sc1\n\t" \
                 "global_load_dword %4, %11, off sc1\n\tglobal_load_dword %5, %11, off offset:1280 sc1\n\t" \
                 "global_load_dword %6, %11, off offset:2560 sc1\n\tglobal_load_dword %7, %11, off offset:3840 sc1\n\t" \
                 "global_load_dword %8, %12, off sc1\n\tglobal_load_dword %9, %12, off offset:1280 sc1" \
                 : "=&v"(pv[0]), "=&v"(pv[1]), "=&v"(pv[2]), "=&v"(pv[3]), "=&v"(pv[4]), "=&v"(pv[5]), "=&v"(pv[6]), "=&v"(pv[7]), "=&v"(pv[8]), "=&v"(pv[9]) \
                 : "v"(q), "v"((q) + 5120), "v"((q) + 10240) : "memory")
#pragma unroll 1
  for (int i = 0; i < 8; ++i) {
    waitl();
    __builtin_amdgcn_sched_barrier(0);
    const int h1 = inc(hs), h2 = inc(h1);
    g.kstep(f0, f1, xs, 1, h1, true, 0, 0);
    if (i == my_i) do_dots(f0);
    const int nxs = xs == 2 ? 0 : xs + 1;
    if (TYPE == 0) {
      if (i == 0 && has_pt) g.template wait<1, AH, 4>();
      else if (i == 1) g.template wait<1, AH, 2>();
      else if (i == 2 && has_pt) g.template wait<1, AH, 6>();
      else if (i == 2) g.template wait<1, AH, 2>();
      else if (i == 3 && has_pt) g.template wait<1, AH, 4>();
      else g.template wait<1, AH, 0>();
    } else {
      if (i == 1) g.template wait<1, AH, 3>();
      else g.template wait<1, AH, 0>();
    }
    barrier();
    if (i == FLAG_I && w == 0 && lane < 3)
      st_flag(p.ws + (lane == 0 ? F_EARLY + blockIdx.x * 8 : (lane == 1 ? F_ST + (rb * 16 + job) * 8 : F_CS + (job * 16 + rb) * 8)), tag);
    g.kstep(f1, f0, nxs, 0, h2, true, i < 7 ? 3 : (NWH == 5 ? 1 : 0), xs);
    if (i == 0) {
      const u32x4 sent = {~0u, ~0u, ~0u, ~0u};
      if (TYPE == 0) {
        unsigned kb = OFF_KVP + (g8 * 16 + rb) * 15360 + lane * 16;
        st16(rs, kb + w * 1024, sent);
        st16(rs, w < 7 ? kb + (8 + w) * 1024 : (lane < 30 ? OFF_KVH + g8 * 7680 + rb * 480 + lane * 16 : OOB), sent);
      } else {
        const unsigned ob = OFF_O + (rb * 8 + w) * OST + ((5 * g8) >> 1) * 1024 + lane * 16;
        st16(rs, ob + 1024, sent);
        st16(rs, ob + ((g8 & 1) ? 2048 : 0), sent);
        st8(rs, ob + ((g8 & 1) ? 8 : 2048), u32x2{~0u, ~0u});
      }
    }
    if (i == 1 && has_pt) {
      const char* q = smem + L_PT + w * 4096 + (4 * lg + (l15 >> 2)) * 128 + (l15 & 3) * 8;
#pragma unroll
      for (int n = 0; n < 4; ++n) {
        bf16x8 f = tr2(q + n * 32, q + 2048 + n * 32);
        int N = 4 * ptL + n, ct = (N * 13) >> 6;
        st16(rs, OFF_W2F + ((ct * 74 + ptu) * 5 + (N - 5 * ct)) * 1024 + lane * 16, __builtin_bit_cast(u32x4, f));
      }
    }
    if (i == my_i) {
      do_dots(f1);
      float v = rscatter_add(sx[0], sx[1], cs[0], cs[1]);
      unsigned a = lg < 2 ? OFF_STP + (rb * 10 + myc) * 1280 + lg * 512 + (32 * r + 16 * C + l15) * 4
                          : OFF_CSP + (job * 10 + myc) * 1280 + ((G::unit_of(0) + r + (lg == 2 ? 0 : 4)) * 16 + l15) * 4;
      bool on = lg < 2 ? job < 10 : (rb < 10 && (lg == 2 || r + 4 < NT));
      st4(rs, on ? a : OOB, v);
      if (NT > 8) {
        float v2 = rscatter_add(cs[2], 0.f, 0.f, 0.f);
        st4(rs, (lg == 0 && rb < 10 && r + 8 < NT) ? OFF_CSP + (job * 10 + myc) * 1280 + ((G::unit_of(0) + r + 8) * 16 + l15) * 4 : OOB, v2);
      }
    }
    if (i == 6) {
      asm volatile("global_load_dwordx2 %0, %1, off sc1" : "=v"(tf) : "v"(tfp) : "memory");
      if (w == 5)
        asm volatile("global_load_dwordx4 %0, %2, off sc1\n\tglobal_load_dwordx4 %1, %2, off offset:16 sc1"
                     : "=&v"(fa), "=&v"(fb) : "v"(p.ws + F_EARLY + lane * 32) : "memory");
    }
    xs = nxs; hs = h2;
  }
  {
    waitl();
    __builtin_amdgcn_sched_barrier(0);
    const int h1 = inc(hs), h2 = inc(h1);
    g.kstep(f0, f1, xs, 1, h1, true, 0, 0);
    const int nxs = xs == 2 ? 0 : xs + 1;
    waitv<0>();
    barrier();
    asm volatile("" : "+v"(tf));
    if (!__all(tf[0] == (unsigned)tag && tf[1] == (unsigned)(tag >> 32))) pollq(tfp, tag);
    if (C == 1 && w == 5) {
      asm volatile("" : "+v"(fa), "+v"(fb));
      if (__all(flags_ok(fa, fb, tag))) {
        if (blockIdx.x == 0 && lane == 0) st_flag(p.ws + OFF_FLG, tag);
        set_cnt(smem, 3, lane);
        constexpr int NL = DMAP == 0 ? 370 : 740;
        const char* q2 = p.ws + OFF_W2F + (DMAP == 0 ? (blockIdx.x & 1) * 4 * 378880 : 0) + ((blockIdx.x >> 3) * NL + lane) * 128;
#pragma unroll
        for (int j = 0; j < (NL + 63) / 64; ++j) touch(q2 + (j * 64 + lane < NL ? j * 8192 : 0), lds0 + L_DUMMY);
      }
    }
    const int tid = w * 64 + lane;
    const char* q = p.ws + (tid < 128 ? OFF_STP + rb * 12800 + tid * 4 : OFF_CSP + job * 12800 + min(tid - 128, 303) * 4);
    LD10(pv, q);
    if constexpr (C == 0) { LD10(pw, q + 512); }
    g.kstep(f1, f0, nxs, 0, h2, true, 0, 0);
    xs = nxs; hs = h2;
  }
  waitl();
  __builtin_amdgcn_sched_barrier(0);
  g.kstep(f0, f1, xs, 1, inc(hs), true, 0, 0);
  waitl();
  __builtin_amdgcn_sched_barrier(0);
  g.kstep(f1, f0, 0, 0, 0, false, 0, 0);
  {
    waitv<0>();
    asm volatile("" : "+v"(pv[0]), "+v"(pv[1]), "+v"(pv[2]), "+v"(pv[3]), "+v"(pv[4]), "+v"(pv[5]), "+v"(pv[6]), "+v"(pv[7]), "+v"(pv[8]), "+v"(pv[9]) :: "memory");
    float v = 0.f;
#pragma unroll
    for (int i = 0; i < 10; ++i) v += __uint_as_float(pv[i]);
    const int tid = w * 64 + lane;
    if constexpr (C == 0) {
      asm volatile("" : "+v"(pw[0]), "+v"(pw[1]), "+v"(pw[2]), "+v"(pw[3]), "+v"(pw[4]), "+v"(pw[5]), "+v"(pw[6]), "+v"(pw[7]), "+v"(pw[8]), "+v"(pw[9]) :: "memory");
      if (w < 2) {
        float v2 = 0.f;
#pragma unroll
        for (int i = 0; i < 10; ++i) v2 += __uint_as_float(pw[i]);
        float mean = v * (1.0f / 640.0f);
        float var = fmaxf(fmaf(-mean, mean, v2 * (1.0f / 640.0f)), 0.f);
        float rstd = __builtin_amdgcn_rsqf(var + 1e-5f);
        *(f32x2*)(smem + L_SUM + tid * 8) = f32x2{rstd, -rstd * mean};
      }
    }
    if (tid >= 128 && tid < 432) *(float*)(smem + L_CS + (tid - 128) * 4) = v;
  }
  barrier();
  return tag;
}

constexpr int RING0 = 8192, RINGB = 35840;
DI int slot_of(int w) { return w >= 4 ? RING0 + (w - 4) * RINGB : RING0 + w * RINGB + 18432; }
DI int d_rt() { const int xcd = blockIdx.x & 7, idx = blockIdx.x >> 3; return DMAP == 0 ? (xcd >> 1) * 8 + (idx >> 2) : xcd * 4 + (idx >> 3); }
DI int d_ct() { const int xcd = blockIdx.x & 7, idx = blockIdx.x >> 3; return DMAP == 0 ? (xcd & 1) * 4 + (idx & 3) : (idx & 7); }
DI int d_u0(int w) { return (w - 4) * 14 - (w == 7 ? 1 : 0); }
DI void ring_fill(const P1& p, char* smem, int w, int lane, int s0, int s1) {
  const char* pb = p.ws + OFF_W2F + (d_ct() * 74 + d_u0(w)) * 5120 + lane * 16;
  const unsigned ringa = lds_addr(smem) + slot_of(w);
#pragma unroll
  for (int s = s0; s < s1; ++s) glds5_sc1(pb + s * 5120, ringa + s * 5120);
}
DI bool early_ok(char* smem) {
  unsigned v = __hip_atomic_load((LDS3 unsigned*)(smem + L_CNT + 12), __ATOMIC_RELAXED, __HIP_MEMORY_SCOPE_WORKGROUP);
  return __builtin_amdgcn_readfirstlane(v) != 0;
}

DI void need_early(const P1& p, char* smem, u64 tag, int lane) {
  if (early_ok(smem)) return;
  poll256(p.ws + F_EARLY, tag, lane);
  if (blockIdx.x == 0 && lane == 0) st_flag(p.ws + OFF_FLG, tag);
  set_cnt(smem, 3, lane);
}
DI void poll_word(const char* q) {
#pragma unroll 1
  for (int spin = 0; spin < (SPIN_MAX >> 3); ++spin) {
    unsigned a;
    asm volatile("global_load_dword %0, %1, off sc1\n\ts_waitcnt vmcnt(0)" : "=&v"(a) : "v"(q) : "memory");
    if (__all(a != ~0u)) break;
    __builtin_amdgcn_s_sleep(2);
  }
}

template <int C>
DI u64 body_a(const P1& p, rsrc_t rs, char* smem, int rb, int g8, int w, int lane, bool& filled) {
  const int r = w & 3, lg = lane >> 4, l15 = lane & 15;
  Gemm<0, C> g;
  const u64 tag = first_pass<0, C>(g, p, rs, smem, rb, g8, 0, w, lane);
  if (C == 1 && w >= 6 && early_ok(smem)) { ring_fill(p, smem, w, lane, 0, 7); filled = true; }
  u32x2 kb[5];
  bf16x8 ap[5][3];
  load_aphi(smem, lane, ap);
  if constexpr (C == 0) {
    const f32x2 s0 = stat_of(smem, 32 * r + l15), s1 = stat_of(smem, 32 * r + 16 + l15);
#pragma unroll
    for (int t = 0; t < 5; ++t) {
      u32x2 k1;
      pack_k(smem, g.acc[t][1], t, s1, lg, k1);
      *(u32x2*)(smem + E_KB + (r * 5 + t) * 512 + lane * 8) = k1;
      pack_k(smem, g.acc[t][0], t, s0, lg, kb[t]);
    }
  } else {
    f32x4 sa[2], sb[2];
#pragma unroll
    for (int tt = 0; tt < 2; ++tt) {
      sa[tt] = *(const f32x4*)(smem + L_SUM + (32 * r + 16 * tt + 4 * lg) * 8);
      sb[tt] = *(const f32x4*)(smem + L_SUM + (32 * r + 16 * tt + 4 * lg + 2) * 8);
    }
#pragma unroll
    for (int t = 0; t < 5; ++t) {
      float c1 = *(const float*)(smem + L_CS + ((5 + t) * 16 + l15) * 4);
      unsigned o[4];
#pragma unroll
      for (int tt = 0; tt < 2; ++tt) {
        float y0 = fmaf(g.acc[t][tt][0], sa[tt][0], sa[tt][1] * c1);
        float y1 = fmaf(g.acc[t][tt][1], sa[tt][2], sa[tt][3] * c1);
        float y2 = fmaf(g.acc[t][tt][2], sb[tt][0], sb[tt][1] * c1);
        float y3 = fmaf(g.acc[t][tt][3], sb[tt][2], sb[tt][3] * c1);
        o[2 * tt] = pack2(y0, y1); o[2 * tt + 1] = pack2(y2, y3);
      }
      *(u32x4*)(smem + E_V + (r * 5 + t) * 1024 + lane * 16) = u32x4{o[0], o[1], o[2], o[3]};
    }
  }
  barrier();
  if constexpr (C == 1) {
#pragma unroll
    for (int t = 0; t < 5; ++t) kb[t] = *(const u32x2*)(smem + E_KB + (r * 5 + t) * 512 + lane * 8);
  }
  {
    const int tok = 32 * r + 16 * C + l15;
    u32x2 pos[5], neg[5];
    features(kb, ap, pos, neg);
#pragma unroll
    for (int t = 0; t < 5; ++t) {
      *(u32x2*)(smem + E_PHI + (t * 128 + tok) * 32 + lg * 8) = pos[t];
      *(u32x2*)(smem + E_PHI + ((5 + t) * 128 + tok) * 32 + lg * 8) = neg[t];
    }
  }
  barrier();
  {
    const unsigned pa = E_PHI + (4 * lg + (l15 >> 2)) * 32 + (l15 & 3) * 8;
    const u32x4 one4 = {0x3f803f80u, 0x3f803f80u, 0x3f803f80u, 0x3f803f80u};
    const bool valid = (l15 >> 2) == lg;
    f32x4 a[3][3];
    const char* qa[3]; const char* vb[3]; bool hf[3];
#pragma unroll
    for (int k3 = 0; k3 < 3; ++k3) {
      const int un0 = w + 8 * k3, un = un0 < 20 ? un0 : 19;
      hf[k3] = un & 1;
      qa[k3] = smem + pa + (un >> 1) * 4096;
      vb[k3] = smem + E_V + (un & 1) * 3072 + lane * 16;
#pragma unroll
      for (int j = 0; j < 3; ++j) a[k3][j] = f32x4{0, 0, 0, 0};
    }
#pragma unroll
    for (int ks = 0; ks < 4; ++ks)
#pragma unroll
      for (int k3 = 0; k3 < (C == 0 ? 3 : 2); ++k3) {
        bf16x8 af = tr2(qa[k3] + ks * 1024, qa[k3] + ks * 1024 + 512);
        const char* v = vb[k3] + ks * 5120;
        a[k3][0] = MFMA16(af, *(const bf16x8*)(v), a[k3][0]);
        a[k3][1] = MFMA16(af, *(const bf16x8*)(v + 1024), a[k3][1]);
        u32x4 b2 = *(const u32x4*)(v + (hf[k3] ? 0 : 2048));
        b2 = hf[k3] ? one4 : b2;
        a[k3][2] = MFMA16(af, __builtin_bit_cast(bf16x8, b2), a[k3][2]);
      }
#pragma unroll
    for (int k3 = 0; k3 < (C == 0 ? 3 : 2); ++k3) {
      const int un0 = w + 8 * k3;
      unsigned dst = (valid && un0 < 20) ? OFF_KVP + ((g8 * 16 + rb) * 60 + un0 * 3) * 256 + (lg * 4 + (l15 & 3)) * 16 : OOB;
#pragma unroll
      for (int j = 0; j < 3; ++j) st16(rs, dst + j * 256, __builtin_bit_cast(u32x4, a[k3][j]));
    }
  }
  (void)arrive(smem, 1, 8, lane);
  if (C == 0 && w == 1 && rb < 15) {
    need_early(p, smem, tag, lane);
    f32x4 a;
    poll_word(p.ws + OFF_KVP + (g8 * 16 + l15) * 15360 + rb * 1024 + lg * 256 + 252);
#pragma unroll 1
    for (int spin = 0; spin < (SPIN_MAX >> 3); ++spin) {
      asm volatile("" ::: "memory");
      u32x4 kv[16];
#pragma unroll
      for (int i = 0; i < 16; ++i) kv[i] = ld16(rs, OFF_KVP + (g8 * 16 + i) * 15360 + rb * 1024 + lane * 16);
      bool ok = ready(kv[0]);
      a = __builtin_bit_cast(f32x4, kv[0]);
#pragma unroll
      for (int i = 1; i < 16; ++i) { ok = ok && ready(kv[i]); a += __builtin_bit_cast(f32x4, kv[i]); }
      if (__all(ok)) break;
      __builtin_amdgcn_s_sleep(2);
    }
    int tau = rb * 4 + lg, m = (tau * 43) >> 8, tn = tau - 6 * m;
    int sg = m >= 5 ? 1 : 0, t = m - 5 * sg;
    int ks = t < 4 ? sg * 2 + (t >> 1) : 4, hf = t < 4 ? (t & 1) : sg;
    st8(rs, OFF_KVH + g8 * 7680 + (tn * 5 + ks) * 256 + l15 * 16 + hf * 8, u32x2{pack2(a[0], a[1]), pack2(a[2], a[3])});
    set_cnt(smem, 2, lane);
  } else if (rb < 15) wait_cnt(smem, 2, 1);
  wait_cnt(smem, 1, 8);
  return tag;
}

template <int C>
DI u64 body_b(const P1& p, rsrc_t rs, char* smem, int rb, int g8, int w, int lane, bool& filled) {
  const int r = w & 3, lg = lane >> 4, l15 = lane & 15;
  const int t0w = min(14 * g8, 94);
  Gemm<1, C> g;
  const u64 tag = first_pass<1, C>(g, p, rs, smem, rb, g8, t0w, w, lane);
  f32x2 st[2] = {stat_of(smem, 32 * r + l15), stat_of(smem, 32 * r + 16 + l15)};
  if constexpr (C == 1) {
    filled = early_ok(smem);
    __builtin_amdgcn_s_setprio(2);
#pragma unroll
    for (int pp = 0; pp < 5; ++pp) {
      if (filled) ring_fill(p, smem, w, lane, pp < 2 ? 2 * pp : pp + 2, pp < 2 ? 2 * pp + 2 : pp + 3);
      __builtin_amdgcn_sched_barrier(0);
      mish_pair(rs, smem, g.acc[2 * pp], g.acc[2 * pp + 1], 9 + 2 * pp, 4 + 2 * pp, t0w + 4 + 2 * pp, false, st, rb * 8 + 2 * r, lane);
      __builtin_amdgcn_sched_barrier(0);
    }
    __builtin_amdgcn_s_setprio(0);
    drain();
    if (arrive(smem, 0, 8, lane) && lane == 0) st_flag(p.ws + F_ACT + (rb * 16 + g8) * 8, tag);
  } else {
    __builtin_amdgcn_s_setprio(2);
    mish_pair(rs, smem, g.acc[5], g.acc[6], 5, 0, t0w, g8 == 7, st, rb * 8 + 2 * r, lane);
    mish_pair(rs, smem, g.acc[7], g.acc[8], 7, 2, t0w + 2, g8 == 7, st, rb * 8 + 2 * r, lane);
    __builtin_amdgcn_s_setprio(0);
    if (w == 0 && lane < 30) *(u32x4*)(smem + E_KV + lane * 272 + 256) = u32x4{0u, 0u, 0u, 0u};
    bf16x8 ap[5][3];
    load_aphi(smem, lane, ap);
    bf16x8 ph[2][5];
#pragma unroll
    for (int tt = 0; tt < 2; ++tt) {
      u32x2 kb[5], pos[5], neg[5];
#pragma unroll
      for (int t = 0; t < 5; ++t) pack_k(smem, g.acc[t][tt], t, st[tt], lg, kb[t]);
      if (tt == 0) {
        drain();
        if (arrive(smem, 0, 8, lane) && lane == 0) st_flag(p.ws + F_ACT + (rb * 16 + g8) * 8, tag);
      }
      features(kb, ap, pos, neg);
      ph[tt][0] = mk8(pos[0], pos[1]); ph[tt][1] = mk8(pos[2], pos[3]);
      ph[tt][2] = mk8(neg[0], neg[1]); ph[tt][3] = mk8(neg[2], neg[3]); ph[tt][4] = mk8(pos[4], neg[4]);
#pragma unroll
      for (int k = 0; k < 5; ++k) asm volatile("" : "+v"(ph[tt][k]));
    }
    if (w == 0) {
      need_early(p, smem, tag, lane);
      poll_word(p.ws + OFF_KVH + g8 * 7680 + (lane < 60 ? (lane >> 1) * 256 + (lane & 1) * 8 : 0));
      set_cnt(smem, 4, lane);
    } else wait_cnt(smem, 4, 1);
    {
      const int e0 = w * 120 + lane, e1 = e0 + 64;
      u32x4 a, b;
#pragma unroll 1
      for (int spin = 0; spin < SPIN_MAX; ++spin) {
        asm volatile("" ::: "memory");
        a = ld16(rs, OFF_KVH + g8 * 7680 + e0 * 16);
        b = ld16(rs, lane < 56 ? OFF_KVH + g8 * 7680 + e1 * 16 : OOB);
        if (__all(ready(a) && ready(b))) break;
        __builtin_amdgcn_s_sleep(2);
      }
      *(u32x4*)(smem + E_KV + (e0 >> 4) * 272 + (e0 & 15) * 16) = a;
      if (lane < 56) *(u32x4*)(smem + E_KV + (e1 >> 4) * 272 + (e1 & 15) * 16) = b;
    }
    waitl();
    asm volatile("" ::: "memory");
    (void)arrive(smem, 1, 4, lane);
    wait_cnt(smem, 1, 4);
    const char* ks_ = smem + E_KV + (((l15 >> 2) == lg) ? (lg * 4 + (l15 & 3)) * 16 : 256);
    f32x4 n[2][6];
#pragma unroll
    for (int tn = 0; tn < 6; ++tn) { n[0][tn] = f32x4{0, 0, 0, 0}; n[1][tn] = f32x4{0, 0, 0, 0}; }
    bf16x8 ka[2][6];
#pragma unroll
    for (int tn = 0; tn < 6; ++tn) ka[0][tn] = *(const bf16x8*)(ks_ + (tn * 5) * 272);
#pragma unroll
    for (int ks = 0; ks < 5; ++ks) {
      if (ks < 4) {
#pragma unroll
        for (int tn = 0; tn < 6; ++tn) ka[(ks + 1) & 1][tn] = *(const bf16x8*)(ks_ + (tn * 5 + ks + 1) * 272);
      }
#pragma unroll
      for (int tn = 0; tn < 6; ++tn) {
        n[0][tn] = MFMA16(ka[ks & 1][tn], ph[0][ks], n[0][tn]);
        n[1][tn] = MFMA16(ka[ks & 1][tn], ph[1][ks], n[1][tn]);
      }
    }
#pragma unroll
    for (int tt = 0; tt < 2; ++tt) {
      const f32x2 rc = splat2(__builtin_amdgcn_rcpf(n[tt][5][0] + 1e-6f));
      u32x2 o[5];
#pragma unroll
      for (int t = 0; t < 5; ++t) o[t] = u32x2{pack2v(lo2(n[tt][t]) * rc), pack2v(hi2(n[tt][t]) * rc)};
      const unsigned ob = OFF_O + (rb * 8 + 2 * r + tt) * OST + ((5 * g8) >> 1) * 1024 + lane * 16;
      if ((g8 & 1) == 0) {
        st16(rs, ob, u32x4{o[0][0], o[0][1], o[1][0], o[1][1]});
        st16(rs, ob + 1024, u32x4{o[2][0], o[2][1], o[3][0], o[3][1]});
        st8(rs, ob + 2048, o[4]);
      } else {
        st8(rs, ob + 8, o[0]);
        st16(rs, ob + 1024, u32x4{o[1][0], o[1][1], o[2][0], o[2][1]});
        st16(rs, ob + 2048, u32x4{o[3][0], o[3][1], o[4][0], o[4][1]});
      }
    }
  }
  return tag;
}

#define GLD(dst, ptr, off) asm volatile("global_load_dwordx4 %0, %1, off offset:" #off " sc1" : "=v"(dst) : "v"(ptr) : "memory")
#define WAITV(n) do { asm volatile("s_waitcnt vmcnt(%0)" :: "i"(n) : "memory"); __builtin_amdgcn_sched_barrier(0); } while (0)

#define ISSUE_A(A, s) do { GLD(A[s][0], pa, 0); GLD(A[s][1], pa1, 0); GLD(A[s][2], pa2, 0); GLD(A[s][3], pa3, 0); \
    pa += 1024; pa1 += 1024; pa2 += 1024; pa3 += 1024; } while (0)
#define ISSUE_B(B, s) do { GLD(B[s][0], pb, -2048); GLD(B[s][1], pb, -1024); GLD(B[s][2], pb, 0); GLD(B[s][3], pb, 1024); GLD(B[s][4], pb, 2048); \
    pb += 5120; } while (0)
DI void stream_o(const char* pa, const char* pb, const char* pw, char* smem, bool poller, int lane, int rot, f32x4 (&acc)[5][4]) {
  u32x4 A[5][4], B[4][5];
#pragma unroll
  for (int s = 0; s < 4; ++s) ISSUE_B(B, s);
  if (poller) { poll_word(pw); set_cnt(smem, 7, lane); }
  else wait_cnt(smem, 7, 1);
#pragma unroll 1
  for (int spin = 0; spin < (SPIN_MAX >> 2); ++spin) {
    const char* q0 = pa + 2048 + rot * OST; const char* q1 = pa + 2048 + ((rot + 1) & 3) * OST;
    const char* q2 = pa + 2048 + ((rot + 2) & 3) * OST; const char* q3 = pa + 2048 + ((rot + 3) & 3) * OST;
#define LD5(t, q) GLD(A[0][t], q, -2048); GLD(A[1][t], q, -1024); GLD(A[2][t], q, 0); GLD(A[3][t], q, 1024); GLD(A[4][t], q, 2048);
    LD5(0, q0) LD5(1, q1) LD5(2, q2) LD5(3, q3)
    WAITV(0);
    unsigned mx = 0;
#pragma unroll
    for (int s = 0; s < 5; ++s)
#pragma unroll
      for (int t = 0; t < 4; ++t) {
        asm volatile("" : "+v"(A[s][t]));
        mx = max(max(mx, A[s][t][0]), max(A[s][t][1], max(A[s][t][2], A[s][t][3])));
      }
    if (__all(mx != ~0u)) break;
    __builtin_amdgcn_s_sleep(4);
  }
#define OSTEP(sa, sb) \
  _Pragma("unroll") for (int i = 0; i < 5; ++i) asm volatile("" : "+v"(B[sb][i])); \
  _Pragma("unroll") for (int n = 0; n < 5; ++n) _Pragma("unroll") for (int t = 0; t < 4; ++t) \
    acc[n][t] = MFMA16(__builtin_bit_cast(bf16x8, B[sb][n]), __builtin_bit_cast(bf16x8, A[sa][t]), acc[n][t]); \
  __builtin_amdgcn_sched_barrier(0);
  OSTEP(0, 0)
  ISSUE_B(B, 0);
  OSTEP(1, 1) OSTEP(2, 2) OSTEP(3, 3)
  WAITV(0);
  OSTEP(4, 0)
}

template <int NST, int NA, int NB, int S>
DI void lds_steps(const char*& pa, const char*& pa1, const char*& pa2, const char*& pa3, const char*& pb, const char* ring, unsigned ringa,
                  u32x4 (&A)[NA][4], f32x4 (&acc)[5][4]) {
  if constexpr (S < NST) {
    constexpr int ja = S - NA, jb = S - NB;
    auto cnt = [](int j0, bool skipA) constexpr {
      int n = 0;
      for (int j = j0; j < S; ++j) {
        if (j + NA < NST && !(skipA && j == j0)) n += 4;
        if (j + NB < NST) n += 5;
      }
      return n;
    };
    constexpr int ya = ja < 0 ? 4 * ((NA < NST ? NA : NST) - 1 - S) + cnt(0, false) : cnt(ja, true);
    constexpr int yb = jb < 0 ? 1000 : cnt(jb + 1, false);
    constexpr int y = ya < yb ? ya : yb;
    WAITV(y);
    bf16x8 bf[5];
#pragma unroll
    for (int n = 0; n < 5; ++n) bf[n] = *(const bf16x8*)(ring + (S % NB) * 5120 + n * 1024);
#pragma unroll
    for (int i = 0; i < 4; ++i) asm volatile("" : "+v"(A[S % NA][i]));
#define LSTEP(t, ptr) \
    _Pragma("unroll") for (int n = 0; n < 5; ++n) acc[n][t] = MFMA16(bf[n], __builtin_bit_cast(bf16x8, A[S % NA][t]), acc[n][t]); \
    __builtin_amdgcn_sched_barrier(0); \
    if constexpr (S + NA < NST) { GLD(A[S % NA][t], ptr, 0); ptr += 1024; } \
    __builtin_amdgcn_sched_barrier(0);
    LSTEP(0, pa) LSTEP(1, pa1) LSTEP(2, pa2) LSTEP(3, pa3)
    if constexpr (S + NB < NST) { glds5_sc1(pb, ringa + (S % NB) * 5120); pb += 5120; }
    lds_steps<NST, NA, NB, S + 1>(pa, pa1, pa2, pa3, pb, ring, ringa, A, acc);
  }
}
template <int NST, int NA, int NB>
DI void stream_lds(const char* pa, unsigned astr, const char* pb, char* smem, int roff, int cnt, bool poller, const char* flags, int nflags,
                   u64 tag, int lane, int rot, f32x4 (&acc)[5][4]) {
  const char* pa1 = pa + ((rot + 1) & 3) * astr; const char* pa2 = pa + ((rot + 2) & 3) * astr; const char* pa3 = pa + ((rot + 3) & 3) * astr;
  pa += rot * astr;
  const unsigned ringa = lds_addr(smem) + roff;
  u32x4 A[NA][4];
  pb += NB * 5120;
  if (poller) { polln(flags, nflags, tag, lane); set_cnt(smem, cnt, lane); }
  else wait_cnt(smem, cnt, 1);
#pragma unroll
  for (int s = 0; s < NA; ++s) ISSUE_A(A, s);
  lds_steps<NST, NA, NB, 0>(pa, pa1, pa2, pa3, pb, smem + roff + lane * 16, ringa, A, acc);
}

template <int C>
DI void finish(const P1& p, char* smem, int rt, int ct, int w, int lane, const f32x4 (&acc)[5][4]) {
  constexpr int N0 = C == 0 ? 0 : 3, NN = C == 0 ? 3 : 2;
  const int lg = lane >> 4, l15 = lane & 15;
  const unsigned o0 = (unsigned)((rt * 64 + (w & 3) * 16 + l15) * 640 + ct * 80 + N0 * 16 + 4 * lg);
  u32x2 xv[NN];
#pragma unroll
  for (int m = 0; m < NN; ++m) xv[m] = *(const u32x2*)(p.x + o0 + 16 * m);
  char* dst = smem + slot_of(w) + lane * 16;
#pragma unroll
  for (int i = 1; i < 4; ++i)
#pragma unroll
    for (int n = 0; n < 5; ++n) *(f32x4*)(dst + ((i - 1) * 5 + n) * 1024) = acc[n][i];
#pragma unroll
  for (int m = 0; m < 5 - NN; ++m) *(f32x4*)(dst + (15 + m) * 1024) = acc[(C == 0 ? 3 : 0) + m][0];
  __syncthreads();
  const char* same[3]; const char* other[3];
#pragma unroll
  for (int j = 1; j < 4; ++j) {
    const int v = (w - j) & 3;
    same[j - 1] = smem + slot_of(4 * C + v) + ((j - 1) * 5 + N0) * 1024 + lane * 16;
    other[j - 1] = smem + slot_of(4 * (1 - C) + v) + ((j - 1) * 5 + N0) * 1024 + lane * 16;
  }
  const char* partner = smem + slot_of(w ^ 4) + 15 * 1024 + lane * 16;
#pragma unroll
  for (int m = 0; m < NN; ++m) {
    f32x4 mine = (acc[N0 + m][0] + *(const f32x4*)(same[0] + m * 1024)) + (*(const f32x4*)(same[1] + m * 1024) + *(const f32x4*)(same[2] + m * 1024));
    f32x4 theirs = (*(const f32x4*)(partner + m * 1024) + *(const f32x4*)(other[0] + m * 1024)) +
                   (*(const f32x4*)(other[1] + m * 1024) + *(const f32x4*)(other[2] + m * 1024));
    const f32x4 ssum = C == 0 ? mine : theirs, bsum = C == 0 ? theirs : mine;
    float xs[4] = {bf_lo(xv[m][0]), bf_hi(xv[m][0]), bf_lo(xv[m][1]), bf_hi(xv[m][1])};
    float r[4];
#pragma unroll
    for (int j = 0; j < 4; ++j) {
      float bb = bsum[j];
      float g = bb * __builtin_amdgcn_rcpf(1.0f + __builtin_amdgcn_exp2f(-bb * LOG2E));
      r[j] = fmaf(ssum[j], g, xs[j]);
    }
    *(u32x2*)(p.out + o0 + 16 * m) = u32x2{pack2(r[0], r[1]), pack2(r[2], r[3])};
  }
}

__global__ __launch_bounds__(512, 2) void k_fused(P1 p) {
  extern __shared__ __attribute__((aligned(16))) char smem[];
  const int b = blockIdx.x, xcd = b & 7, idx = b >> 3;
  const int w = __builtin_amdgcn_readfirstlane(threadIdx.x >> 6), lane = threadIdx.x & 63;
  const int rb = (xcd >> 2) * 8 + (idx >> 2);
  const int jl = idx & 3, xc = xcd & 3;
  const int type = jl >> 1, g8 = 2 * xc + (jl & 1);
  const int c = w >> 2;
  const rsrc_t rs = __builtin_amdgcn_make_buffer_rsrc(p.ws, 0, WS_USED, 0x00020000);
  if (w == 7) {
    u64 pc;
    asm volatile("s_getpc_b64 %0" : "=s"(pc));
    touch((const char*)(pc & ~127ull) + (idx * 18 + (lane < 18 ? lane : 0)) * 128, lds_addr(smem) + L_DUMMY);
  }
  u64 tag;
  bool filled = false;
  if (type == 0) {
    if (c == 0) tag = body_a<0>(p, rs, smem, rb, g8, w, lane, filled);
    else tag = body_a<1>(p, rs, smem, rb, g8, w, lane, filled);
  } else {
    if (c == 0) tag = body_b<0>(p, rs, smem, rb, g8, w, lane, filled);
    else tag = body_b<1>(p, rs, smem, rb, g8, w, lane, filled);
  }
  if (w == 5) {
    unsigned v = __hip_atomic_load((LDS3 unsigned*)(smem + L_CNT + 12), __ATOMIC_RELAXED, __HIP_MEMORY_SCOPE_WORKGROUP);
    if (__builtin_amdgcn_readfirstlane(v) == 0) {
      poll256(p.ws + F_EARLY, tag, lane);
      if (blockIdx.x == 0 && lane == 0) st_flag(p.ws + OFF_FLG, tag);
      set_cnt(smem, 3, lane);
    }
  } else wait_cnt(smem, 3, 1);
  const int rt = DMAP == 0 ? (xcd >> 1) * 8 + (idx >> 2) : xcd * 4 + (idx >> 3);
  const int ct = DMAP == 0 ? (xcd & 1) * 4 + (idx & 3) : (idx & 7);
  f32x4 acc[5][4];
#pragma unroll
  for (int n = 0; n < 5; ++n)
#pragma unroll
    for (int t = 0; t < 4; ++t) acc[n][t] = f32x4{0, 0, 0, 0};
  if (w >= 4) {
    const int u0 = (w - 4) * 14 - (w == 7 ? 1 : 0);
    const char* pa = p.ws + OFF_ACT + (rt * 4) * AST + u0 * 1024 + lane * 16;
    const char* pb = p.ws + OFF_W2F + (ct * 74 + u0) * 5120 + lane * 16;
    const char* fact = p.ws + F_ACT + (rt >> 1) * 128;
    bool poller = w == 4;
    if (!filled) {
      if (poller) { polln(fact, 8, tag, lane); set_cnt(smem, 5, lane); poller = false; }
      ring_fill(p, smem, w, lane, 0, 7);
    }
    if (w < 6) stream_lds<14, NA_ACT, 7>(pa, AST, pb, smem, slot_of(w), 5, poller, fact, 8, tag, lane, w & 3, acc);
    else stream_lds<13, NA_ACT, 7>(pa, AST, pb, smem, slot_of(w), 5, false, fact, 8, tag, lane, w & 3, acc);
    (void)arrive(smem, 8, 4, lane);
  } else {
    stream_o(p.ws + OFF_O + (rt * 4) * OST + 5 * w * 1024 + lane * 16,
             p.ws + OFF_W2F + (ct * 74 + 54 + 5 * w) * 5120 + lane * 16 + 2048,
             p.ws + OFF_O + (rt * 4 + 1 + 2 * ((lane >> 3) & 1)) * OST + (((5 * (lane & 7)) >> 1) + 2) * 1024 + 1008 - ((lane & 1) ? 0 : 1024) , smem, w == 0, lane, w & 3, acc);
    wait_cnt(smem, 8, 4);
  }
  if (w >= 4) finish<1>(p, smem, rt, ct, w, lane, acc);
  else finish<0>(p, smem, rt, ct, w, lane, acc);
}
}

extern "C" void kernel_launch(void* const* d_in, const int* in_sizes, int n_in, void* d_out, int out_size, void* d_ws,
                              size_t ws_size, hipStream_t stream) {
  using namespace hk;
  P1 p;
  p.x = (const u16*)d_in[0]; p.wqkv = (const u16*)d_in[1]; p.wphi = (const u16*)d_in[2]; p.wo = (const u16*)d_in[3];
  p.w1 = (const u16*)d_in[5]; p.b1 = (const u16*)d_in[6]; p.w2 = (const u16*)d_in[7]; p.ws = (char*)d_ws; p.out = (u16*)d_out;
  (void)hipFuncSetAttribute((const void*)k_fused, hipFuncAttributeMaxDynamicSharedMemorySize, L_END);
  k_fused<<<256, 512, L_END, stream>>>(p);
}
```
